```python
import torch

def _softmax(x, axis=-1):
    return torch.softmax(x, dim=axis)
def _gelu(x):
    return torch.nn.functional.gelu(x, approximate="tanh")
def _silu(x):
    return torch.nn.functional.silu(x)
def _sigmoid(x):
    return torch.sigmoid(x)
def _layernorm(x, axis=-1, eps=1e-5):
    m = x.mean(dim=axis, keepdim=True)
    v = x.var(dim=axis, keepdim=True, unbiased=False)
    return (x - m) / torch.sqrt(v + eps)
def _scatter_add(tgt, idx, src, axis=0):
    return tgt.clone().index_add_(axis, idx, src)
def _segment_sum(src, seg, n):
    out = torch.zeros((n,) + src.shape[1:], dtype=src.dtype)
    return out.index_add_(0, seg, src)
def _pad(x, pw):
    flat = tuple(v for pair in reversed(pw) for v in pair)
    return torch.nn.functional.pad(x, flat)

kernel_name = 'interleave_layers__linear_attn_block__mha_block__prompt'

def setup_inputs():

    _w = (lambda a: (lambda t: t.to(torch.bfloat16) if t.is_floating_point() else t)(torch.as_tensor(a)))
    def _A_setup():
        B, S, D = 1, 2048, 576
        return {
            "x": _w((lambda *s: torch.randn(*s, dtype=torch.bfloat16) * 0.05)(B, S, D)),
            "wqkv": _w((lambda *s: torch.randn(*s, dtype=torch.bfloat16) * 0.05)(D, 3 * D)),
            "wo": _w((lambda *s: torch.randn(*s, dtype=torch.bfloat16) * 0.05)(D, D)),
        }
    _A_in = _A_setup()

    def _B_setup():
        B, S, D, H = 1, 2048, 576, 32
        return {
            "x": _w((lambda *s: torch.randn(*s, dtype=torch.bfloat16) * 0.05)(B, S, D)),
            "wqkv": _w((lambda *s: torch.randn(*s, dtype=torch.bfloat16) * 0.1)(D, 3 * D)),
            "wo": _w((lambda *s: torch.randn(*s, dtype=torch.bfloat16) * 0.05)(D, D)),
        }
    _B_in = _B_setup()

    return {
        "A_x": _A_in["x"],
        "A_wqkv": _A_in["wqkv"],
        "A_wo": _A_in["wo"],
        "B_x": _B_in["x"],
        "B_wqkv": _B_in["wqkv"],
        "B_wo": _B_in["wo"],
    }

def reference(A_x, A_wqkv, A_wo, B_x, B_wqkv, B_wo):
    def _A_ref(x, wqkv, wo):
        B, S, D = x.shape
        H = 32; Dh = D // H
        qkv = (lambda x, *d: x.permute(*d))((x @ wqkv).reshape(B, S, 3, H, Dh), 2, 0, 3, 1, 4)
        q, k, v = qkv[0], qkv[1], qkv[2]
        phi = lambda t: torch.where(t > 0, t + 1, torch.exp(t)) + 1e-6
        pq, pk = phi(q), phi(k)
        kv = torch.einsum("bhsd,bhse->bhde", pk, v)
        z = torch.einsum("bhsd->bhd", pk)
        num = torch.einsum("bhqd,bhde->bhqe", pq, kv)
        den = torch.einsum("bhqd,bhd->bhq", pq, z)[..., None] + 1e-6
        o = num / den
        return (lambda x, *d: x.permute(*d))(o, 0, 2, 1, 3).reshape(B, S, D) @ wo

    def _B_ref(x, wqkv, wo):
        B, S, D = x.shape
        H = 32
        Dh = D // H
        qkv = (lambda x, *d: x.permute(*d))((x @ wqkv).reshape(B, S, 3, H, Dh), 2, 0, 3, 1, 4)
        q, k, v = qkv[0], qkv[1], qkv[2]
        scores = torch.einsum("bhqd,bhkd->bhqk", q, k) * (Dh ** -0.5)
        p = _softmax(scores, axis=-1)
        o = torch.einsum("bhqk,bhkd->bhqd", p, v)
        return (lambda x, *d: x.permute(*d))(o, 0, 2, 1, 3).reshape(B, S, D) @ wo

    x = A_x
    x = x + _A_ref(_layernorm(x, axis=-1), A_wqkv, A_wo)
    x = x + _B_ref(_layernorm(x, axis=-1), B_wqkv, B_wo)
    x = x + _A_ref(_layernorm(x, axis=-1), A_wqkv, A_wo)
    x = x + _B_ref(_layernorm(x, axis=-1), B_wqkv, B_wo)
    return x
```

```cpp
#include <hip/hip_runtime.h>
#include <hip/hip_bf16.h>
typedef __hip_bfloat16 bf16;
constexpr int S=2048, D=576, H=32, DH=18;
__device__ float b2f(bf16 x){ return __bfloat162float(x);}
__global__ void k_cvt(const bf16* x, float* y, int n){ int i=blockIdx.x*256+threadIdx.x; if(i<n) y[i]=b2f(x[i]); }
__global__ void k_ln(const float* x, float* y){
  int row=blockIdx.x; int l=threadIdx.x; float s=0; for(int i=l;i<D;i+=64) s+=x[row*D+i];
  for(int o=32;o;o>>=1) s+=__shfl_xor(s,o); float m=s/D; float v=0; for(int i=l;i<D;i+=64){float d=x[row*D+i]-m; v+=d*d;}
  for(int o=32;o;o>>=1) v+=__shfl_xor(v,o); float r=rsqrtf(v/D+1e-5f);
  for(int i=l;i<D;i+=64) y[row*D+i]=(x[row*D+i]-m)*r; }
__global__ void k_gemm(const float* A, const bf16* W, float* C, int N, const float* res){
  int n=blockIdx.x*64+threadIdx.x; int m=blockIdx.y; if(n>=N) return; float acc=0; for(int k=0;k<D;k++) acc+=A[m*D+k]*b2f(W[k*N+n]);
  if(res) acc = b2f(__float2bfloat16(acc + res[m*N+n])); C[m*N+n]=acc; }
__device__ float phi(float t){ return (t>0? t+1.f : __expf(t)) + 1e-6f; }
__global__ void k_kv(const float* qkv, float* kv){
  int h=blockIdx.x; int t=threadIdx.x; if(t>=342) return; int d=t/19, e=t%19; float acc=0;
  for(int s=0;s<S;s++){ float pk=phi(qkv[s*3*D + D + h*DH + d]); float v = e<18? qkv[s*3*D+2*D+h*DH+e] : 1.f; acc+=pk*v; }
  kv[h*342+t]=acc; }
__global__ void k_lin_o(const float* qkv, const float* kv, float* o){ int s=blockIdx.x; int c=threadIdx.x; if(c>=D) return; int h=c/DH, e=c%DH;
  float num=0, den=0; for(int d=0;d<DH;d++){ float pq=phi(qkv[s*3*D+h*DH+d]); num+=pq*kv[h*342+d*19+e]; den+=pq*kv[h*342+d*19+18]; }
  o[s*D+c]=num/(den+1e-6f); }
__global__ void k_attn(const float* qkv, float* o){
  int q=blockIdx.x*64+threadIdx.x; int h=blockIdx.y; float qv[DH], acc[DH];
  #pragma unroll
  for(int d=0;d<DH;d++){ qv[d]=qkv[q*3*D+h*DH+d]*0.23570226f; acc[d]=0; }
  float m=-1e30f, l=0;
  for(int k=0;k<S;k++){ float s=0;
    #pragma unroll
    for(int d=0;d<DH;d++) s+=qv[d]*qkv[k*3*D+D+h*DH+d];
    float mn=fmaxf(m,s); float a=__expf(m-mn), p=__expf(s-mn); l=l*a+p;
    #pragma unroll
    for(int d=0;d<DH;d++) acc[d]=acc[d]*a+p*qkv[k*3*D+2*D+h*DH+d]; m=mn; }
  #pragma unroll
  for(int d=0;d<DH;d++) o[q*D+h*DH+d]=acc[d]/l; }
__global__ void k_out(const float* x, bf16* y, int n){ int i=blockIdx.x*256+threadIdx.x; if(i<n) y[i]=__float2bfloat16(x[i]); }
extern "C" void kernel_launch(void* const* d_in, const int* in_sizes, int n_in, void* d_out, int out_size, void* d_ws, size_t ws_size, hipStream_t stream) {
  const bf16* x0=(const bf16*)d_in[0]; const bf16* wA=(const bf16*)d_in[1]; const bf16* woA=(const bf16*)d_in[2];
  const bf16* wB=(const bf16*)d_in[4]; const bf16* woB=(const bf16*)d_in[5];
  float* ws=(float*)d_ws; float* x=ws; float* x2=x+S*D; float* xn=x2+S*D; float* qkv=xn+S*D; float* o=qkv+S*3*D; float* kv=o+S*D;
  k_cvt<<<S*D/256,256,0,stream>>>(x0,x,S*D);
  for(int L=0;L<4;L++){
    k_ln<<<S,64,0,stream>>>(x,xn);
    k_gemm<<<dim3(27,S),64,0,stream>>>(xn,(L&1)?wB:wA,qkv,3*D,nullptr);
    if(L&1){ k_attn<<<dim3(S/64,H),64,0,stream>>>(qkv,o);} else { k_kv<<<H,384,0,stream>>>(qkv,kv); k_lin_o<<<S,576,0,stream>>>(qkv,kv,o);}
    k_gemm<<<dim3(9,S),64,0,stream>>>(o,(L&1)?woB:woA,x2,D,x);
    float* t=x; x=x2; x2=t;
  }
  k_out<<<S*D/256,256,0,stream>>>(x,(bf16*)d_out,S*D);
}
```

```cpp
#include <hip/hip_runtime.h>
#include <hip/hip_bf16.h>
#include <stdint.h>

typedef __attribute__((ext_vector_type(8))) short bf16x8;
typedef __attribute__((ext_vector_type(4))) short bf16x4;
typedef __attribute__((ext_vector_type(4))) float f32x4;
typedef __attribute__((ext_vector_type(2))) float f32x2;
typedef __attribute__((ext_vector_type(16))) float f32x16;
typedef __attribute__((ext_vector_type(4))) unsigned u32x4;
typedef __attribute__((ext_vector_type(2))) unsigned u32x2;
typedef __attribute__((ext_vector_type(2))) __bf16 bfx2;
#define LDSAS __attribute__((address_space(3)))
typedef LDSAS char* lds_ptr;
typedef const LDSAS char* lds_cptr;
typedef LDSAS f32x4* lds_f4;
typedef LDSAS f32x2* lds_f2;
typedef LDSAS float* lds_f1;
typedef LDSAS u32x4* lds_u4;
typedef const volatile LDSAS unsigned* lds_vu32;
typedef const LDSAS bf16x8* lds_f8;

constexpr int SEQ = 2048, DM = 576, NH = 32;

constexpr size_t WT_TILE = 126 * 1024;
constexpr size_t WO_TILE = 90 * 1024;
constexpr size_t IMG_CHUNK = 19 * 1024;
constexpr size_t IMG_HEAD = 8 * IMG_CHUNK;
constexpr int KV_HEAD = 19 * 20;
constexpr size_t XF_BYTES = (size_t)SEQ * DM * 2, ST_BYTES = (size_t)SEQ * 128;
constexpr size_t OFF_CNT = 0;
constexpr size_t CNT_BYTES = 2 * 96 * 256 + 256;
constexpr size_t OFF_KV  = 49664;
constexpr size_t ZERO_BYTES = OFF_KV + 2 * NH * KV_HEAD * 4;
constexpr size_t OFF_WTA = 147456;
constexpr size_t OFF_WTB = OFF_WTA + 16 * WT_TILE + 4096;
constexpr size_t OFF_CSA = OFF_WTB + 16 * WT_TILE + 4096;
constexpr size_t OFF_CSB = OFF_CSA + 8192;
constexpr size_t OFF_WOA = OFF_CSB + 8192;
constexpr size_t OFF_WOB = OFF_WOA + 8 * WO_TILE + 8192;
constexpr size_t OFF_XF  = OFF_WOB + 8 * WO_TILE + 8192;
constexpr size_t OFF_ST  = OFF_XF + 4 * XF_BYTES;
constexpr size_t OFF_LAY = OFF_ST + 4 * ST_BYTES;
constexpr size_t O_BYTES = (size_t)128 * 16 * 1024 + (size_t)128 * 32 * 64;
constexpr size_t O_TAIL = (size_t)128 * 16 * 1024;
constexpr size_t LO_O   = 0;
constexpr size_t LO_Q1  = LO_O + 2 * O_BYTES;
constexpr size_t LO_Q2  = LO_Q1 + (size_t)NH * SEQ * 32;
constexpr size_t LO_IMG = LO_Q2 + (size_t)NH * SEQ * 4;
constexpr size_t LAY_BYTES = LO_IMG + (size_t)NH * IMG_HEAD;
static_assert(ZERO_BYTES <= OFF_WTA && CNT_BYTES <= OFF_KV && ZERO_BYTES % 16 == 0, "layout");
constexpr int C_QA = 0, C_OA = 16, C_WA = 32, C_QB = 48, C_AT = 64, C_WB = 80, C_LAY = 96;

constexpr float QSCALE = 0.23570226039551584f * 1.4426950408889634f;

__device__ __forceinline__ unsigned pack2(float lo, float hi) {
    f32x2 v = {lo, hi};
    bfx2 r = __builtin_convertvector(v, bfx2);
    return __builtin_bit_cast(unsigned, r);
}
__device__ __forceinline__ float bf_lo(unsigned u) { return __uint_as_float(u << 16); }
__device__ __forceinline__ float bf_hi(unsigned u) { return __uint_as_float(u & 0xffff0000u); }
__device__ __forceinline__ float phi_f(float t) { return (t > 0.f ? t + 1.f : __expf(t)) + 1e-6f; }
__device__ __forceinline__ unsigned lds_off(const void* p) { return (unsigned)(size_t)(lds_cptr)p; }

#define MFMA16(a, b, c) __builtin_amdgcn_mfma_f32_16x16x32_bf16(a, b, c, 0, 0, 0)
#define MFMA32(a, b, c) __builtin_amdgcn_mfma_f32_32x32x16_bf16(a, b, c, 0, 0, 0)
#define MFMA16K16(a, b, c) __builtin_amdgcn_mfma_f32_16x16x16bf16_1k(a, b, c, 0, 0, 0)
#define PIN(x) asm volatile("" : "+v"(x))
#define LAUNDER() int bid = blockIdx.x; asm volatile("" : "+v"(lane)); asm volatile("" : "+s"(w), "+s"(ws), "+s"(bid), "+s"(L))
#define SBAR() __builtin_amdgcn_sched_barrier(0)
#define EXP2(x) __builtin_amdgcn_exp2f(x)

template <int N>
__device__ __forceinline__ void glds(const void* g, unsigned lds) {
    unsigned keep;
    if constexpr (N == 4)
        asm volatile("s_mov_b32 %0, m0\n\ts_mov_b32 m0, %2\n\ts_nop 0\n\t"
                     "global_load_lds_dwordx4 %1, off\n\tglobal_load_lds_dwordx4 %1, off offset:1024\n\t"
                     "global_load_lds_dwordx4 %1, off offset:2048\n\tglobal_load_lds_dwordx4 %1, off offset:3072\n\t"
                     "s_mov_b32 m0, %0" : "=&s"(keep) : "v"(g), "s"(lds) : "memory");
    else
        asm volatile("s_mov_b32 %0, m0\n\ts_mov_b32 m0, %2\n\ts_nop 0\n\t"
                     "global_load_lds_dwordx4 %1, off\n\tglobal_load_lds_dwordx4 %1, off offset:1024\n\t"
                     "s_mov_b32 m0, %0" : "=&s"(keep) : "v"(g), "s"(lds) : "memory");
}
__device__ __forceinline__ void glds1_sc1(const void* g, unsigned lds) {
    unsigned keep;
    asm volatile("s_mov_b32 %0, m0\n\ts_mov_b32 m0, %2\n\ts_nop 0\n\tglobal_load_lds_dwordx4 %1, off sc1\n\ts_mov_b32 m0, %0"
                 : "=&s"(keep) : "v"(g), "s"(lds) : "memory");
}
template <int OFF, class T> __device__ __forceinline__ void gld16(T& d, const void* p) {
    asm volatile("global_load_dwordx4 %0, %1, off offset:%2" : "=v"(d) : "v"(p), "i"(OFF) : "memory"); }
template <int OFF, class T> __device__ __forceinline__ void gld4(T& d, const void* p) {
    asm volatile("global_load_dword %0, %1, off offset:%2" : "=v"(d) : "v"(p), "i"(OFF) : "memory"); }
template <int OFF, class T> __device__ __forceinline__ void gld16c(T& d, const void* p) {
    asm volatile("global_load_dwordx4 %0, %1, off offset:%2 sc1" : "=v"(d) : "v"(p), "i"(OFF) : "memory"); }
template <int OFF, class T> __device__ __forceinline__ void gld8c(T& d, const void* p) {
    asm volatile("global_load_dwordx2 %0, %1, off offset:%2 sc1" : "=v"(d) : "v"(p), "i"(OFF) : "memory"); }
template <int OFF, class T> __device__ __forceinline__ void gld4c(T& d, const void* p) {
    asm volatile("global_load_dword %0, %1, off offset:%2 sc1" : "=v"(d) : "v"(p), "i"(OFF) : "memory"); }
__device__ __forceinline__ void gst16c(void* p, u32x4 v) { asm volatile("global_store_dwordx4 %0, %1, off sc1\n\ts_nop 1" :: "v"(p), "v"(v) : "memory"); }
__device__ __forceinline__ void gst8c(void* p, f32x2 v) { asm volatile("global_store_dwordx2 %0, %1, off sc1" :: "v"(p), "v"(v) : "memory"); }

#define STAMP_DECL
#define ST()
#define STAMP_OUT(kid)

typedef __attribute__((address_space(1))) unsigned gu32;
#define RLX_AGENT __ATOMIC_RELAXED, __HIP_MEMORY_SCOPE_AGENT
template <int N> __device__ __forceinline__ void signal_cnt(char* ws, int line) {
    asm volatile("s_waitcnt vmcnt(%0) lgkmcnt(0)\n\ts_barrier" :: "i"(N) : "memory");
    if (threadIdx.x == 0) __hip_atomic_fetch_add((gu32*)(ws + OFF_CNT + (size_t)line * 256), 1u, RLX_AGENT);
}
__device__ __forceinline__ void wait_cnt(char* ws, int line, unsigned want) {
    if (threadIdx.x < 64) {
        gu32* p = (gu32*)(ws + OFF_CNT + (size_t)line * 256);
        gu32* tmo = (gu32*)(ws + OFF_CNT + 2 * C_LAY * 256);
        for (unsigned spins = 0;;) {
            if ((unsigned)__builtin_amdgcn_readfirstlane(__hip_atomic_load(p, RLX_AGENT)) >= want) break;
            __builtin_amdgcn_s_sleep(1);
            if (++spins > (1u << 18)) { __hip_atomic_store(tmo, 1u, RLX_AGENT); break; }
            if ((spins & 255u) == 0u && __builtin_amdgcn_readfirstlane(__hip_atomic_load(tmo, RLX_AGENT))) break;
        }
    }
    asm volatile("s_waitcnt vmcnt(0)\n\ts_barrier" ::: "memory");
}

constexpr int PB_W = 504, PB_CS = 108, PB_WO = 180, PB_X = 64, PB_Z = 36, PB_ONE = 64;
constexpr int PREP_BLOCKS = PB_W + PB_CS + PB_WO + PB_X + PB_Z + PB_ONE;
static_assert(PB_Z * 256 * 16 >= ZERO_BYTES, "zero blocks");

__device__ __forceinline__ void qkv_col_map(int n, int& ct, int& cb, int& c) {
    const int part = n / 576, rem = n - part * 576, head = rem / 18, d = rem - head * 18;
    ct = head >> 1; const int hb = head & 1;
    if (d < 16) { cb = part * 2 + hb; c = d; } else { cb = 6; c = part * 4 + hb * 2 + (d - 16); }
}

__global__ __launch_bounds__(256) void prep_kernel(const unsigned short* __restrict__ x0, const unsigned short* __restrict__ wA,
                                                   const unsigned short* __restrict__ woA, const unsigned short* __restrict__ wB,
                                                   const unsigned short* __restrict__ woB, char* __restrict__ ws) {
    __shared__ float red[16][32];
    int b = blockIdx.x; const int t = threadIdx.x;
    if (b < PB_X) {
        const int idx = b * 256 + t, m = idx & 15, j = (idx >> 4) & 7, rb = idx >> 7, row = rb * 16 + m;
        const u32x4* src = (const u32x4*)(x0 + (size_t)row * 576 + 72 * j);
        u32x4 v[9];
#pragma unroll
        for (int i = 0; i < 9; ++i) v[i] = src[i];
        float sa = 0.f, sb = 0.f;
#pragma unroll
        for (int i = 0; i < 9; ++i) {
            const int q = 9 * j + i;
            *(u32x4*)(ws + OFF_XF + ((size_t)(rb * 18 + (q >> 2)) * 64 + (q & 3) * 16 + m) * 16) = v[i];
            float a = 0.f;
#pragma unroll
            for (int k = 0; k < 4; ++k) a += bf_lo(v[i][k]) + bf_hi(v[i][k]);
            if (i < 4 || i == 8) sa += a; else sb += a;
        }
        const float ma = sa * (1.f / 40.f), mb = sb * (1.f / 32.f);
        float qa = 0.f, qb = 0.f;
#pragma unroll
        for (int i = 0; i < 9; ++i) {
            const bool pa = i < 4 || i == 8; const float mu = pa ? ma : mb; float a = 0.f;
#pragma unroll
            for (int k = 0; k < 4; ++k) { const float d0 = bf_lo(v[i][k]) - mu, d1 = bf_hi(v[i][k]) - mu; a += d0 * d0 + d1 * d1; }
            if (pa) qa += a; else qb += a;
        }
        *(f32x4*)(ws + OFF_ST + (size_t)row * 128 + j * 16) = (f32x4){sa, qa, sb, qb};
        return;
    }
    b -= PB_X;
    if (b < PB_CS) {
        const int mat = b / 54, bi = b - mat * 54, rg = t >> 4, cp = t & 15;
        const int n = 2 * (bi * 16 + cp);
        const unsigned short* w = (mat ? wB : wA) + (size_t)(rg * 36) * 1728 + n;
        float s0 = 0.f, s1 = 0.f;
        unsigned v[36];
#pragma unroll
        for (int j = 0; j < 36; ++j) v[j] = *(const unsigned*)(w + j * 1728);
#pragma unroll
        for (int j = 0; j < 36; ++j) { s0 += bf_lo(v[j]); s1 += bf_hi(v[j]); }
        red[rg][2 * cp] = s0; red[rg][2 * cp + 1] = s1;
        __syncthreads();
        float* cs = (float*)(ws + (mat ? OFF_CSB : OFF_CSA));
        if (t < 32) {
            float a = 0.f;
#pragma unroll
            for (int j = 0; j < 16; ++j) a += red[j][t];
            int ct, cb, c; qkv_col_map(bi * 32 + t, ct, cb, c);
            cs[ct * 112 + cb * 16 + c] = a;
        } else if (bi == 0 && t >= 64 && t < 128) { const int q = t - 64; cs[(q >> 2) * 112 + 108 + (q & 3)] = 0.f; }
        return;
    }
    b -= PB_CS;
    if (b < PB_W) {
        const int idx = b * 256 + t, mat = idx / 64512, r = idx - mat * 64512, sg = r / 896, pr = r - sg * 896;
        const int s = sg >> 2, g = sg & 3;
        char* wt = ws + (mat ? OFF_WTB : OFF_WTA);
        u32x4 c0 = {0, 0, 0, 0}, c1 = {0, 0, 0, 0};
        int ct, cb, c;
        if (pr < 864) {
            const int n = 2 * pr;
            const unsigned short* w = (mat ? wB : wA) + (size_t)(32 * s + 8 * g) * 1728 + n;
            unsigned v[8];
#pragma unroll
            for (int j = 0; j < 8; ++j) v[j] = *(const unsigned*)(w + j * 1728);
#pragma unroll
            for (int j = 0; j < 4; ++j) {
                c0[j] = (v[2 * j] & 0xffffu) | (v[2 * j + 1] << 16);
                c1[j] = (v[2 * j] >> 16) | (v[2 * j + 1] & 0xffff0000u);
            }
            qkv_col_map(n, ct, cb, c);
        } else { const int pi = pr - 864; ct = pi >> 1; cb = 6; c = 12 + 2 * (pi & 1); }
        u32x4* dst = (u32x4*)(wt + ((size_t)((ct * 18 + s) * 7 + cb) * 64 + g * 16 + c) * 16);
        dst[0] = c0; dst[1] = c1;
        return;
    }
    b -= PB_W;
    if (b < PB_WO) {
        const int idx = b * 256 + t, mat = idx / 23040, r = idx - mat * 23040, sg = r / 320, pr = r - sg * 320;
        const int s = sg >> 2, g = sg & 3, jt = pr / 40, lc = 2 * (pr - jt * 40), cb = lc >> 4, c = lc & 15;
        u32x4 c0 = {0, 0, 0, 0}, c1 = {0, 0, 0, 0};
        if (lc < 72) {
            const unsigned short* w = (mat ? woB : woA) + 72 * jt + lc;
            unsigned v[8];
#pragma unroll
            for (int j = 0; j < 8; ++j) {
                int head, e;
                if (s >= 16) { head = 16 * (s - 16) + 4 * g + (j >> 1); e = 16 + (j & 1); }
                else if (mat == 0) { head = 2 * s + (j >> 2); e = 4 * g + (j & 3); }
                else { head = 2 * s + (g >> 1); e = 4 * (g & 1) + (j & 3) + 8 * (j >> 2); }
                v[j] = *(const unsigned*)(w + (size_t)(head * 18 + e) * 576);
            }
#pragma unroll
            for (int j = 0; j < 4; ++j) {
                c0[j] = (v[2 * j] & 0xffffu) | (v[2 * j + 1] << 16);
                c1[j] = (v[2 * j] >> 16) | (v[2 * j + 1] & 0xffff0000u);
            }
        }
        const int kh = s < 16 ? (s >> 3) : (s - 16), i = s < 16 ? (s & 7) : 8;
        u32x4* dst = (u32x4*)(ws + (mat ? OFF_WOB : OFF_WOA) + (size_t)jt * WO_TILE + (size_t)((i * 2 + kh) * 5 + cb) * 1024 + (g * 16 + c) * 16);
        dst[0] = c0; dst[1] = c1;
        return;
    }
    b -= PB_WO;
    if (b < PB_Z) {
        const int idx = b * 256 + t;
        if ((size_t)idx * 16 < ZERO_BYTES) *(u32x4*)(ws + (size_t)idx * 16) = (u32x4){0u, 0u, 0u, 0u};
        return;
    }
    b -= PB_Z;
    {
        const int L = b >> 5, head = b & 31, ch = t >> 5, th = t & 31;
        char* p = ws + OFF_LAY + L * LAY_BYTES + LO_IMG + (size_t)head * IMG_HEAD + ch * IMG_CHUNK + 9216 + th * 320 + 18 * 16;
        *(u32x4*)p = (u32x4){0x3f803f80u, 0x3f803f80u, 0x3f803f80u, 0x3f803f80u};
        *(u32x4*)(p + 16) = (u32x4){0u, 0u, 0u, 0u};
    }
}

constexpr int LDS_W = 34 * 1024, LDS_WO = 70 * 1024;
struct Pf { const char* src; unsigned dst; };
__device__ __forceinline__ Pf pf_qkv(char* ws, char* smem, int layer, int bid, int w, int lane) {
    const int ct = 8 * (bid & 1) + ((bid >> 3) & 7);
    return Pf{ws + (layer ? OFF_WTB : OFF_WTA) + (size_t)ct * WT_TILE + w * 2048 + lane * 16, lds_off(smem) + LDS_W + w * 2048};
}
__device__ __forceinline__ Pf pf_wo(char* ws, char* smem, int mode, int bid, int w, int lane) {
    const int jt = 4 * (bid & 1) + ((bid >> 3) & 3);
    return Pf{ws + (mode ? OFF_WOB : OFF_WOA) + (size_t)jt * WO_TILE + w * 2048 + lane * 16, lds_off(smem) + LDS_WO + w * 2048};
}
#define PF(p, j) glds<2>((p).src + (j) * 16384, (p).dst + (j) * 16384)
#define PF_LAST(p, j, n) do { const int jj_ = w < (n) ? (j) : (j) - 1; PF(p, jj_); } while (0)
#define PFQ_LAST(p) PF_LAST(p, 7, 7)
#define PFO_LAST(p) PF_LAST(p, 5, 5)

template <int LAYER, bool FUSED>
__device__ __forceinline__ void qkv_phase(char* ws, char* smem, int L, int w, int lane) {
    LAUNDER();
    int m = lane & 15, g = lane >> 4; const int rp = w & 3, kh = w >> 2;
    const int xcd = bid & 7, idx = bid >> 3;
    const int panel = 4 * (xcd >> 1) + (idx >> 3), ct = 8 * (xcd & 1) + (idx & 7);
    const int rb = panel * 8 + 2 * rp + kh, rbo = panel * 8 + 2 * rp + 1 - kh;
    const int xv = 2 * L + LAYER;
    char* lay = ws + OFF_LAY + L * LAY_BYTES;
    STAMP_DECL; ST();
    f32x4 st[2], cs4[4]; float cs1[5];
    {
        const float* cs = (const float*)(ws + (LAYER ? OFF_CSB : OFF_CSA)) + ct * 112;
        const float* c4 = cs + 4 * g; const float* c1 = cs + m;
        if constexpr (LAYER == 0) {
            gld16<0>(cs4[0], c4); gld16<64>(cs4[1], c4);
            gld4<128>(cs1[0], c1); gld4<192>(cs1[1], c1); gld4<256>(cs1[2], c1); gld4<320>(cs1[3], c1); gld4<384>(cs1[4], c1);
        } else {
            gld16<0>(cs4[0], c4); gld16<64>(cs4[1], c4); gld16<128>(cs4[2], c4); gld16<192>(cs4[3], c4);
            gld4<256>(cs1[0], c1); gld4<320>(cs1[1], c1); gld4<384>(cs1[2], c1);
        }
    }
    if (!FUSED || xv == 0) {
        const Pf pf = pf_qkv(ws, smem, LAYER, bid, w, lane);
        PF(pf, 0); PF(pf, 1); PF(pf, 2); PF(pf, 3); PF(pf, 4); PF(pf, 5); PF(pf, 6); PFQ_LAST(pf);
    }
    wait_cnt(ws, LAYER ? L * C_LAY + C_WA + panel : C_WB + panel, xv == 0 ? 0u : 16u);
    ST();
    bf16x8 a[2][9];
    const char* xa0 = ws + OFF_XF + xv * XF_BYTES + (size_t)(rb * 18 + kh) * 1024 + lane * 16;
    const char* xa1 = ws + OFF_XF + xv * XF_BYTES + (size_t)(rbo * 18 + kh) * 1024 + lane * 16;
#define LDA(j) do { gld16c<((j) & 1) * 2048>(a[0][j], xa0 + ((j) >> 1) * 4096); gld16c<((j) & 1) * 2048>(a[1][j], xa1 + ((j) >> 1) * 4096); } while (0)
    {
        const char* sp = ws + OFF_ST + xv * ST_BYTES + (size_t)(rb * 16 + m) * 128 + g * 32;
        gld16c<0>(st[0], sp); gld16c<16>(st[1], sp);
    }
    LDA(0); LDA(1); LDA(2);
    const lds_cptr bl = (lds_cptr)smem + LDS_W + kh * 7 * 1024 + lane * 16;
    bf16x8 bb[2][7];
#pragma unroll
    for (int c = 0; c < 7; ++c) bb[0][c] = *(lds_f8)(bl + c * 1024);
    if constexpr (LAYER == 0)
        asm volatile("s_waitcnt vmcnt(6)" : "+v"(st[0]), "+v"(st[1]), "+v"(cs4[0]), "+v"(cs4[1]), "+v"(cs1[0]), "+v"(cs1[1]), "+v"(cs1[2]), "+v"(cs1[3]), "+v"(cs1[4]) :: "memory");
    else
        asm volatile("s_waitcnt vmcnt(6)" : "+v"(st[0]), "+v"(st[1]), "+v"(cs4[0]), "+v"(cs4[1]), "+v"(cs4[2]), "+v"(cs4[3]), "+v"(cs1[0]), "+v"(cs1[1]), "+v"(cs1[2]) :: "memory");
    ST();
    float nmu, rs, nmu4[4], rs4[4];
    {
        float s = st[0][0] + st[0][2] + st[1][0] + st[1][2];
        s += __shfl_xor(s, 16); s += __shfl_xor(s, 32);
        const float mean = s * (1.f / 576.f);
        const float d0 = st[0][0] * (1.f / 40.f) - mean, d1 = st[0][2] * (1.f / 32.f) - mean;
        const float d2 = st[1][0] * (1.f / 40.f) - mean, d3 = st[1][2] * (1.f / 32.f) - mean;
        float q = st[0][1] + st[0][3] + st[1][1] + st[1][3] + 40.f * (d0 * d0 + d2 * d2) + 32.f * (d1 * d1 + d3 * d3);
        q += __shfl_xor(q, 16); q += __shfl_xor(q, 32);
        nmu = -mean; rs = rsqrtf(q * (1.f / 576.f) + 1e-5f);
#pragma unroll
        for (int r = 0; r < 4; ++r) { nmu4[r] = __shfl(nmu, 4 * g + r); rs4[r] = __shfl(rs, 4 * g + r); }
    }
    constexpr int NTR = LAYER == 0 ? 2 : 4;
    f32x4 acc[2][7];
#pragma unroll
    for (int c = 0; c < 7; ++c) {
        if (c < NTR) acc[0][c] = cs4[c] * nmu;
        else { const float cv = cs1[c - NTR]; acc[0][c] = (f32x4){nmu4[0] * cv, nmu4[1] * cv, nmu4[2] * cv, nmu4[3] * cv}; }
        acc[1][c] = (f32x4){0.f, 0.f, 0.f, 0.f};
    }
#define MM(j) do { _Pragma("unroll") for (int c = 0; c < 7; ++c) { \
            if ((j) < 8) bb[((j) + 1) & 1][c] = *(lds_f8)(bl + (((j) + 1) * 14 + c) * 1024); \
            SBAR(); \
            if (c < NTR) { acc[1][c] = MFMA16(bb[(j) & 1][c], a[1][j], acc[1][c]); acc[0][c] = MFMA16(bb[(j) & 1][c], a[0][j], acc[0][c]); } \
            else         { acc[1][c] = MFMA16(a[1][j], bb[(j) & 1][c], acc[1][c]); acc[0][c] = MFMA16(a[0][j], bb[(j) & 1][c], acc[0][c]); } \
            SBAR(); \
        } } while (0)
#define WAITA(j, N) asm volatile("s_waitcnt vmcnt(" #N ")" : "+v"(a[0][j]), "+v"(a[1][j]) :: "memory")
    SBAR();
    LDA(3); WAITA(0, 6); MM(0);
    LDA(4); WAITA(1, 6); MM(1);
    LDA(5); WAITA(2, 6); MM(2);
    LDA(6); WAITA(3, 6); MM(3);
    LDA(7); WAITA(4, 6); MM(4);
    asm volatile("s_barrier" ::: "memory");
    LDA(8); WAITA(5, 6); MM(5);
    WAITA(6, 4); MM(6);
    WAITA(7, 2); MM(7);
    WAITA(8, 0); MM(8);
    ST();
#undef MM
#undef WAITA
#undef LDA
    asm volatile("" : "+v"(lane)); m = lane & 15; g = lane >> 4;
    const int tid = w * 64 + lane;
    {
        lds_ptr ex = (lds_ptr)smem + w * 7168 + lane * 16;
#pragma unroll
        for (int c = 0; c < 7; ++c) *(lds_f4)(ex + c * 1024) = acc[1][c];
        asm volatile("s_waitcnt lgkmcnt(0)\n\ts_barrier" ::: "memory");
        lds_ptr ey = (lds_ptr)smem + (w ^ 4) * 7168 + lane * 16;
#pragma unroll
        for (int c = 0; c < 7; ++c) acc[0][c] += *(lds_f4)(ey + c * 1024);
    }
    ST();
    const int h0 = 2 * ct;
    if constexpr (LAYER == 0) {
        u32x2 fr[5];
#pragma unroll
        for (int c = 2; c < 7; ++c) {
            float v[4];
#pragma unroll
            for (int r = 0; r < 4; ++r) {
                float x = rs4[r] * acc[0][c][r];
                if (c < 4) x = phi_f(x);
                else if (c == 6) { const float p = phi_f(x); x = m < 8 ? p : x; x = m == 12 ? 1.f : x; }
                v[r] = x;
            }
            fr[c - 2] = (u32x2){pack2(v[0], v[1]), pack2(v[2], v[3])};
        }
        const bf16x4 k0 = __builtin_bit_cast(bf16x4, fr[0]), k1 = __builtin_bit_cast(bf16x4, fr[1]);
        const bf16x4 v0 = __builtin_bit_cast(bf16x4, fr[2]), v1 = __builtin_bit_cast(bf16x4, fr[3]), T = __builtin_bit_cast(bf16x4, fr[4]);
        const f32x4 z4 = {0.f, 0.f, 0.f, 0.f};
        const f32x4 d1 = MFMA16K16(k0, v0, z4), d2 = MFMA16K16(k1, v1, z4), d3 = MFMA16K16(T, v0, z4), d4 = MFMA16K16(T, v1, z4);
        const f32x4 d5 = MFMA16K16(k0, T, z4), d6 = MFMA16K16(k1, T, z4), d7 = MFMA16K16(T, T, z4);
        lds_ptr red = (lds_ptr)smem + (w ^ 4) * 7168;
        *(lds_f4)(red + (m * 20 + 4 * g) * 4) = d1;
        *(lds_f4)(red + (KV_HEAD + m * 20 + 4 * g) * 4) = d2;
        if (g == 1) {
            *(lds_f2)(red + (m * 20 + 16) * 4) = (f32x2){d3[0], d3[1]};
            *(lds_f2)(red + (KV_HEAD + m * 20 + 16) * 4) = (f32x2){d4[2], d4[3]};
        }
        if (m == 8 || m == 9 || m == 12) {
            const int e = m == 12 ? 18 : 8 + m;
            *(lds_f4)(red + (e * 20 + 4 * g) * 4) = d5;
            if (g == 1) *(lds_f2)(red + (e * 20 + 16) * 4) = (f32x2){d7[0], d7[1]};
        }
        if (m == 10 || m == 11 || m == 12) {
            const int e = m == 12 ? 18 : 6 + m;
            *(lds_f4)(red + (KV_HEAD + e * 20 + 4 * g) * 4) = d6;
            if (g == 1) *(lds_f2)(red + (KV_HEAD + e * 20 + 16) * 4) = (f32x2){d7[2], d7[3]};
        }
        {
            const unsigned o0 = fr[4][0], o1 = fr[4][1];
            const unsigned n0 = __builtin_amdgcn_mov_dpp(o0, 0xB1, 0xf, 0xf, true), n1 = __builtin_amdgcn_mov_dpp(o1, 0xB1, 0xf, 0xf, true);
            if (m == 0 || m == 2)
                *(lds_u4)(red + 4096 + (m >> 1) * 64 + g * 16) =
                    (u32x4){(o0 & 0xffffu) | (n0 << 16), (o0 >> 16) | (n0 & 0xffff0000u), (o1 & 0xffffu) | (n1 << 16), (o1 >> 16) | (n1 & 0xffff0000u)};
        }
        asm volatile("s_waitcnt lgkmcnt(0)\n\ts_barrier" ::: "memory");
        const lds_f1 rd = (lds_f1)smem;
        __attribute__((address_space(1))) float* kvacc = (__attribute__((address_space(1))) float*)(ws + OFF_KV) + (L * NH + h0) * KV_HEAD;
#pragma unroll
        for (int k = 0; k < 2; ++k) {
            const int e = tid + 512 * k;
            if (e < 2 * KV_HEAD && (e % 20) < 18) {
                float s = 0.f;
#pragma unroll
                for (int i = 0; i < 8; ++i) s += rd[i * 1792 + e];
                __hip_atomic_fetch_add(kvacc + e, s, RLX_AGENT);
            }
        }
        ST();
        const Pf pf = pf_wo(ws, smem, 0, bid, w, lane);
        if (FUSED) { PF(pf, 0); PF(pf, 1); signal_cnt<4>(ws, L * C_LAY + C_QA + ct); PF(pf, 2); PF(pf, 3); PF(pf, 4); PFO_LAST(pf); }
        else signal_cnt<0>(ws, L * C_LAY + C_QA + ct);
        u32x4 bq[2];
#pragma unroll
        for (int c = 0; c < 2; ++c) {
            float v[4];
#pragma unroll
            for (int r = 0; r < 4; ++r) v[r] = phi_f(rs * acc[0][c][r]);
            bq[c] = (u32x4){pack2(v[0], v[1]), pack2(v[2], v[3]), *(lds_vu32)(red + 4096 + c * 64 + m * 4), 0u};
        }
        ST();
        wait_cnt(ws, L * C_LAY + C_QA + ct, 16);
        ST();
        f32x4 ka[2], kb[2]; u32x2 ka2[2], kb2[2];
        const int r3 = m & 3; const bool isz = r3 == 2;
        {
            const char* p1 = ws + OFF_KV + ((size_t)(L * NH + h0) * KV_HEAD + m * 20) * 4;
            const char* p2 = ws + OFF_KV + ((size_t)(L * NH + h0) * KV_HEAD + (isz ? 18 : 16 + (r3 & 1)) * 20) * 4;
            gld16c<0>(ka[0], p1 + 16 * g); gld8c<64>(ka2[0], p1); gld16c<0>(kb[0], p2 + 16 * g); gld8c<64>(kb2[0], p2);
            gld16c<KV_HEAD * 4>(ka[1], p1 + 16 * g); gld8c<KV_HEAD * 4 + 64>(ka2[1], p1); gld16c<KV_HEAD * 4>(kb[1], p2 + 16 * g); gld8c<KV_HEAD * 4 + 64>(kb2[1], p2);
            asm volatile("s_waitcnt vmcnt(0)" : "+v"(ka[0]), "+v"(ka[1]), "+v"(kb[0]), "+v"(kb[1]), "+v"(ka2[0]), "+v"(ka2[1]), "+v"(kb2[0]), "+v"(kb2[1]) :: "memory");
        }
        const bool use2 = isz || m < 2;
        unsigned om[2][2], ot[2];
#pragma unroll
        for (int h = 0; h < 2; ++h) {
            const u32x4 f1 = {pack2(ka[h][0], ka[h][1]), pack2(ka[h][2], ka[h][3]), g == 0 ? pack2(__uint_as_float(ka2[h][0]), __uint_as_float(ka2[h][1])) : 0u, 0u};
            u32x4 f2 = {pack2(kb[h][0], kb[h][1]), pack2(kb[h][2], kb[h][3]), g == 0 ? pack2(__uint_as_float(kb2[h][0]), __uint_as_float(kb2[h][1])) : 0u, 0u};
            if (!use2) f2 = (u32x4){0u, 0u, 0u, 0u};
            const bf16x8 b = __builtin_bit_cast(bf16x8, bq[h]);
            const f32x4 e1 = MFMA16(__builtin_bit_cast(bf16x8, f1), b, z4), e2 = MFMA16(__builtin_bit_cast(bf16x8, f2), b, z4);
            const float r = __builtin_amdgcn_rcpf(e2[2] + 1e-6f);
            om[h][0] = pack2(e1[0] * r, e1[1] * r); om[h][1] = pack2(e1[2] * r, e1[3] * r);
            ot[h] = pack2(e2[0] * r, e2[1] * r);
        }
        gst16c(lay + LO_O + ((size_t)(rb * 16 + ct) * 64 + lane) * 16, (u32x4){om[0][0], om[0][1], om[1][0], om[1][1]});
        {
            const unsigned a0 = __builtin_amdgcn_mov_dpp(ot[0], 0x00, 0xf, 0xf, true), a1 = __builtin_amdgcn_mov_dpp(ot[0], 0x55, 0xf, 0xf, true),
                           a2 = __builtin_amdgcn_mov_dpp(ot[0], 0xAA, 0xf, 0xf, true), a3 = __builtin_amdgcn_mov_dpp(ot[0], 0xFF, 0xf, 0xf, true);
            const unsigned b0 = __builtin_amdgcn_mov_dpp(ot[1], 0x00, 0xf, 0xf, true), b1 = __builtin_amdgcn_mov_dpp(ot[1], 0x55, 0xf, 0xf, true),
                           b2 = __builtin_amdgcn_mov_dpp(ot[1], 0xAA, 0xf, 0xf, true), b3 = __builtin_amdgcn_mov_dpp(ot[1], 0xFF, 0xf, 0xf, true);
            const bool h1 = r3 == 1;
            if (g == 0 && r3 < 2)
                gst16c(lay + LO_O + O_TAIL + ((size_t)(rb * 32 + h0 + r3) * 16 + (m & 12)) * 4, (u32x4){h1 ? b0 : a0, h1 ? b1 : a1, h1 ? b2 : a2, h1 ? b3 : a3});
        }
        ST();
        signal_cnt<0>(ws, L * C_LAY + C_OA + panel);
    } else {
        const int tok = rb * 16 + m;
        char* img = lay + LO_IMG + (size_t)h0 * IMG_HEAD + (size_t)(rb >> 4) * IMG_CHUNK;
        const float rq = rs * QSCALE;
#pragma unroll
        for (int p = 0; p < 2; ++p) {
            const float sc = p == 0 ? rq : rs;
            const f32x4 A = acc[0][2 * p], B = acc[0][2 * p + 1];
            auto s0 = __builtin_amdgcn_permlane16_swap(pack2(sc * A[0], sc * A[1]), pack2(sc * B[0], sc * B[1]), false, false);
            auto s1 = __builtin_amdgcn_permlane16_swap(pack2(sc * A[2], sc * A[3]), pack2(sc * B[2], sc * B[3]), false, false);
            const u32x4 d = {s0[0], s1[0], s0[1], s1[1]};
            if (p == 0) gst16c(lay + LO_Q1 + ((size_t)(h0 + (g & 1)) * SEQ + tok) * 32 + (g >> 1) * 16, d);
            else gst16c(img + (g & 1) * IMG_HEAD + ((tok & 255) >> 5) * 1024 + (g >> 1) * 512 + (tok & 31) * 16, d);
        }
        {
            const f32x4 A = acc[0][4], B = acc[0][5];
            auto s0 = __builtin_amdgcn_permlane32_swap(pack2(rs4[0] * A[0], rs4[1] * A[1]), pack2(rs4[0] * B[0], rs4[1] * B[1]), false, false);
            auto s1 = __builtin_amdgcn_permlane32_swap(pack2(rs4[2] * A[2], rs4[3] * A[3]), pack2(rs4[2] * B[2], rs4[3] * B[3]), false, false);
            gst16c(img + (g >> 1) * IMG_HEAD + 9216 + ((rb & 15) * 2 + (g & 1)) * 320 + m * 16, (u32x4){s0[0], s1[0], s0[1], s1[1]});
        }
        {
            const float qs = m < 4 ? QSCALE : 1.f;
            const f32x4 A = acc[0][6];
            const unsigned o0 = pack2(qs * rs4[0] * A[0], qs * rs4[1] * A[1]), o1 = pack2(qs * rs4[2] * A[2], qs * rs4[3] * A[3]);
            const unsigned n0 = __builtin_amdgcn_mov_dpp(o0, 0xB1, 0xf, 0xf, true), n1 = __builtin_amdgcn_mov_dpp(o1, 0xB1, 0xf, 0xf, true);
            auto u0 = __builtin_amdgcn_permlane32_swap(o0, o0, false, false); auto u1 = __builtin_amdgcn_permlane32_swap(o1, o1, false, false);
            const int hsel = (m >> 1) & 1, tok0 = rb * 16 + 4 * g;
            u32x4 d; char* p;
            if (m < 8) {
                d = (u32x4){(o0 & 0xffffu) | (n0 << 16), (o0 >> 16) | (n0 & 0xffff0000u), (o1 & 0xffffu) | (n1 << 16), (o1 >> 16) | (n1 & 0xffff0000u)};
                p = m < 4 ? lay + LO_Q2 + ((size_t)(h0 + hsel) * SEQ + tok0) * 4 : img + hsel * IMG_HEAD + 8192 + (tok0 & 255) * 4;
            } else {
                d = (u32x4){o0, o1, u0[1], u1[1]};
                p = img + hsel * IMG_HEAD + 9216 + ((rb & 15) * 2 + (g & 1)) * 320 + (16 + (m & 1)) * 16;
            }
            if (m < 8 ? !(m & 1) : (m < 12 && g < 2)) gst16c(p, d);
        }
        ST();
        signal_cnt<0>(ws, L * C_LAY + C_QB + ct);
    }
    ST();
    STAMP_OUT(L * 5 + (LAYER ? 2 : 0));
}

template <bool FUSED>
__device__ __forceinline__ void wo_phase(char* ws, char* smem, unsigned short* out, int L, int mode, int w, int lane) {
    LAUNDER();
    const int m = lane & 15, g = lane >> 4, rb4 = w & 3, kh = w >> 2;
    const int xcd = bid & 7, idx = bid >> 3;
    const int panel = 8 * (xcd >> 1) + (idx >> 2), jt = 4 * (xcd & 1) + (idx & 3);
    const int rb = panel * 4 + rb4;
    const int xv = 2 * L + mode;
    const char* ob = ws + OFF_LAY + L * LAY_BYTES + LO_O + mode * O_BYTES;
    const bool last = xv == 3;
    const bool dopf = FUSED && !last;
    const Pf pf = pf_qkv(ws, smem, mode ? 0 : 1, bid, w, lane);
    STAMP_DECL; ST();
    if (!FUSED) { const Pf po = pf_wo(ws, smem, mode, bid, w, lane); PF(po, 0); PF(po, 1); PF(po, 2); PF(po, 3); PF(po, 4); PFO_LAST(po); }
    u32x2 xr[3];
#pragma unroll
    for (int c = 0; c < 3; ++c) {
        const int cb = c == 2 ? 4 : 2 * kh + c, gg = c == 2 ? (g & 1) : g;
        const int n0 = 72 * jt + 16 * cb + 4 * gg;
        gld8c<0>(xr[c], ws + OFF_XF + xv * XF_BYTES + ((size_t)(rb * 18 + (n0 >> 5)) * 64 + ((n0 >> 3) & 3) * 16 + m) * 16 + ((n0 >> 2) & 1) * 8);
    }
    if (dopf) { PF(pf, 0); PF(pf, 1); }
    wait_cnt(ws, L * C_LAY + (mode ? C_AT + (panel >> 2) : C_OA + (panel >> 1)), mode ? 32u : 16u);
    asm volatile("" : "+v"(xr[0]), "+v"(xr[1]), "+v"(xr[2]));
    ST();
    bf16x8 af[9]; unsigned pt[4];
    {
        const char* op = ob + ((size_t)(rb * 16 + 8 * kh) * 64 + lane) * 16;
        gld16c<0>(af[0], op); gld16c<1024>(af[1], op); gld16c<2048>(af[2], op); gld16c<3072>(af[3], op);
        gld16c<0>(af[4], op + 4096); gld16c<1024>(af[5], op + 4096); gld16c<2048>(af[6], op + 4096); gld16c<3072>(af[7], op + 4096);
        const char* tp = ob + O_TAIL + ((size_t)(rb * 32 + 16 * kh + 4 * g) * 16 + m) * 4;
        gld4c<0>(pt[0], tp); gld4c<64>(pt[1], tp); gld4c<128>(pt[2], tp); gld4c<192>(pt[3], tp);
    }
    f32x4 acc[5];
#pragma unroll
    for (int c = 0; c < 5; ++c) acc[c] = (f32x4){0.f, 0.f, 0.f, 0.f};
    const lds_cptr bl = (lds_cptr)smem + LDS_WO + kh * 5 * 1024 + lane * 16;
    bf16x8 bb[3][5];
#pragma unroll
    for (int c = 0; c < 5; ++c) { bb[0][c] = *(lds_f8)(bl + c * 1024); bb[1][c] = *(lds_f8)(bl + (10 + c) * 1024); }
#define KSTEP(i_) do { _Pragma("unroll") for (int c = 0; c < 5; ++c) { \
        if ((i_) < 7) bb[((i_) + 2) % 3][c] = *(lds_f8)(bl + (((i_) + 2) * 10 + c) * 1024); \
        SBAR(); acc[c] = MFMA16(bb[(i_) % 3][c], af[i_], acc[c]); SBAR(); } } while (0)
#define WAITF(i_, N) asm volatile("s_waitcnt vmcnt(" #N ")" : "+v"(af[i_]) :: "memory")
    SBAR();
    WAITF(0, 11); ST(); KSTEP(0); WAITF(1, 10); KSTEP(1); WAITF(2, 9); KSTEP(2); WAITF(3, 8); KSTEP(3);
    WAITF(4, 7); KSTEP(4); WAITF(5, 6); KSTEP(5); WAITF(6, 5); KSTEP(6); WAITF(7, 4); KSTEP(7);
    asm volatile("s_waitcnt vmcnt(0)" : "+v"(pt[0]), "+v"(pt[1]), "+v"(pt[2]), "+v"(pt[3]) :: "memory");
    af[8] = __builtin_bit_cast(bf16x8, (u32x4){pt[0], pt[1], pt[2], pt[3]});
    KSTEP(8);
#undef KSTEP
#undef WAITF
    ST();
    lds_ptr ex = (lds_ptr)smem + rb4 * 5 * 1024 + lane * 16;
    if (kh == 0) { *(lds_f4)(ex + 2 * 1024) = acc[2]; *(lds_f4)(ex + 3 * 1024) = acc[3]; }
    else { *(lds_f4)(ex) = acc[0]; *(lds_f4)(ex + 1024) = acc[1]; *(lds_f4)(ex + 4 * 1024) = acc[4]; }
    asm volatile("s_waitcnt lgkmcnt(0)\n\ts_barrier" ::: "memory");
    unsigned pk[3][2];
    {
        const f32x4 o0 = (kh ? acc[2] : acc[0]) + *(lds_f4)(ex + (2 * kh) * 1024), o1 = (kh ? acc[3] : acc[1]) + *(lds_f4)(ex + (2 * kh + 1) * 1024);
        const f32x4 o2 = acc[4] + *(lds_f4)(ex + 4 * 1024);
        pk[0][0] = pack2(o0[0] + bf_lo(xr[0][0]), o0[1] + bf_hi(xr[0][0])); pk[0][1] = pack2(o0[2] + bf_lo(xr[0][1]), o0[3] + bf_hi(xr[0][1]));
        pk[1][0] = pack2(o1[0] + bf_lo(xr[1][0]), o1[1] + bf_hi(xr[1][0])); pk[1][1] = pack2(o1[2] + bf_lo(xr[1][1]), o1[3] + bf_hi(xr[1][1]));
        pk[2][0] = pack2(o2[0] + bf_lo(xr[2][0]), o2[1] + bf_hi(xr[2][0])); pk[2][1] = pack2(o2[2] + bf_lo(xr[2][1]), o2[3] + bf_hi(xr[2][1]));
    }
    {
        auto s0 = __builtin_amdgcn_permlane16_swap(pk[0][0], pk[1][0], false, false), s1 = __builtin_amdgcn_permlane16_swap(pk[0][1], pk[1][1], false, false);
        auto t0 = __builtin_amdgcn_permlane16_swap(pk[2][0], pk[2][0], false, false), t1 = __builtin_amdgcn_permlane16_swap(pk[2][1], pk[2][1], false, false);
        const u32x4 d = {s0[0], s1[0], s0[1], s1[1]}, e = {t0[0], t1[0], t0[1], t1[1]};
        const int n0 = 72 * jt + 32 * kh + 16 * (g & 1) + 8 * (g >> 1), n1 = 72 * jt + 64;
        if (last) {
            typedef __attribute__((address_space(1))) u32x4* g_u4;
            unsigned short* orow = out + (size_t)(rb * 16 + m) * 576;
            *(g_u4)(orow + n0) = d;
            if (kh == 0 && g == 0) *(g_u4)(orow + n1) = e;
        } else {
            char* xo = ws + OFF_XF + (xv + 1) * XF_BYTES + (size_t)rb * 18 * 1024 + m * 16;
            gst16c(xo + (n0 >> 5) * 1024 + ((n0 >> 3) & 3) * 256, d);
            if (kh == 0 && g == 0) gst16c(xo + (n1 >> 5) * 1024 + ((n1 >> 3) & 3) * 256, e);
        }
    }
    if (!last) {
        const bool v2 = kh == 0 && g < 2;
        float v[12];
#pragma unroll
        for (int c = 0; c < 3; ++c) { v[4 * c] = bf_lo(pk[c][0]); v[4 * c + 1] = bf_hi(pk[c][0]); v[4 * c + 2] = bf_lo(pk[c][1]); v[4 * c + 3] = bf_hi(pk[c][1]); }
        float s = ((v[0] + v[1]) + (v[2] + v[3])) + ((v[4] + v[5]) + (v[6] + v[7])) + (v2 ? (v[8] + v[9]) + (v[10] + v[11]) : 0.f);
        s += __shfl_xor(s, 16); s += __shfl_xor(s, 32);
        const float mean = s * (kh ? (1.f / 32.f) : (1.f / 40.f));
        float q = 0.f, q2 = 0.f;
#pragma unroll
        for (int r = 0; r < 8; ++r) { const float dd = v[r] - mean; q += dd * dd; }
#pragma unroll
        for (int r = 8; r < 12; ++r) { const float dd = v[r] - mean; q2 += dd * dd; }
        q += v2 ? q2 : 0.f;
        q += __shfl_xor(q, 16); q += __shfl_xor(q, 32);
        if (g == 0) gst8c(ws + OFF_ST + (xv + 1) * ST_BYTES + (size_t)(rb * 16 + m) * 128 + jt * 16 + kh * 8, (f32x2){s, q});
        ST();
        const int line = L * C_LAY + (mode ? C_WB : C_WA) + (panel >> 1);
        if (FUSED) { PF(pf, 2); PF(pf, 3); signal_cnt<4>(ws, line); ST(); PF(pf, 4); PF(pf, 5); PF(pf, 6); PFQ_LAST(pf); }
        else signal_cnt<0>(ws, line);
    }
    ST();
    STAMP_OUT(L * 5 + (mode ? 4 : 1));
}

__device__ __forceinline__ float half_max(float v) {
    auto r = __builtin_amdgcn_permlane32_swap(__float_as_uint(v), __float_as_uint(v), false, false);
    return fmaxf(__uint_as_float(r[0]), __uint_as_float(r[1]));
}
#define STR_(x) #x
#define STR(x) STR_(x)
#define AEXP(r) "v_exp_f32 v" STR(r) ", v" STR(r) "\n\t"
#define ACVT(d, r0, r1) "v_cvt_pk_bf16_f32 v" STR(d) ", v" STR(r0) ", v" STR(r1) "\n\t"
#define AE4(r0, r1, r2, r3, d0, d1) AEXP(r0) AEXP(r1) AEXP(r2) AEXP(r3) ACVT(d0, r0, r1) ACVT(d1, r2, r3)
#define ATILE(KO, K2O, V1O, V0O, XK, X0, X1, XV) \
    "v_mfma_f32_32x32x16_bf16 v[48:63], v[80:83], v[72:75], 0\n\t" XK \
    "ds_read_b128 v[80:83], v104 offset:" STR(KO) "\n\t" \
    AE4(32, 33, 34, 35, 96, 97) \
    "v_mfma_f32_32x32x16_bf16 v[16:31], v[92:95], v[100:103], v[16:31]\n\t" \
    "ds_read_b128 v[92:95], v106 offset:" STR(V1O) "\n\t" X0 \
    AE4(36, 37, 38, 39, 98, 99) \
    "v_mfma_f32_32x32x16_bf16 v[48:63], v[84:87], v[76:79], v[48:63]\n\t" \
    "ds_read_b32 v84, v105 offset:" STR(K2O) "\n\t" \
    AE4(40, 41, 42, 43, 100, 101) \
    "s_waitcnt lgkmcnt(3)\n\t" \
    "v_mfma_f32_32x32x16_bf16 v[0:15], v[88:91], v[96:99], v[0:15]\n\t" \
    AE4(44, 45, 46, 47, 102, 103) \
    "s_waitcnt lgkmcnt(1)\n\t" \
    "v_mfma_f32_32x32x16_bf16 v[32:47], v[80:83], v[64:67], 0\n\t" \
    AE4(48, 49, 50, 51, 96, 97) \
    "v_mfma_f32_32x32x16_bf16 v[0:15], v[92:95], v[100:103], v[0:15]\n\t" X1 \
    AE4(52, 53, 54, 55, 98, 99) \
    "s_waitcnt lgkmcnt(0)\n\t" \
    "v_mfma_f32_32x32x16_bf16 v[32:47], v[84:87], v[68:71], v[32:47]\n\t" \
    AE4(56, 57, 58, 59, 100, 101) \
    "v_mfma_f32_32x32x16_bf16 v[16:31], v[88:91], v[96:99], v[16:31]\n\t" XV \
    "ds_read_b128 v[88:91], v106 offset:" STR(V0O) "\n\t" \
    AE4(60, 61, 62, 63, 102, 103)
#define ACHUNK \
    "s_mov_b32 m0, s22\n\ts_add_u32 s28, s20, 0x2000\n\ts_addc_u32 s29, s21, 0\n\tglobal_load_lds_dwordx4 v107, s[20:21] sc1\n\t" \
    "s_add_u32 s27, s22, 0x2000\n\ts_mov_b32 m0, s27\n\ts_cmp_lt_u32 %[w], 3\n\tglobal_load_lds_dwordx4 v107, s[28:29] sc1\n\t" \
    "s_cbranch_scc0 7f\n\t" \
    "s_add_u32 s27, s22, 0x4000\n\ts_mov_b32 m0, s27\n\ts_add_u32 s28, s20, 0x4000\n\ts_addc_u32 s29, s21, 0\n\tglobal_load_lds_dwordx4 v107, s[28:29] sc1\n\t" \
    "7:\n\t" \
    "s_add_u32 s20, s20, 0x4c00\n\ts_addc_u32 s21, s21, 0\n\t"
#define AWPF \
    "s_mov_b32 m0, s25\n\ts_add_u32 s25, s25, 0x4000\n\tglobal_load_lds_dwordx4 v107, s[30:31]\n\tglobal_load_lds_dwordx4 v107, s[30:31] offset:1024\n\t" \
    "s_add_u32 s30, s30, 0x4000\n\ts_addc_u32 s31, s31, 0\n\t"
#define AKADV "v_add_u32 v104, s23, v104\n\tv_add_u32 v105, s23, v105\n\t"
#define AVADV "v_add_u32 v106, s23, v106\n\t"

template <bool FUSED>
__device__ __forceinline__ void attn_phase(char* ws, char* smem, int L, int w, int lane) {
    LAUNDER();
    const int r32 = lane & 31, hh = lane >> 5, kh = w >> 2;
    const int xcd = bid & 7, idx = bid >> 3;
    const int head = 4 * xcd + (idx >> 3), qb = idx & 7;
    const int qA = qb * 256 + (w & 3) * 64 + r32;
    char* lay = ws + OFF_LAY + L * LAY_BYTES;
    const unsigned lds0 = lds_off(smem) + w * 1024;
    STAMP_DECL; ST();
    wait_cnt(ws, L * C_LAY + C_QB + (head >> 1), 16);
    ST();
    bf16x8 q1a, q1b; unsigned q2wa, q2wb;
    {
        const char* qp = lay + LO_Q1 + ((size_t)head * SEQ + qA) * 32 + hh * 16;
        const char* q2p = lay + LO_Q2 + ((size_t)head * SEQ + qA) * 4;
        gld16c<0>(q1a, qp); gld16c<1024>(q1b, qp); gld4c<0>(q2wa, q2p); gld4c<128>(q2wb, q2p);
    }
    const char* srcw = lay + LO_IMG + (size_t)head * IMG_HEAD + w * 1024;
    {
        const char* src = srcw + lane * 16;
#pragma unroll
        for (int k = 0; k < 4; ++k) glds1_sc1(src + k * 8192, lds0 + k * 8192);
        glds1_sc1(src + (w < 6 ? 4 : 3) * 8192, lds0 + (w < 6 ? 4 : 3) * 8192);
    }
    lds_cptr kp1 = (lds_cptr)smem + lane * 16;
    lds_cptr kp2 = (lds_cptr)smem + 8192 + r32 * 4;
    const int dmap = r32 < 19 ? r32 : 19;
    lds_cptr vp = (lds_cptr)smem + 9216 + hh * 320 + dmap * 16;
    asm volatile("s_waitcnt vmcnt(2)\n\ts_barrier" : "+v"(q1a), "+v"(q1b), "+v"(q2wa), "+v"(q2wb) :: "memory");
    ST();
#define LDK1(t) (*(lds_f8)(kp1 + (t) * 1024))
#define LDK2(t) (__builtin_bit_cast(bf16x8, (u32x4){*(lds_vu32)(kp2 + (t) * 128), 0x3f80u, 0u, 0u}))
#define LDV(u) (*(lds_f8)(vp + (u) * 640))
    const f32x16 z16 = {};
    f32x16 s0, s1, oa = {}, ob = {};
    bf16x8 k1 = LDK1(0), k2 = LDK2(0), v0, v1, q2a, q2b;
    u32x4 p0, p1 = {0u, 0u, 0u, 0u};
    {
        q2a = __builtin_bit_cast(bf16x8, (u32x4){hh ? 0u : q2wa, 0u, 0u, 0u});
        q2b = __builtin_bit_cast(bf16x8, (u32x4){hh ? 0u : q2wb, 0u, 0u, 0u});
        f32x16 sa = MFMA32(k1, q1a, z16), sb = MFMA32(k1, q1b, z16); sa = MFMA32(k2, q2a, sa); sb = MFMA32(k2, q2b, sb);
        kp1 += kh * 4096; kp2 += kh * 512; vp += kh * 5120;
        k1 = LDK1(0); k2 = LDK2(0); v0 = LDV(0); v1 = v0;
        float ma = fmaxf(sa[0], sa[1]), mb = fmaxf(sb[0], sb[1]);
#pragma unroll
        for (int i = 2; i < 16; ++i) { ma = fmaxf(ma, sa[i]); mb = fmaxf(mb, sb[i]); }
        ma = half_max(ma); mb = half_max(mb);
        q2a = __builtin_bit_cast(bf16x8, (u32x4){hh ? 0u : q2wa, hh ? 0u : (pack2(-ma, 0.f) & 0xffffu), 0u, 0u});
        q2b = __builtin_bit_cast(bf16x8, (u32x4){hh ? 0u : q2wb, hh ? 0u : (pack2(-mb, 0.f) & 0xffffu), 0u, 0u});
    }
    s0 = MFMA32(k1, q1a, z16); s0 = MFMA32(k2, q2a, s0);
    ST();
    {
        unsigned a1 = (unsigned)(size_t)kp1, a2 = (unsigned)(size_t)kp2, a3 = (unsigned)(size_t)vp;
        const unsigned loff = lane * 16;
        const char* dsrc = srcw + 2 * IMG_CHUNK; const unsigned dbase = lds0;
        const int jt = 4 * (bid & 1) + ((bid >> 3) & 3);
        const char* wsrc = ws + OFF_WOB + (size_t)jt * WO_TILE + w * 2048; const unsigned wdst = lds_off(smem) + LDS_WO + w * 2048;
        asm volatile(
            "s_mov_b32 s26, m0\n\t"
            "s_mov_b64 s[20:21], %[dsrc]\n\t"
            "s_add_u32 s22, %[dbase], 0x9800\n\t"
            "s_mov_b64 s[30:31], %[wsrc]\n\t"
            "s_mov_b32 s25, %[wdst]\n\t"
            "s_mov_b32 s24, 0\n\t"
            "s_mov_b32 s33, 0\n\t"
            "s_mov_b32 s34, 0x4c00\n\t"
            "s_waitcnt lgkmcnt(0)\n\t"
            "s_nop 7\n\t"
            "s_nop 3\n\t"
            "2:\n\t"
            "s_setprio 2\n\t"
            ATILE(1024, 128, 640, 1280, "", "", "", "")
            "s_setprio 1\n\t"
            "s_cmp_eq_u32 s33, 2\n\t"
            "s_cselect_b32 s23, 0xffff6800, s34\n\t"
            "s_sub_u32 s35, s24, 1\n\t"
            "s_cmp_lt_u32 s35, %[npf]\n\t"
            "s_cbranch_scc0 3f\n\t"
            AWPF
            "3:\n\t"
            ATILE(2048, 256, 1920, 2560, "", "", "", "")
            "s_setprio 0\n\t"
            ATILE(3072, 384, 3200, 3840, "", "", "", "")
            "s_waitcnt vmcnt(2)\n\t"
            "s_cmp_lt_u32 s35, %[npf]\n\t"
            "s_cbranch_scc1 4f\n\t"
            "s_waitcnt vmcnt(0)\n\t"
            "4:\n\t"
            "s_barrier\n\t"
            "s_cmp_lt_u32 s24, 6\n\t"
            "s_cbranch_scc0 5f\n\t"
            ACHUNK
            "s_cmp_eq_u32 s33, 0\n\t"
            "s_cselect_b32 s27, 0xffff6800, s34\n\t"
            "s_add_u32 s22, s22, s27\n\t"
            "5:\n\t"
            "s_setprio 3\n\t"
            ATILE(0, 0, 4480, 0, AKADV, "", "", AVADV)
            "s_add_u32 s24, s24, 1\n\t"
            "s_add_u32 s33, s33, 1\n\t"
            "s_cmp_eq_u32 s33, 3\n\t"
            "s_cselect_b32 s33, 0, s33\n\t"
            "s_cmp_lt_u32 s24, 8\n\t"
            "s_cbranch_scc1 2b\n\t"
            "s_waitcnt lgkmcnt(0)\n\t"
            "v_mfma_f32_32x32x16_bf16 v[16:31], v[92:95], v[100:103], v[16:31]\n\t"
            "s_mov_b32 m0, s26\n\t"
            "s_setprio 0\n\t"
            "s_nop 7\n\t"
            "s_nop 3\n\t"
            : "+{v[0:15]}"(oa), "+{v[16:31]}"(ob), "+{v[32:47]}"(s0), "={v[48:63]}"(s1), "+{v[80:83]}"(k1), "+{v[84:87]}"(k2), "+{v[88:91]}"(v0), "+{v[92:95]}"(v1),
              "={v[96:99]}"(p0), "+{v[100:103]}"(p1), "+{v104}"(a1), "+{v105}"(a2), "+{v106}"(a3)
            : "{v[64:67]}"(q1a), "{v[68:71]}"(q2a), "{v[72:75]}"(q1b), "{v[76:79]}"(q2b), "{v107}"(loff), [dsrc] "s"(dsrc), [dbase] "s"(dbase),
              [wsrc] "s"(wsrc), [wdst] "s"(wdst), [w] "s"(w), [npf] "s"(FUSED ? (w < 5 ? 6 : 5) : 0)
            : "s20", "s21", "s22", "s23", "s24", "s25", "s26", "s27", "s28", "s29", "s30", "s31", "s33", "s34", "s35", "scc", "memory");
    }
    ST();
    lds_ptr ex = (lds_ptr)smem + (w < 6 ? w * 3072 : 2 * (int)IMG_CHUNK + 12288 + (w - 6) * 3072) + lane * 16;
    const int wo_ = w ^ 4;
    lds_ptr ey = (lds_ptr)smem + (wo_ < 6 ? wo_ * 3072 : 2 * (int)IMG_CHUNK + 12288 + (wo_ - 6) * 3072) + lane * 16;
    f32x16 o;
    {
        const f32x16 snd = kh ? oa : ob; o = kh ? ob : oa;
        *(lds_f4)(ex) = (f32x4){snd[0], snd[1], snd[2], snd[3]}; *(lds_f4)(ex + 1024) = (f32x4){snd[4], snd[5], snd[6], snd[7]};
        *(lds_f4)(ex + 2048) = (f32x4){snd[8], snd[9], snd[10], snd[11]};
    }
    asm volatile("s_waitcnt lgkmcnt(0)\n\ts_barrier" ::: "memory");
    {
        const f32x4 a = *(lds_f4)(ey), b = *(lds_f4)(ey + 1024), c = *(lds_f4)(ey + 2048);
#pragma unroll
        for (int i = 0; i < 4; ++i) { o[i] += a[i]; o[4 + i] += b[i]; o[8 + i] += c[i]; }
    }
    const int q = qA + 32 * kh;
    bool bad = !(fabsf(o[10]) < 1.0e30f);
#pragma unroll
    for (int i = 0; i < 10; ++i) bad = bad || !(fabsf(o[i]) < 1.0e38f);
    if (__any(bad)) {
        o = (f32x16){};
        float mrun = -INFINITY;
        const bf16x8 q1 = kh ? q1b : q1a;
        const bf16x8 q2 = __builtin_bit_cast(bf16x8, (u32x4){hh ? 0u : (kh ? q2wb : q2wa), 0u, 0u, 0u});
        const char* img = lay + LO_IMG + (size_t)head * IMG_HEAD;
#pragma unroll 1
        for (int t = 0; t < 64; ++t) {
            const char* cb = img + (t >> 3) * IMG_CHUNK; const int tt = t & 7;
            const char* vb = cb + 9216 + tt * 1280 + hh * 320 + dmap * 16;
            bf16x8 fk, fv0, fv1; unsigned fk2;
            gld16c<0>(fk, cb + tt * 1024 + lane * 16); gld4c<0>(fk2, cb + 8192 + tt * 128 + r32 * 4); gld16c<0>(fv0, vb); gld16c<640>(fv1, vb);
            asm volatile("s_waitcnt vmcnt(0)" : "+v"(fk), "+v"(fk2), "+v"(fv0), "+v"(fv1) :: "memory");
            f32x16 s = MFMA32(fk, q1, z16);
            s = MFMA32(__builtin_bit_cast(bf16x8, (u32x4){fk2, 0u, 0u, 0u}), q2, s);
            float mx = fmaxf(s[0], s[1]);
#pragma unroll
            for (int i = 2; i < 16; ++i) mx = fmaxf(mx, s[i]);
            mx = fmaxf(half_max(mx), mrun);
            const float al = EXP2(mrun - mx);
            mrun = mx;
#pragma unroll
            for (int i = 0; i < 16; ++i) o[i] *= al;
#pragma unroll
            for (int i = 0; i < 4; ++i) {
                p0[i] = pack2(EXP2(s[2 * i] - mx), EXP2(s[2 * i + 1] - mx));
                p1[i] = pack2(EXP2(s[8 + 2 * i] - mx), EXP2(s[8 + 2 * i + 1] - mx));
            }
            o = MFMA32(fv0, __builtin_bit_cast(bf16x8, p0), o);
            o = MFMA32(fv1, __builtin_bit_cast(bf16x8, p1), o);
        }
    }
    {
        auto r = __builtin_amdgcn_permlane32_swap(__float_as_uint(o[10]), __float_as_uint(o[10]), false, false);
        const float rl = __builtin_amdgcn_rcpf(__uint_as_float(r[0]));
        const int rb = q >> 4, m = q & 15;
        gst16c(lay + LO_O + O_BYTES + ((size_t)(rb * 16 + (head >> 1)) * 64 + (2 * (head & 1) + hh) * 16 + m) * 16,
               (u32x4){pack2(o[0] * rl, o[1] * rl), pack2(o[2] * rl, o[3] * rl), pack2(o[4] * rl, o[5] * rl), pack2(o[6] * rl, o[7] * rl)});
        const unsigned t0 = pack2(o[8] * rl, o[9] * rl);
        const unsigned t1 = __builtin_amdgcn_mov_dpp(t0, 0x55, 0xf, 0xf, true), t2 = __builtin_amdgcn_mov_dpp(t0, 0xAA, 0xf, 0xf, true),
                       t3 = __builtin_amdgcn_mov_dpp(t0, 0xFF, 0xf, 0xf, true);
        if (hh == 0 && (r32 & 3) == 0) gst16c(lay + LO_O + O_BYTES + O_TAIL + ((size_t)(rb * 32 + head) * 16 + m) * 4, (u32x4){t0, t1, t2, t3});
    }
    ST();
    signal_cnt<0>(ws, L * C_LAY + C_AT + qb);
    ST();
    STAMP_OUT(L * 5 + 3);
#undef LDK1
#undef LDK2
#undef LDV
}

template <int MASK>
__global__ __launch_bounds__(512, 2) void mega_kernel(char* ws, unsigned short* out, int h_lo, int h_hi) {
    __shared__ __attribute__((aligned(16))) char smem[160 * 1024];
    const int lane = threadIdx.x & 63, w = __builtin_amdgcn_readfirstlane(threadIdx.x >> 6);
    constexpr bool FUSED = MASK == 7;
#pragma unroll 1
    for (int hl = h_lo; hl < h_hi; ++hl) {
        const int L = hl >> 1;
        if (hl & 1) {
            if (MASK & 1) qkv_phase<1, FUSED>(ws, smem, L, w, lane);
            if (MASK & 2) attn_phase<FUSED>(ws, smem, L, w, lane);
        } else {
            if (MASK & 1) qkv_phase<0, FUSED>(ws, smem, L, w, lane);
        }
        if (MASK & 4) wo_phase<FUSED>(ws, smem, out, L, hl & 1, w, lane);
    }
}

extern "C" void kernel_launch(void* const* d_in, const int* in_sizes, int n_in, void* d_out, int out_size, void* d_ws, size_t ws_size,
                              hipStream_t stream) {
    const unsigned short* x0 = (const unsigned short*)d_in[0];
    const unsigned short* wA = (const unsigned short*)d_in[1];
    const unsigned short* woA = (const unsigned short*)d_in[2];
    const unsigned short* wB = (const unsigned short*)d_in[4];
    const unsigned short* woB = (const unsigned short*)d_in[5];
    char* ws = (char*)d_ws;
    unsigned short* out = (unsigned short*)d_out;
    prep_kernel<<<PREP_BLOCKS, 256, 0, stream>>>(x0, wA, woA, wB, woB, ws);
    mega_kernel<7><<<256, 512, 0, stream>>>(ws, out, 0, 4);
}
```

```cpp
#include <hip/hip_runtime.h>
#include <hip/hip_bf16.h>
#include <stdint.h>

typedef __attribute__((ext_vector_type(8))) short bf16x8;
typedef __attribute__((ext_vector_type(4))) short bf16x4;
typedef __attribute__((ext_vector_type(4))) float f32x4;
typedef __attribute__((ext_vector_type(2))) float f32x2;
typedef __attribute__((ext_vector_type(16))) float f32x16;
typedef __attribute__((ext_vector_type(4))) unsigned u32x4;
typedef __attribute__((ext_vector_type(2))) unsigned u32x2;
typedef __attribute__((ext_vector_type(2))) __bf16 bfx2;
#define LDSAS __attribute__((address_space(3)))
typedef LDSAS char* lds_ptr;
typedef const LDSAS char* lds_cptr;
typedef LDSAS f32x4* lds_f4;
typedef LDSAS f32x2* lds_f2;
typedef LDSAS float* lds_f1;
typedef LDSAS u32x4* lds_u4;
typedef const volatile LDSAS unsigned* lds_vu32;
typedef const LDSAS bf16x8* lds_f8;

constexpr int SEQ = 2048, DM = 576, NH = 32;

constexpr size_t WT_TILE = 126 * 1024;
constexpr size_t WO_TILE = 90 * 1024;
constexpr size_t IMG_CHUNK = 19 * 1024;
constexpr size_t IMG_HEAD = 8 * IMG_CHUNK;
constexpr int KV_HEAD = 19 * 20;
constexpr size_t XF_BYTES = (size_t)SEQ * DM * 2, ST_BYTES = (size_t)SEQ * 128;
constexpr size_t OFF_CNT = 0;
constexpr size_t CNT_BYTES = 2 * 96 * 256 + 256;
constexpr size_t ZERO_BYTES = 49664;
constexpr size_t OFF_KV  = 65536;
constexpr size_t OFF_PQ  = OFF_KV + 2 * 16 * 16 * 3072;
constexpr size_t OFF_WTA = OFF_PQ + 256 * 512 * 32;
constexpr size_t OFF_WTB = OFF_WTA + 16 * WT_TILE + 4096;
constexpr size_t OFF_CSA = OFF_WTB + 16 * WT_TILE + 4096;
constexpr size_t OFF_CSB = OFF_CSA + 8192;
constexpr size_t OFF_WOA = OFF_CSB + 8192;
constexpr size_t OFF_WOB = OFF_WOA + 8 * WO_TILE + 8192;
constexpr size_t OFF_XF  = OFF_WOB + 8 * WO_TILE + 8192;
constexpr size_t OFF_ST  = OFF_XF + 4 * XF_BYTES;
constexpr size_t OFF_LAY = OFF_ST + 4 * ST_BYTES;
constexpr size_t O_BYTES = (size_t)128 * 16 * 1024 + (size_t)128 * 32 * 64;
constexpr size_t O_TAIL = (size_t)128 * 16 * 1024;
constexpr size_t LO_O   = 0;
constexpr size_t LO_Q1  = LO_O + 2 * O_BYTES;
constexpr size_t LO_Q2  = LO_Q1 + (size_t)NH * SEQ * 32;
constexpr size_t LO_IMG = LO_Q2 + (size_t)NH * SEQ * 4;
constexpr size_t LAY_BYTES = LO_IMG + (size_t)NH * IMG_HEAD;
static_assert(ZERO_BYTES <= OFF_KV && CNT_BYTES <= ZERO_BYTES && ZERO_BYTES % 16 == 0, "layout");
constexpr int C_QA = 0, C_OA = 16, C_WA = 32, C_QB = 48, C_AT = 64, C_WB = 80, C_LAY = 96;

constexpr float QSCALE = 0.23570226039551584f * 1.4426950408889634f;

__device__ __forceinline__ unsigned pack2(float lo, float hi) {
    f32x2 v = {lo, hi};
    bfx2 r = __builtin_convertvector(v, bfx2);
    return __builtin_bit_cast(unsigned, r);
}
__device__ __forceinline__ float bf_lo(unsigned u) { return __uint_as_float(u << 16); }
__device__ __forceinline__ float bf_hi(unsigned u) { return __uint_as_float(u & 0xffff0000u); }
__device__ __forceinline__ float phi_f(float t) { return (t > 0.f ? t + 1.f : __expf(t)) + 1e-6f; }
__device__ __forceinline__ unsigned lds_off(const void* p) { return (unsigned)(size_t)(lds_cptr)p; }

#define MFMA16(a, b, c) __builtin_amdgcn_mfma_f32_16x16x32_bf16(a, b, c, 0, 0, 0)
#define MFMA32(a, b, c) __builtin_amdgcn_mfma_f32_32x32x16_bf16(a, b, c, 0, 0, 0)
#define MFMA16K16(a, b, c) __builtin_amdgcn_mfma_f32_16x16x16bf16_1k(a, b, c, 0, 0, 0)
__device__ __forceinline__ float rowsum4(float v) {
    auto a = __builtin_amdgcn_permlane16_swap(__float_as_uint(v), __float_as_uint(v), false, false);
    v = __uint_as_float(a[0]) + __uint_as_float(a[1]);
    auto b = __builtin_amdgcn_permlane32_swap(__float_as_uint(v), __float_as_uint(v), false, false);
    return __uint_as_float(b[0]) + __uint_as_float(b[1]);
}
#define WARM 3
#define PIN(x) asm volatile("" : "+v"(x))
#define LAUNDER() int bid = blockIdx.x; asm volatile("" : "+v"(lane)); asm volatile("" : "+s"(w), "+s"(ws), "+s"(bid), "+s"(L))
#define SBAR() __builtin_amdgcn_sched_barrier(0)
#define EXP2(x) __builtin_amdgcn_exp2f(x)

template <int N>
__device__ __forceinline__ void glds(const void* g, unsigned lds) {
    unsigned keep;
    if constexpr (N == 4)
        asm volatile("s_mov_b32 %0, m0\n\ts_mov_b32 m0, %2\n\ts_nop 0\n\t"
                     "global_load_lds_dwordx4 %1, off\n\tglobal_load_lds_dwordx4 %1, off offset:1024\n\t"
                     "global_load_lds_dwordx4 %1, off offset:2048\n\tglobal_load_lds_dwordx4 %1, off offset:3072\n\t"
                     "s_mov_b32 m0, %0" : "=&s"(keep) : "v"(g), "s"(lds) : "memory");
    else
        asm volatile("s_mov_b32 %0, m0\n\ts_mov_b32 m0, %2\n\ts_nop 0\n\t"
                     "global_load_lds_dwordx4 %1, off\n\tglobal_load_lds_dwordx4 %1, off offset:1024\n\t"
                     "s_mov_b32 m0, %0" : "=&s"(keep) : "v"(g), "s"(lds) : "memory");
}
__device__ __forceinline__ void glds1_sc1(const void* g, unsigned lds) {
    unsigned keep;
    asm volatile("s_mov_b32 %0, m0\n\ts_mov_b32 m0, %2\n\ts_nop 0\n\tglobal_load_lds_dwordx4 %1, off sc1\n\ts_mov_b32 m0, %0"
                 : "=&s"(keep) : "v"(g), "s"(lds) : "memory");
}
template <int OFF, class T> __device__ __forceinline__ void gld16(T& d, const void* p) {
    asm volatile("global_load_dwordx4 %0, %1, off offset:%2" : "=v"(d) : "v"(p), "i"(OFF) : "memory"); }
template <int OFF, class T> __device__ __forceinline__ void gld4(T& d, const void* p) {
    asm volatile("global_load_dword %0, %1, off offset:%2" : "=v"(d) : "v"(p), "i"(OFF) : "memory"); }
template <int OFF, class T> __device__ __forceinline__ void gld16c(T& d, const void* p) {
    asm volatile("global_load_dwordx4 %0, %1, off offset:%2 sc1" : "=v"(d) : "v"(p), "i"(OFF) : "memory"); }
template <int OFF, class T> __device__ __forceinline__ void gld8c(T& d, const void* p) {
    asm volatile("global_load_dwordx2 %0, %1, off offset:%2 sc1" : "=v"(d) : "v"(p), "i"(OFF) : "memory"); }
template <int OFF, class T> __device__ __forceinline__ void gld4c(T& d, const void* p) {
    asm volatile("global_load_dword %0, %1, off offset:%2 sc1" : "=v"(d) : "v"(p), "i"(OFF) : "memory"); }
__device__ __forceinline__ void gst16c(void* p, u32x4 v) { asm volatile("global_store_dwordx4 %0, %1, off sc1\n\ts_nop 1" :: "v"(p), "v"(v) : "memory"); }
__device__ __forceinline__ void gst8c(void* p, f32x2 v) { asm volatile("global_store_dwordx2 %0, %1, off sc1" :: "v"(p), "v"(v) : "memory"); }

#define STAMP_DECL
#define ST()
#define STAMP_OUT(kid)

typedef __attribute__((address_space(1))) unsigned gu32;
#define RLX_AGENT __ATOMIC_RELAXED, __HIP_MEMORY_SCOPE_AGENT
template <int N> __device__ __forceinline__ void signal_cnt(char* ws, int line) {
    asm volatile("s_waitcnt vmcnt(%0) lgkmcnt(0)\n\ts_barrier" :: "i"(N) : "memory");
    if (threadIdx.x == 0) __hip_atomic_fetch_add((gu32*)(ws + OFF_CNT + (size_t)line * 256), 1u, RLX_AGENT);
}
__device__ __forceinline__ void wait_cnt(char* ws, int line, unsigned want) {
    if (threadIdx.x < 64) {
        gu32* p = (gu32*)(ws + OFF_CNT + (size_t)line * 256);
        gu32* tmo = (gu32*)(ws + OFF_CNT + 2 * C_LAY * 256);
        for (unsigned spins = 0;;) {
            if ((unsigned)__builtin_amdgcn_readfirstlane(__hip_atomic_load(p, RLX_AGENT)) >= want) break;
            __builtin_amdgcn_s_sleep(1);
            if (++spins > (1u << 18)) { __hip_atomic_store(tmo, 1u, RLX_AGENT); break; }
            if ((spins & 255u) == 0u && __builtin_amdgcn_readfirstlane(__hip_atomic_load(tmo, RLX_AGENT))) break;
        }
    }
    asm volatile("s_waitcnt vmcnt(0) lgkmcnt(0)\n\ts_barrier" ::: "memory");
}

__device__ __forceinline__ void qkv_col_map(int n, int& ct, int& cb, int& c) {
    const int part = n / 576, rem = n - part * 576, head = rem / 18, d = rem - head * 18;
    ct = head >> 1; const int hb = head & 1;
    if (d < 16) { cb = part * 2 + hb; c = d; } else { cb = 6; c = part * 4 + hb * 2 + (d - 16); }
}
__device__ __forceinline__ float dot_ones(unsigned u, float acc) {
    return __builtin_amdgcn_fdot2_f32_bf16(__builtin_bit_cast(bfx2, u), __builtin_bit_cast(bfx2, 0x3f803f80u), acc, false); }
__device__ __forceinline__ float dot_self(unsigned u, float acc) {
    return __builtin_amdgcn_fdot2_f32_bf16(__builtin_bit_cast(bfx2, u), __builtin_bit_cast(bfx2, u), acc, false); }
__device__ __forceinline__ void transpose8x4(const u32x2 (&v)[8], u32x4 (&c)[4]) {
#pragma unroll
    for (int k = 0; k < 4; ++k) {
        c[0][k] = __builtin_amdgcn_perm(v[2 * k + 1][0], v[2 * k][0], 0x05040100u); c[1][k] = __builtin_amdgcn_perm(v[2 * k + 1][0], v[2 * k][0], 0x07060302u);
        c[2][k] = __builtin_amdgcn_perm(v[2 * k + 1][1], v[2 * k][1], 0x05040100u); c[3][k] = __builtin_amdgcn_perm(v[2 * k + 1][1], v[2 * k][1], 0x07060302u);
    }
}
template <int OFF, class T> __device__ __forceinline__ void gld8(T& d, const void* p) {
    asm volatile("global_load_dwordx2 %0, %1, off offset:%2" : "=v"(d) : "v"(p), "i"(OFF) : "memory"); }

constexpr int LDS_W = 34 * 1024, LDS_WO = 70 * 1024;
struct Pf { const char* src; unsigned dst; };
__device__ __forceinline__ Pf pf_qkv(char* ws, char* smem, int layer, int bid, int w, int lane) {
    const int ct = 8 * (bid & 1) + ((bid >> 3) & 7);
    return Pf{ws + (layer ? OFF_WTB : OFF_WTA) + (size_t)ct * WT_TILE + w * 2048 + lane * 16, lds_off(smem) + LDS_W + w * 2048};
}
__device__ __forceinline__ Pf pf_wo(char* ws, char* smem, int mode, int bid, int w, int lane) {
    const int jt = (bid >> 3) & 7;
    return Pf{ws + (mode ? OFF_WOB : OFF_WOA) + (size_t)jt * WO_TILE + w * 2048 + lane * 16, lds_off(smem) + LDS_WO + w * 2048};
}
#define PF(p, j) glds<2>((p).src + (j) * 16384, (p).dst + (j) * 16384)
#define PF_LAST(p, j, n) do { const int jj_ = w < (n) ? (j) : (j) - 1; PF(p, jj_); } while (0)
#define PFQ_LAST(p) PF_LAST(p, 7, 7)
#define PFO_LAST(p) PF_LAST(p, 5, 5)

__device__ __forceinline__ void lin_kv_part(char* ws, char* smem, int L, int panel, int ct, int w, int lane, const f32x4 (&acc)[7], float rs, const float (&rs4)[4], u32x4 (&bq)[2]) {
    const int m = lane & 15, g = lane >> 4, tid = w * 64 + lane;
    {
        u32x2 fr[5];
#pragma unroll
        for (int c = 2; c < 7; ++c) {
            float v[4];
#pragma unroll
            for (int r = 0; r < 4; ++r) {
                float x = rs4[r] * acc[c][r];
                if (c < 4) x = phi_f(x);
                else if (c == 6) { const float p = phi_f(x); x = m < 8 ? p : x; x = m == 12 ? 1.f : x; }
                v[r] = x;
            }
            fr[c - 2] = (u32x2){pack2(v[0], v[1]), pack2(v[2], v[3])};
        }
        const bf16x4 k0 = __builtin_bit_cast(bf16x4, fr[0]), k1 = __builtin_bit_cast(bf16x4, fr[1]);
        const bf16x4 v0 = __builtin_bit_cast(bf16x4, fr[2]), v1 = __builtin_bit_cast(bf16x4, fr[3]), T = __builtin_bit_cast(bf16x4, fr[4]);
        const f32x4 z4 = {0.f, 0.f, 0.f, 0.f};
        const f32x4 d1 = MFMA16K16(k0, v0, z4), d2 = MFMA16K16(k1, v1, z4), d3 = MFMA16K16(T, v0, z4), d4 = MFMA16K16(T, v1, z4);
        const f32x4 d5 = MFMA16K16(k0, T, z4), d6 = MFMA16K16(k1, T, z4), d7 = MFMA16K16(T, T, z4);
        lds_ptr red = (lds_ptr)smem + (w ^ 4) * 7168;
        *(lds_f4)(red + (m * 20 + 4 * g) * 4) = d1;
        *(lds_f4)(red + (KV_HEAD + m * 20 + 4 * g) * 4) = d2;
        if (g == 1) {
            *(lds_f2)(red + (m * 20 + 16) * 4) = (f32x2){d3[0], d3[1]};
            *(lds_f2)(red + (KV_HEAD + m * 20 + 16) * 4) = (f32x2){d4[2], d4[3]};
        }
        if (m == 8 || m == 9 || m == 12) {
            const int e = m == 12 ? 18 : 8 + m;
            *(lds_f4)(red + (e * 20 + 4 * g) * 4) = d5;
            if (g == 1) *(lds_f2)(red + (e * 20 + 16) * 4) = (f32x2){d7[0], d7[1]};
        }
        if (m == 10 || m == 11 || m == 12) {
            const int e = m == 12 ? 18 : 6 + m;
            *(lds_f4)(red + (KV_HEAD + e * 20 + 4 * g) * 4) = d6;
            if (g == 1) *(lds_f2)(red + (KV_HEAD + e * 20 + 16) * 4) = (f32x2){d7[2], d7[3]};
        }
        {
            const unsigned o0 = fr[4][0], o1 = fr[4][1];
            const unsigned n0 = __builtin_amdgcn_mov_dpp(o0, 0xB1, 0xf, 0xf, true), n1 = __builtin_amdgcn_mov_dpp(o1, 0xB1, 0xf, 0xf, true);
            if (m == 0 || m == 2)
                *(lds_u4)(red + 4096 + (m >> 1) * 64 + g * 16) =
                    (u32x4){(o0 & 0xffffu) | (n0 << 16), (o0 >> 16) | (n0 & 0xffff0000u), (o1 & 0xffffu) | (n1 << 16), (o1 >> 16) | (n1 & 0xffff0000u)};
        }
        asm volatile("s_waitcnt lgkmcnt(0)\n\ts_barrier" ::: "memory");
        char* kvp = ws + OFF_KV + (size_t)(L * 16 + ct) * 16 * 3072;
        if (tid < 190) {
            f32x4 s4 = *(lds_f4)((lds_ptr)smem + tid * 16);
#pragma unroll
            for (int i = 1; i < 8; ++i) s4 += *(lds_f4)((lds_ptr)smem + i * 7168 + tid * 16);
            gst16c(kvp + panel * 3072 + tid * 16, __builtin_bit_cast(u32x4, s4));
        }
#pragma unroll
        for (int c = 0; c < 2; ++c) {
            float v[4];
#pragma unroll
            for (int r = 0; r < 4; ++r) v[r] = phi_f(rs * acc[c][r]);
            bq[c] = (u32x4){pack2(v[0], v[1]), pack2(v[2], v[3]), *(lds_vu32)(red + 4096 + c * 64 + m * 4), 0u};
        }
    }
}
__device__ __forceinline__ void lin_o_part(char* ws, char* smem, int L, int rb, int ct, int w, int lane, u32x4 (&bq)[2]) {
    const int m = lane & 15, g = lane >> 4, tid = w * 64 + lane, h0 = 2 * ct;
    char* lay = ws + OFF_LAY + L * LAY_BYTES;
    char* kvp = ws + OFF_KV + (size_t)(L * 16 + ct) * 16 * 3072;
    const f32x4 z4 = {0.f, 0.f, 0.f, 0.f};
    {
        {
            f32x4 pa[3], pb[3];
            const char* pp = kvp + w * 6144 + lane * 16;
            gld16c<0>(pa[0], pp); gld16c<1024>(pa[1], pp); gld16c<2048>(pa[2], pp);
            gld16c<0>(pb[0], pp + 3072); gld16c<1024>(pb[1], pp + 3072); gld16c<2048>(pb[2], pp + 3072);
            asm volatile("s_waitcnt vmcnt(0)" : "+v"(pa[0]), "+v"(pa[1]), "+v"(pa[2]), "+v"(pb[0]), "+v"(pb[1]), "+v"(pb[2]), "+v"(bq[0]), "+v"(bq[1]) :: "memory");
#pragma unroll
            for (int i = 0; i < 3; ++i) *(lds_f4)((lds_ptr)smem + w * 3072 + i * 1024 + lane * 16) = pa[i] + pb[i];
            asm volatile("s_waitcnt lgkmcnt(0)\n\ts_barrier" ::: "memory");
            if (tid < 190) {
                f32x4 s4 = *(lds_f4)((lds_ptr)smem + tid * 16);
#pragma unroll
                for (int i = 1; i < 8; ++i) s4 += *(lds_f4)((lds_ptr)smem + i * 3072 + tid * 16);
                *(lds_f4)((lds_ptr)smem + 24576 + tid * 16) = s4;
            }
            asm volatile("s_waitcnt lgkmcnt(0)\n\ts_barrier" ::: "memory");
        }
        f32x4 ka[2], kb[2]; f32x2 ka2[2], kb2[2];
        const int r3 = m & 3; const bool isz = r3 == 2;
        {
            lds_ptr p1 = (lds_ptr)smem + 24576 + m * 80, p2 = (lds_ptr)smem + 24576 + (isz ? 18 : 16 + (r3 & 1)) * 80;
#pragma unroll
            for (int h = 0; h < 2; ++h) {
                ka[h] = *(lds_f4)(p1 + h * KV_HEAD * 4 + 16 * g); ka2[h] = *(lds_f2)(p1 + h * KV_HEAD * 4 + 64);
                kb[h] = *(lds_f4)(p2 + h * KV_HEAD * 4 + 16 * g); kb2[h] = *(lds_f2)(p2 + h * KV_HEAD * 4 + 64);
            }
        }
        const bool use2 = isz || m < 2;
        unsigned om[2][2], ot[2];
#pragma unroll
        for (int h = 0; h < 2; ++h) {
            const u32x4 f1 = {pack2(ka[h][0], ka[h][1]), pack2(ka[h][2], ka[h][3]), g == 0 ? pack2(ka2[h][0], ka2[h][1]) : 0u, 0u};
            u32x4 f2 = {pack2(kb[h][0], kb[h][1]), pack2(kb[h][2], kb[h][3]), g == 0 ? pack2(kb2[h][0], kb2[h][1]) : 0u, 0u};
            if (!use2) f2 = (u32x4){0u, 0u, 0u, 0u};
            const bf16x8 b = __builtin_bit_cast(bf16x8, bq[h]);
            const f32x4 e1 = MFMA16(__builtin_bit_cast(bf16x8, f1), b, z4), e2 = MFMA16(__builtin_bit_cast(bf16x8, f2), b, z4);
            const float r = __builtin_amdgcn_rcpf(e2[2] + 1e-6f);
            om[h][0] = pack2(e1[0] * r, e1[1] * r); om[h][1] = pack2(e1[2] * r, e1[3] * r);
            ot[h] = pack2(e2[0] * r, e2[1] * r);
        }
        gst16c(lay + LO_O + ((size_t)(rb * 16 + ct) * 64 + lane) * 16, (u32x4){om[0][0], om[0][1], om[1][0], om[1][1]});
        {
            const unsigned a0 = __builtin_amdgcn_mov_dpp(ot[0], 0x00, 0xf, 0xf, true), a1 = __builtin_amdgcn_mov_dpp(ot[0], 0x55, 0xf, 0xf, true),
                           a2 = __builtin_amdgcn_mov_dpp(ot[0], 0xAA, 0xf, 0xf, true), a3 = __builtin_amdgcn_mov_dpp(ot[0], 0xFF, 0xf, 0xf, true);
            const unsigned b0 = __builtin_amdgcn_mov_dpp(ot[1], 0x00, 0xf, 0xf, true), b1 = __builtin_amdgcn_mov_dpp(ot[1], 0x55, 0xf, 0xf, true),
                           b2 = __builtin_amdgcn_mov_dpp(ot[1], 0xAA, 0xf, 0xf, true), b3 = __builtin_amdgcn_mov_dpp(ot[1], 0xFF, 0xf, 0xf, true);
            const bool h1 = r3 == 1;
            if (g == 0 && r3 < 2)
                gst16c(lay + LO_O + O_TAIL + ((size_t)(rb * 32 + h0 + r3) * 16 + (m & 12)) * 4, (u32x4){h1 ? b0 : a0, h1 ? b1 : a1, h1 ? b2 : a2, h1 ? b3 : a3});
        }
    }
}

template <int LAYER, bool FUSED>
__device__ __forceinline__ void qkv_phase(char* ws, char* smem, int L, int w, int lane) {
    LAUNDER();
    int m = lane & 15, g = lane >> 4; const int rp = w & 3, kh = w >> 2;
    const int xcd = bid & 7, idx = bid >> 3;
    const int panel = 4 * (xcd >> 1) + (idx >> 3), ct = 8 * (xcd & 1) + (idx & 7);
    const int rb = panel * 8 + 2 * rp + kh, rbo = panel * 8 + 2 * rp + 1 - kh;
    const int xv = 2 * L + LAYER;
    char* lay = ws + OFF_LAY + L * LAY_BYTES;
    STAMP_DECL; ST();
    f32x4 st[2], cs4[4]; float cs1[5];
    {
        const float* cs = (const float*)(ws + (LAYER ? OFF_CSB : OFF_CSA)) + ct * 112;
        const float* c4 = cs + 4 * g; const float* c1 = cs + m;
        if constexpr (LAYER == 0) {
            gld16<0>(cs4[0], c4); gld16<64>(cs4[1], c4);
            gld4<128>(cs1[0], c1); gld4<192>(cs1[1], c1); gld4<256>(cs1[2], c1); gld4<320>(cs1[3], c1); gld4<384>(cs1[4], c1);
        } else {
            gld16<0>(cs4[0], c4); gld16<64>(cs4[1], c4); gld16<128>(cs4[2], c4); gld16<192>(cs4[3], c4);
            gld4<256>(cs1[0], c1); gld4<320>(cs1[1], c1); gld4<384>(cs1[2], c1);
        }
    }
    if (!FUSED || xv == 0) {
        const Pf pf = pf_qkv(ws, smem, LAYER, bid, w, lane);
        PF(pf, 0); PF(pf, 1); PF(pf, 2); PF(pf, 3); PF(pf, 4); PF(pf, 5); PF(pf, 6); PFQ_LAST(pf);
    }
    if (xv) wait_cnt(ws, LAYER ? L * C_LAY + C_WA + panel : C_WB + panel, 16u);
    else asm volatile("s_waitcnt vmcnt(0)\n\ts_barrier" ::: "memory");
    ST();
    bf16x8 a[2][9];
    const char* xa0 = ws + OFF_XF + xv * XF_BYTES + (size_t)(rb * 18 + kh) * 1024 + lane * 16;
    const char* xa1 = ws + OFF_XF + xv * XF_BYTES + (size_t)(rbo * 18 + kh) * 1024 + lane * 16;
#define LDA(j) do { gld16c<((j) & 1) * 2048>(a[0][j], xa0 + ((j) >> 1) * 4096); gld16c<((j) & 1) * 2048>(a[1][j], xa1 + ((j) >> 1) * 4096); } while (0)
    {
        const char* sp = ws + OFF_ST + xv * ST_BYTES + (size_t)(rb * 16 + m) * 128 + g * 32;
        gld16c<0>(st[0], sp); gld16c<16>(st[1], sp);
    }
    LDA(0); LDA(1); LDA(2);
    const lds_cptr bl = (lds_cptr)smem + LDS_W + kh * 7 * 1024 + lane * 16;
    bf16x8 bb[2][7];
#pragma unroll
    for (int c = 0; c < 7; ++c) bb[0][c] = *(lds_f8)(bl + c * 1024);
    constexpr int NTR = LAYER == 0 ? 2 : 4;
    f32x4 acc[2][7];
#pragma unroll
    for (int c = 0; c < 7; ++c) { acc[0][c] = (f32x4){0.f, 0.f, 0.f, 0.f}; acc[1][c] = (f32x4){0.f, 0.f, 0.f, 0.f}; }
    float nmu, rs, nmu4[4], rs4[4];
#define MM(j) do { _Pragma("unroll") for (int c = 0; c < 7; ++c) { \
            if ((j) < 8) bb[((j) + 1) & 1][c] = *(lds_f8)(bl + (((j) + 1) * 14 + c) * 1024); \
            SBAR(); \
            if (c < NTR) { acc[1][c] = MFMA16(bb[(j) & 1][c], a[1][j], acc[1][c]); acc[0][c] = MFMA16(bb[(j) & 1][c], a[0][j], acc[0][c]); } \
            else         { acc[1][c] = MFMA16(a[1][j], bb[(j) & 1][c], acc[1][c]); acc[0][c] = MFMA16(a[0][j], bb[(j) & 1][c], acc[0][c]); } \
            SBAR(); \
        } } while (0)
#define WAITA(j, N) asm volatile("s_waitcnt vmcnt(" #N ")" : "+v"(a[0][j]), "+v"(a[1][j]) :: "memory")
    SBAR();
    WAITA(0, 4); ST(); LDA(3); LDA(4); MM(0);
    WAITA(1, 6); LDA(5); LDA(6); MM(1);
    {
        if constexpr (LAYER == 0) asm volatile("" : "+v"(st[0]), "+v"(st[1]), "+v"(cs4[0]), "+v"(cs4[1]), "+v"(cs1[0]), "+v"(cs1[1]), "+v"(cs1[2]), "+v"(cs1[3]), "+v"(cs1[4]));
        else asm volatile("" : "+v"(st[0]), "+v"(st[1]), "+v"(cs4[0]), "+v"(cs4[1]), "+v"(cs4[2]), "+v"(cs4[3]), "+v"(cs1[0]), "+v"(cs1[1]), "+v"(cs1[2]));
        float s = rowsum4(st[0][0] + st[0][2] + st[1][0] + st[1][2]);
        const float mean = s * (1.f / 576.f);
        const float d0 = st[0][0] * (1.f / 40.f) - mean, d1 = st[0][2] * (1.f / 32.f) - mean;
        const float d2 = st[1][0] * (1.f / 40.f) - mean, d3 = st[1][2] * (1.f / 32.f) - mean;
        const float q = rowsum4(st[0][1] + st[0][3] + st[1][1] + st[1][3] + 40.f * (d0 * d0 + d2 * d2) + 32.f * (d1 * d1 + d3 * d3));
        nmu = -mean; rs = __builtin_amdgcn_rsqf(q * (1.f / 576.f) + 1e-5f);
#pragma unroll
        for (int r = 0; r < 4; ++r) { nmu4[r] = __shfl(nmu, 4 * g + r); rs4[r] = __shfl(rs, 4 * g + r); }
#pragma unroll
        for (int c = 0; c < NTR; ++c) cs4[c] *= nmu;
        SBAR();
    }
    WAITA(2, 8); LDA(7); LDA(8); MM(2);
    WAITA(3, 10); MM(3);
    WAITA(4, 8); MM(4);
    asm volatile("s_barrier" ::: "memory");
    WAITA(5, 6); MM(5);
    WAITA(6, 4); MM(6);
    WAITA(7, 2); MM(7);
    WAITA(8, 0); MM(8);
    ST();
#undef MM
#undef WAITA
#undef LDA
    asm volatile("" : "+v"(lane)); m = lane & 15; g = lane >> 4;
    {
        lds_ptr ex = (lds_ptr)smem + w * 7168 + lane * 16;
#pragma unroll
        for (int c = 0; c < 7; ++c) *(lds_f4)(ex + c * 1024) = acc[1][c];
        asm volatile("s_waitcnt lgkmcnt(0)\n\ts_barrier" ::: "memory");
        lds_ptr ey = (lds_ptr)smem + (w ^ 4) * 7168 + lane * 16;
#pragma unroll
        for (int c = 0; c < 7; ++c) {
            acc[0][c] += *(lds_f4)(ey + c * 1024);
            if (c < NTR) acc[0][c] += cs4[c];
            else { const float cv = cs1[c - NTR]; acc[0][c] += (f32x4){nmu4[0] * cv, nmu4[1] * cv, nmu4[2] * cv, nmu4[3] * cv}; }
        }
    }
    ST();
    const int h0 = 2 * ct;
    if constexpr (LAYER == 0) {
        u32x4 bq[2];
        lin_kv_part(ws, smem, L, panel, ct, w, lane, acc[0], rs, rs4, bq);
        ST();
        const Pf pf = pf_wo(ws, smem, 0, bid, w, lane);
        if (FUSED) { PF(pf, 0); PF(pf, 1); signal_cnt<4>(ws, L * C_LAY + C_QA + ct); PF(pf, 2); PF(pf, 3); PF(pf, 4); PFO_LAST(pf); }
        else signal_cnt<0>(ws, L * C_LAY + C_QA + ct);
        ST();
        wait_cnt(ws, L * C_LAY + C_QA + ct, 16);
        ST();
        lin_o_part(ws, smem, L, rb, ct, w, lane, bq);
        ST();
        signal_cnt<0>(ws, L * C_LAY + C_OA + panel);
    } else {
        const int tok = rb * 16 + m;
        char* img = lay + LO_IMG + (size_t)h0 * IMG_HEAD + (size_t)(rb >> 4) * IMG_CHUNK;
        const float rq = rs * QSCALE;
#pragma unroll
        for (int p = 0; p < 2; ++p) {
            const float sc = p == 0 ? rq : rs;
            const f32x4 A = acc[0][2 * p], B = acc[0][2 * p + 1];
            auto s0 = __builtin_amdgcn_permlane16_swap(pack2(sc * A[0], sc * A[1]), pack2(sc * B[0], sc * B[1]), false, false);
            auto s1 = __builtin_amdgcn_permlane16_swap(pack2(sc * A[2], sc * A[3]), pack2(sc * B[2], sc * B[3]), false, false);
            const u32x4 d = {s0[0], s1[0], s0[1], s1[1]};
            if (p == 0) gst16c(lay + LO_Q1 + ((size_t)(h0 + (g & 1)) * SEQ + tok) * 32 + (g >> 1) * 16, d);
            else gst16c(img + (g & 1) * IMG_HEAD + ((tok & 255) >> 5) * 1024 + (g >> 1) * 512 + (tok & 31) * 16, d);
        }
        {
            const f32x4 A = acc[0][4], B = acc[0][5];
            auto s0 = __builtin_amdgcn_permlane32_swap(pack2(rs4[0] * A[0], rs4[1] * A[1]), pack2(rs4[0] * B[0], rs4[1] * B[1]), false, false);
            auto s1 = __builtin_amdgcn_permlane32_swap(pack2(rs4[2] * A[2], rs4[3] * A[3]), pack2(rs4[2] * B[2], rs4[3] * B[3]), false, false);
            gst16c(img + (g >> 1) * IMG_HEAD + 9216 + ((rb & 15) * 2 + (g & 1)) * 320 + m * 16, (u32x4){s0[0], s1[0], s0[1], s1[1]});
        }
        {
            const float qs = m < 4 ? QSCALE : 1.f;
            const f32x4 A = acc[0][6];
            const unsigned o0 = pack2(qs * rs4[0] * A[0], qs * rs4[1] * A[1]), o1 = pack2(qs * rs4[2] * A[2], qs * rs4[3] * A[3]);
            const unsigned n0 = __builtin_amdgcn_mov_dpp(o0, 0xB1, 0xf, 0xf, true), n1 = __builtin_amdgcn_mov_dpp(o1, 0xB1, 0xf, 0xf, true);
            auto u0 = __builtin_amdgcn_permlane32_swap(o0, o0, false, false); auto u1 = __builtin_amdgcn_permlane32_swap(o1, o1, false, false);
            const int hsel = (m >> 1) & 1, tok0 = rb * 16 + 4 * g;
            u32x4 d; char* p;
            if (m < 8) {
                d = (u32x4){(o0 & 0xffffu) | (n0 << 16), (o0 >> 16) | (n0 & 0xffff0000u), (o1 & 0xffffu) | (n1 << 16), (o1 >> 16) | (n1 & 0xffff0000u)};
                p = m < 4 ? lay + LO_Q2 + ((size_t)(h0 + hsel) * SEQ + tok0) * 4 : img + hsel * IMG_HEAD + 8192 + (tok0 & 255) * 4;
            } else {
                const unsigned cst = (m & 1) ? 0u : 0x3f803f80u;
                d = m < 12 ? (u32x4){o0, o1, u0[1], u1[1]} : (u32x4){cst, cst, cst, cst};
                p = img + hsel * IMG_HEAD + 9216 + ((rb & 15) * 2 + (g & 1)) * 320 + (16 + (m & 1) + ((m >> 1) & 2)) * 16;
            }
            if (m < 8 ? !(m & 1) : g < 2) gst16c(p, d);
        }
        ST();
        signal_cnt<0>(ws, L * C_LAY + C_QB + ct);
    }
    ST();
    STAMP_OUT(L * 5 + (LAYER ? 2 : 0));
}

template <bool FUSED>
__device__ __forceinline__ void wo_phase(char* ws, char* smem, const unsigned short* x0, unsigned short* out, int L, int mode, int w, int lane) {
    LAUNDER();
    const int m = lane & 15, g = lane >> 4, rb4 = w & 3, kh = w >> 2;
    const int xcd = bid & 7, idx = bid >> 3;
    const int panel = 4 * xcd + (idx >> 3), jt = idx & 7;
    const int rb = panel * 4 + rb4;
    const int xv = 2 * L + mode;
    const char* ob = ws + OFF_LAY + L * LAY_BYTES + LO_O + mode * O_BYTES;
    const bool last = xv == 3;
    const bool dopf = FUSED && !last;
    const Pf pf = pf_qkv(ws, smem, mode ? 0 : 1, bid, w, lane);
    STAMP_DECL; ST();
    if (!FUSED) { const Pf po = pf_wo(ws, smem, mode, bid, w, lane); PF(po, 0); PF(po, 1); PF(po, 2); PF(po, 3); PF(po, 4); PFO_LAST(po); }
    u32x2 xr[3];
#pragma unroll
    for (int c = 0; c < 3; ++c) {
        const int cb = c == 2 ? 4 : 2 * kh + c, gg = c == 2 ? (g & 1) : g;
        const int n0 = 72 * jt + 16 * cb + 4 * gg;
        const char* xp = xv == 0 ? (const char*)(x0 + (size_t)(rb * 16 + m) * 576 + n0)
                                 : ws + OFF_XF + xv * XF_BYTES + ((size_t)(rb * 18 + (n0 >> 5)) * 64 + ((n0 >> 3) & 3) * 16 + m) * 16 + ((n0 >> 2) & 1) * 8;
        gld8c<0>(xr[c], xp);
    }
    if (dopf) { PF(pf, 0); PF(pf, 1); }
    wait_cnt(ws, L * C_LAY + (mode ? C_AT + (panel >> 2) : C_OA + (panel >> 1)), mode ? 32u : 16u);
    asm volatile("" : "+v"(xr[0]), "+v"(xr[1]), "+v"(xr[2]));
    ST();
    bf16x8 af[9]; unsigned pt[4];
    {
        const char* op = ob + ((size_t)(rb * 16 + 8 * kh) * 64 + lane) * 16;
        gld16c<0>(af[0], op); gld16c<1024>(af[1], op); gld16c<2048>(af[2], op); gld16c<3072>(af[3], op);
        gld16c<0>(af[4], op + 4096); gld16c<1024>(af[5], op + 4096); gld16c<2048>(af[6], op + 4096); gld16c<3072>(af[7], op + 4096);
        const char* tp = ob + O_TAIL + ((size_t)(rb * 32 + 16 * kh + 4 * g) * 16 + m) * 4;
        gld4c<0>(pt[0], tp); gld4c<64>(pt[1], tp); gld4c<128>(pt[2], tp); gld4c<192>(pt[3], tp);
    }
    f32x4 acc[5];
#pragma unroll
    for (int c = 0; c < 5; ++c) acc[c] = (f32x4){0.f, 0.f, 0.f, 0.f};
    const lds_cptr bl = (lds_cptr)smem + LDS_WO + kh * 5 * 1024 + lane * 16;
    bf16x8 bb[3][5];
#pragma unroll
    for (int c = 0; c < 5; ++c) { bb[0][c] = *(lds_f8)(bl + c * 1024); bb[1][c] = *(lds_f8)(bl + (10 + c) * 1024); }
#define KSTEP(i_) do { _Pragma("unroll") for (int c = 0; c < 5; ++c) { \
        if ((i_) < 7) bb[((i_) + 2) % 3][c] = *(lds_f8)(bl + (((i_) + 2) * 10 + c) * 1024); \
        SBAR(); acc[c] = MFMA16(bb[(i_) % 3][c], af[i_], acc[c]); SBAR(); } } while (0)
#define WAITF(i_, N) asm volatile("s_waitcnt vmcnt(" #N ")" : "+v"(af[i_]) :: "memory")
    SBAR();
    WAITF(0, 11); ST(); KSTEP(0); WAITF(1, 10); KSTEP(1); WAITF(2, 9); KSTEP(2); WAITF(3, 8); KSTEP(3);
    WAITF(4, 7); KSTEP(4); WAITF(5, 6); KSTEP(5); WAITF(6, 5); KSTEP(6); WAITF(7, 4); KSTEP(7);
    asm volatile("s_waitcnt vmcnt(0)" : "+v"(pt[0]), "+v"(pt[1]), "+v"(pt[2]), "+v"(pt[3]) :: "memory");
    af[8] = __builtin_bit_cast(bf16x8, (u32x4){pt[0], pt[1], pt[2], pt[3]});
    KSTEP(8);
#undef KSTEP
#undef WAITF
    ST();
    lds_ptr ex = (lds_ptr)smem + rb4 * 5 * 1024 + lane * 16;
    if (kh == 0) { *(lds_f4)(ex + 2 * 1024) = acc[2]; *(lds_f4)(ex + 3 * 1024) = acc[3]; }
    else { *(lds_f4)(ex) = acc[0]; *(lds_f4)(ex + 1024) = acc[1]; *(lds_f4)(ex + 4 * 1024) = acc[4]; }
    asm volatile("s_waitcnt lgkmcnt(0)\n\ts_barrier" ::: "memory");
    unsigned pk[3][2];
    {
        const f32x4 o0 = (kh ? acc[2] : acc[0]) + *(lds_f4)(ex + (2 * kh) * 1024), o1 = (kh ? acc[3] : acc[1]) + *(lds_f4)(ex + (2 * kh + 1) * 1024);
        const f32x4 o2 = acc[4] + *(lds_f4)(ex + 4 * 1024);
        pk[0][0] = pack2(o0[0] + bf_lo(xr[0][0]), o0[1] + bf_hi(xr[0][0])); pk[0][1] = pack2(o0[2] + bf_lo(xr[0][1]), o0[3] + bf_hi(xr[0][1]));
        pk[1][0] = pack2(o1[0] + bf_lo(xr[1][0]), o1[1] + bf_hi(xr[1][0])); pk[1][1] = pack2(o1[2] + bf_lo(xr[1][1]), o1[3] + bf_hi(xr[1][1]));
        pk[2][0] = pack2(o2[0] + bf_lo(xr[2][0]), o2[1] + bf_hi(xr[2][0])); pk[2][1] = pack2(o2[2] + bf_lo(xr[2][1]), o2[3] + bf_hi(xr[2][1]));
    }
    {
        auto s0 = __builtin_amdgcn_permlane16_swap(pk[0][0], pk[1][0], false, false), s1 = __builtin_amdgcn_permlane16_swap(pk[0][1], pk[1][1], false, false);
        auto t0 = __builtin_amdgcn_permlane16_swap(pk[2][0], pk[2][0], false, false), t1 = __builtin_amdgcn_permlane16_swap(pk[2][1], pk[2][1], false, false);
        const u32x4 d = {s0[0], s1[0], s0[1], s1[1]}, e = {t0[0], t1[0], t0[1], t1[1]};
        const int n0 = 72 * jt + 32 * kh + 16 * (g & 1) + 8 * (g >> 1), n1 = 72 * jt + 64;
        if (last) {
            typedef __attribute__((address_space(1))) u32x4* g_u4;
            unsigned short* orow = out + (size_t)(rb * 16 + m) * 576;
            *(g_u4)(orow + n0) = d;
            if (kh == 0 && g == 0) *(g_u4)(orow + n1) = e;
        } else {
            char* xo = ws + OFF_XF + (xv + 1) * XF_BYTES + (size_t)rb * 18 * 1024 + m * 16;
            gst16c(xo + (n0 >> 5) * 1024 + ((n0 >> 3) & 3) * 256, d);
            if (kh == 0 && g == 0) gst16c(xo + (n1 >> 5) * 1024 + ((n1 >> 3) & 3) * 256, e);
        }
    }
    if (!last) {
        const bool v2 = kh == 0 && g < 2;
        float v[12];
#pragma unroll
        for (int c = 0; c < 3; ++c) { v[4 * c] = bf_lo(pk[c][0]); v[4 * c + 1] = bf_hi(pk[c][0]); v[4 * c + 2] = bf_lo(pk[c][1]); v[4 * c + 3] = bf_hi(pk[c][1]); }
        float s = ((v[0] + v[1]) + (v[2] + v[3])) + ((v[4] + v[5]) + (v[6] + v[7])) + (v2 ? (v[8] + v[9]) + (v[10] + v[11]) : 0.f);
        s = rowsum4(s);
        const float mean = s * (kh ? (1.f / 32.f) : (1.f / 40.f));
        float q = 0.f, q2 = 0.f;
#pragma unroll
        for (int r = 0; r < 8; ++r) { const float dd = v[r] - mean; q += dd * dd; }
#pragma unroll
        for (int r = 8; r < 12; ++r) { const float dd = v[r] - mean; q2 += dd * dd; }
        q += v2 ? q2 : 0.f;
        q = rowsum4(q);
        if (g == 0) gst8c(ws + OFF_ST + (xv + 1) * ST_BYTES + (size_t)(rb * 16 + m) * 128 + jt * 16 + kh * 8, (f32x2){s, q});
        ST();
        const int line = L * C_LAY + (mode ? C_WB : C_WA) + (panel >> 1);
        if (FUSED) { PF(pf, 2); PF(pf, 3); signal_cnt<4>(ws, line); ST(); PF(pf, 4); PF(pf, 5); PF(pf, 6); PFQ_LAST(pf); }
        else signal_cnt<0>(ws, line);
    }
    ST();
    STAMP_OUT(L * 5 + (mode ? 4 : 1));
}

__device__ __forceinline__ float half_max(float v) {
    auto r = __builtin_amdgcn_permlane32_swap(__float_as_uint(v), __float_as_uint(v), false, false);
    return fmaxf(__uint_as_float(r[0]), __uint_as_float(r[1]));
}
#define STR_(x) #x
#define STR(x) STR_(x)
#define AEXP(r) "v_exp_f32 v" STR(r) ", v" STR(r) "\n\t"
#define ACVT(d, r0, r1) "v_cvt_pk_bf16_f32 v" STR(d) ", v" STR(r0) ", v" STR(r1) "\n\t"
#define AE4(r0, r1, r2, r3, d0, d1) AEXP(r0) AEXP(r1) AEXP(r2) AEXP(r3) ACVT(d0, r0, r1) ACVT(d1, r2, r3)
#define ATILE(KO, K2O, V1O, V0O, XK, X0, X1, XV) \
    "v_mfma_f32_32x32x16_bf16 v[48:63], v[80:83], v[72:75], 0\n\t" XK \
    "ds_read_b128 v[80:83], v104 offset:" STR(KO) "\n\t" \
    AE4(32, 33, 34, 35, 96, 97) \
    "v_mfma_f32_32x32x16_bf16 v[16:31], v[92:95], v[100:103], v[16:31]\n\t" \
    "ds_read_b128 v[92:95], v106 offset:" STR(V1O) "\n\t" X0 \
    AE4(36, 37, 38, 39, 98, 99) \
    "v_mfma_f32_32x32x16_bf16 v[48:63], v[84:87], v[76:79], v[48:63]\n\t" \
    "ds_read_b32 v84, v105 offset:" STR(K2O) "\n\t" \
    AE4(40, 41, 42, 43, 100, 101) \
    "s_waitcnt lgkmcnt(3)\n\t" \
    "v_mfma_f32_32x32x16_bf16 v[0:15], v[88:91], v[96:99], v[0:15]\n\t" \
    AE4(44, 45, 46, 47, 102, 103) \
    "s_waitcnt lgkmcnt(1)\n\t" \
    "v_mfma_f32_32x32x16_bf16 v[32:47], v[80:83], v[64:67], 0\n\t" \
    AE4(48, 49, 50, 51, 96, 97) \
    "v_mfma_f32_32x32x16_bf16 v[0:15], v[92:95], v[100:103], v[0:15]\n\t" X1 \
    AE4(52, 53, 54, 55, 98, 99) \
    "s_waitcnt lgkmcnt(0)\n\t" \
    "v_mfma_f32_32x32x16_bf16 v[32:47], v[84:87], v[68:71], v[32:47]\n\t" \
    AE4(56, 57, 58, 59, 100, 101) \
    "v_mfma_f32_32x32x16_bf16 v[16:31], v[88:91], v[96:99], v[16:31]\n\t" XV \
    "ds_read_b128 v[88:91], v106 offset:" STR(V0O) "\n\t" \
    AE4(60, 61, 62, 63, 102, 103)
#define ACHUNK \
    "s_mov_b32 m0, s22\n\ts_add_u32 s28, s20, 0x2000\n\ts_addc_u32 s29, s21, 0\n\tglobal_load_lds_dwordx4 v107, s[20:21] sc1\n\t" \
    "s_add_u32 s27, s22, 0x2000\n\ts_mov_b32 m0, s27\n\ts_cmp_lt_u32 %[w], 3\n\tglobal_load_lds_dwordx4 v107, s[28:29] sc1\n\t" \
    "s_cbranch_scc0 7f\n\t" \
    "s_add_u32 s27, s22, 0x4000\n\ts_mov_b32 m0, s27\n\ts_add_u32 s28, s20, 0x4000\n\ts_addc_u32 s29, s21, 0\n\tglobal_load_lds_dwordx4 v107, s[28:29] sc1\n\t" \
    "7:\n\t" \
    "s_add_u32 s20, s20, 0x4c00\n\ts_addc_u32 s21, s21, 0\n\t"
#define AWPF \
    "s_mov_b32 m0, s25\n\ts_add_u32 s25, s25, 0x4000\n\tglobal_load_lds_dwordx4 v107, s[30:31]\n\tglobal_load_lds_dwordx4 v107, s[30:31] offset:1024\n\t" \
    "s_add_u32 s30, s30, 0x4000\n\ts_addc_u32 s31, s31, 0\n\t"
#define AKADV "v_add_u32 v104, s23, v104\n\tv_add_u32 v105, s23, v105\n\t"
#define AVADV "v_add_u32 v106, s23, v106\n\t"

template <bool FUSED>
__device__ __forceinline__ void attn_phase(char* ws, char* smem, int L, int w, int lane) {
    LAUNDER();
    const int r32 = lane & 31, hh = lane >> 5, kh = w >> 2;
    const int xcd = bid & 7, idx = bid >> 3;
    const int head = 4 * xcd + (idx >> 3), qb = idx & 7;
    const int qA = qb * 256 + (w & 3) * 64 + r32;
    char* lay = ws + OFF_LAY + L * LAY_BYTES;
    const unsigned lds0 = lds_off(smem) + w * 1024;
    STAMP_DECL; ST();
    wait_cnt(ws, L * C_LAY + C_QB + (head >> 1), 16);
    ST();
    bf16x8 q1a, q1b; unsigned q2wa, q2wb;
    {
        const char* qp = lay + LO_Q1 + ((size_t)head * SEQ + qA) * 32 + hh * 16;
        const char* q2p = lay + LO_Q2 + ((size_t)head * SEQ + qA) * 4;
        gld16c<0>(q1a, qp); gld16c<1024>(q1b, qp); gld4c<0>(q2wa, q2p); gld4c<128>(q2wb, q2p);
    }
    const char* srcw = lay + LO_IMG + (size_t)head * IMG_HEAD + w * 1024;
    {
        const char* src = srcw + lane * 16;
#pragma unroll
        for (int k = 0; k < 4; ++k) glds1_sc1(src + k * 8192, lds0 + k * 8192);
        glds1_sc1(src + (w < 6 ? 4 : 3) * 8192, lds0 + (w < 6 ? 4 : 3) * 8192);
    }
    lds_cptr kp1 = (lds_cptr)smem + lane * 16;
    lds_cptr kp2 = (lds_cptr)smem + 8192 + r32 * 4;
    const int dmap = r32 < 19 ? r32 : 19;
    lds_cptr vp = (lds_cptr)smem + 9216 + hh * 320 + dmap * 16;
    asm volatile("s_waitcnt vmcnt(2)\n\ts_barrier" : "+v"(q1a), "+v"(q1b), "+v"(q2wa), "+v"(q2wb) :: "memory");
    ST();
#define LDK1(t) (*(lds_f8)(kp1 + (t) * 1024))
#define LDK2(t) (__builtin_bit_cast(bf16x8, (u32x4){*(lds_vu32)(kp2 + (t) * 128), 0x3f80u, 0u, 0u}))
#define LDV(u) (*(lds_f8)(vp + (u) * 640))
    const f32x16 z16 = {};
    f32x16 s0, s1, oa = {}, ob = {};
    bf16x8 k1 = LDK1(0), k2 = LDK2(0), v0, v1, q2a, q2b;
    u32x4 p0, p1 = {0u, 0u, 0u, 0u};
    {
        q2a = __builtin_bit_cast(bf16x8, (u32x4){hh ? 0u : q2wa, 0u, 0u, 0u});
        q2b = __builtin_bit_cast(bf16x8, (u32x4){hh ? 0u : q2wb, 0u, 0u, 0u});
        f32x16 sa = MFMA32(k1, q1a, z16), sb = MFMA32(k1, q1b, z16); sa = MFMA32(k2, q2a, sa); sb = MFMA32(k2, q2b, sb);
        kp1 += kh * 4096; kp2 += kh * 512; vp += kh * 5120;
        k1 = LDK1(0); k2 = LDK2(0); v0 = LDV(0); v1 = v0;
        float ma = fmaxf(sa[0], sa[1]), mb = fmaxf(sb[0], sb[1]);
#pragma unroll
        for (int i = 2; i < 16; ++i) { ma = fmaxf(ma, sa[i]); mb = fmaxf(mb, sb[i]); }
        ma = half_max(ma); mb = half_max(mb);
        q2a = __builtin_bit_cast(bf16x8, (u32x4){hh ? 0u : q2wa, hh ? 0u : (pack2(-ma, 0.f) & 0xffffu), 0u, 0u});
        q2b = __builtin_bit_cast(bf16x8, (u32x4){hh ? 0u : q2wb, hh ? 0u : (pack2(-mb, 0.f) & 0xffffu), 0u, 0u});
    }
    s0 = MFMA32(k1, q1a, z16); s0 = MFMA32(k2, q2a, s0);
    ST();
    {
        unsigned a1 = (unsigned)(size_t)kp1, a2 = (unsigned)(size_t)kp2, a3 = (unsigned)(size_t)vp;
        const unsigned loff = lane * 16;
        const char* dsrc = srcw + 2 * IMG_CHUNK; const unsigned dbase = lds0;
        const int jt = (bid >> 3) & 7;
        const char* wsrc = ws + OFF_WOB + (size_t)jt * WO_TILE + w * 2048; const unsigned wdst = lds_off(smem) + LDS_WO + w * 2048;
        asm volatile(
            "s_mov_b32 s26, m0\n\t"
            "s_mov_b64 s[20:21], %[dsrc]\n\t"
            "s_add_u32 s22, %[dbase], 0x9800\n\t"
            "s_mov_b64 s[30:31], %[wsrc]\n\t"
            "s_mov_b32 s25, %[wdst]\n\t"
            "s_mov_b32 s24, 0\n\t"
            "s_mov_b32 s33, 0\n\t"
            "s_mov_b32 s34, 0x4c00\n\t"
            "s_waitcnt lgkmcnt(0)\n\t"
            "s_nop 7\n\t"
            "s_nop 3\n\t"
            "2:\n\t"
            "s_setprio 2\n\t"
            ATILE(1024, 128, 640, 1280, "", "", "", "")
            "s_setprio 1\n\t"
            "s_cmp_eq_u32 s33, 2\n\t"
            "s_cselect_b32 s23, 0xffff6800, s34\n\t"
            "s_sub_u32 s35, s24, 1\n\t"
            "s_cmp_lt_u32 s35, %[npf]\n\t"
            "s_cbranch_scc0 3f\n\t"
            AWPF
            "3:\n\t"
            ATILE(2048, 256, 1920, 2560, "", "", "", "")
            "s_setprio 0\n\t"
            ATILE(3072, 384, 3200, 3840, "", "", "", "")
            "s_waitcnt vmcnt(2)\n\t"
            "s_cmp_lt_u32 s35, %[npf]\n\t"
            "s_cbranch_scc1 4f\n\t"
            "s_waitcnt vmcnt(0)\n\t"
            "4:\n\t"
            "s_barrier\n\t"
            "s_cmp_lt_u32 s24, 6\n\t"
            "s_cbranch_scc0 5f\n\t"
            ACHUNK
            "s_cmp_eq_u32 s33, 0\n\t"
            "s_cselect_b32 s27, 0xffff6800, s34\n\t"
            "s_add_u32 s22, s22, s27\n\t"
            "5:\n\t"
            "s_setprio 3\n\t"
            ATILE(0, 0, 4480, 0, AKADV, "", "", AVADV)
            "s_add_u32 s24, s24, 1\n\t"
            "s_add_u32 s33, s33, 1\n\t"
            "s_cmp_eq_u32 s33, 3\n\t"
            "s_cselect_b32 s33, 0, s33\n\t"
            "s_cmp_lt_u32 s24, 8\n\t"
            "s_cbranch_scc1 2b\n\t"
            "s_waitcnt lgkmcnt(0)\n\t"
            "v_mfma_f32_32x32x16_bf16 v[16:31], v[92:95], v[100:103], v[16:31]\n\t"
            "s_mov_b32 m0, s26\n\t"
            "s_setprio 0\n\t"
            "s_nop 7\n\t"
            "s_nop 3\n\t"
            : "+{v[0:15]}"(oa), "+{v[16:31]}"(ob), "+{v[32:47]}"(s0), "={v[48:63]}"(s1), "+{v[80:83]}"(k1), "+{v[84:87]}"(k2), "+{v[88:91]}"(v0), "+{v[92:95]}"(v1),
              "={v[96:99]}"(p0), "+{v[100:103]}"(p1), "+{v104}"(a1), "+{v105}"(a2), "+{v106}"(a3)
            : "{v[64:67]}"(q1a), "{v[68:71]}"(q2a), "{v[72:75]}"(q1b), "{v[76:79]}"(q2b), "{v107}"(loff), [dsrc] "s"(dsrc), [dbase] "s"(dbase),
              [wsrc] "s"(wsrc), [wdst] "s"(wdst), [w] "s"(w), [npf] "s"(FUSED ? (w < 5 ? 6 : 5) : 0)
            : "s20", "s21", "s22", "s23", "s24", "s25", "s26", "s27", "s28", "s29", "s30", "s31", "s33", "s34", "s35", "scc", "memory");
    }
    ST();
    lds_ptr ex = (lds_ptr)smem + (w < 6 ? w * 3072 : 2 * (int)IMG_CHUNK + 12288 + (w - 6) * 3072) + lane * 16;
    const int wo_ = w ^ 4;
    lds_ptr ey = (lds_ptr)smem + (wo_ < 6 ? wo_ * 3072 : 2 * (int)IMG_CHUNK + 12288 + (wo_ - 6) * 3072) + lane * 16;
    f32x16 o;
    {
        const f32x16 snd = kh ? oa : ob; o = kh ? ob : oa;
        *(lds_f4)(ex) = (f32x4){snd[0], snd[1], snd[2], snd[3]}; *(lds_f4)(ex + 1024) = (f32x4){snd[4], snd[5], snd[6], snd[7]};
        *(lds_f4)(ex + 2048) = (f32x4){snd[8], snd[9], snd[10], snd[11]};
    }
    asm volatile("s_waitcnt lgkmcnt(0)\n\ts_barrier" ::: "memory");
    {
        const f32x4 a = *(lds_f4)(ey), b = *(lds_f4)(ey + 1024), c = *(lds_f4)(ey + 2048);
#pragma unroll
        for (int i = 0; i < 4; ++i) { o[i] += a[i]; o[4 + i] += b[i]; o[8 + i] += c[i]; }
    }
    const int q = qA + 32 * kh;
    bool bad = !(fabsf(o[10]) < 1.0e30f);
#pragma unroll
    for (int i = 0; i < 10; ++i) bad = bad || !(fabsf(o[i]) < 1.0e38f);
    if (__any(bad)) {
        o = (f32x16){};
        float mrun = -INFINITY;
        const bf16x8 q1 = kh ? q1b : q1a;
        const bf16x8 q2 = __builtin_bit_cast(bf16x8, (u32x4){hh ? 0u : (kh ? q2wb : q2wa), 0u, 0u, 0u});
        const char* img = lay + LO_IMG + (size_t)head * IMG_HEAD;
#pragma unroll 1
        for (int t = 0; t < 64; ++t) {
            const char* cb = img + (t >> 3) * IMG_CHUNK; const int tt = t & 7;
            const char* vb = cb + 9216 + tt * 1280 + hh * 320 + dmap * 16;
            bf16x8 fk, fv0, fv1; unsigned fk2;
            gld16c<0>(fk, cb + tt * 1024 + lane * 16); gld4c<0>(fk2, cb + 8192 + tt * 128 + r32 * 4); gld16c<0>(fv0, vb); gld16c<640>(fv1, vb);
            asm volatile("s_waitcnt vmcnt(0)" : "+v"(fk), "+v"(fk2), "+v"(fv0), "+v"(fv1) :: "memory");
            f32x16 s = MFMA32(fk, q1, z16);
            s = MFMA32(__builtin_bit_cast(bf16x8, (u32x4){fk2, 0u, 0u, 0u}), q2, s);
            float mx = fmaxf(s[0], s[1]);
#pragma unroll
            for (int i = 2; i < 16; ++i) mx = fmaxf(mx, s[i]);
            mx = fmaxf(half_max(mx), mrun);
            const float al = EXP2(mrun - mx);
            mrun = mx;
#pragma unroll
            for (int i = 0; i < 16; ++i) o[i] *= al;
#pragma unroll
            for (int i = 0; i < 4; ++i) {
                p0[i] = pack2(EXP2(s[2 * i] - mx), EXP2(s[2 * i + 1] - mx));
                p1[i] = pack2(EXP2(s[8 + 2 * i] - mx), EXP2(s[8 + 2 * i + 1] - mx));
            }
            o = MFMA32(fv0, __builtin_bit_cast(bf16x8, p0), o);
            o = MFMA32(fv1, __builtin_bit_cast(bf16x8, p1), o);
        }
    }
    {
        auto r = __builtin_amdgcn_permlane32_swap(__float_as_uint(o[10]), __float_as_uint(o[10]), false, false);
        const float rl = __builtin_amdgcn_rcpf(__uint_as_float(r[0]));
        const int rb = q >> 4, m = q & 15;
        gst16c(lay + LO_O + O_BYTES + ((size_t)(rb * 16 + (head >> 1)) * 64 + (2 * (head & 1) + hh) * 16 + m) * 16,
               (u32x4){pack2(o[0] * rl, o[1] * rl), pack2(o[2] * rl, o[3] * rl), pack2(o[4] * rl, o[5] * rl), pack2(o[6] * rl, o[7] * rl)});
        const unsigned t0 = pack2(o[8] * rl, o[9] * rl);
        const unsigned t1 = __builtin_amdgcn_mov_dpp(t0, 0x55, 0xf, 0xf, true), t2 = __builtin_amdgcn_mov_dpp(t0, 0xAA, 0xf, 0xf, true),
                       t3 = __builtin_amdgcn_mov_dpp(t0, 0xFF, 0xf, 0xf, true);
        if (hh == 0 && (r32 & 3) == 0) gst16c(lay + LO_O + O_BYTES + O_TAIL + ((size_t)(rb * 32 + head) * 16 + m) * 4, (u32x4){t0, t1, t2, t3});
    }
    ST();
    signal_cnt<0>(ws, L * C_LAY + C_AT + qb);
    ST();
    STAMP_OUT(L * 5 + 3);
#undef LDK1
#undef LDK2
#undef LDV
}

__global__ __launch_bounds__(512) void prep_kernel(const unsigned short* __restrict__ x0, const unsigned short* __restrict__ wA,
                                                   const unsigned short* __restrict__ woA, const unsigned short* __restrict__ wB,
                                                   const unsigned short* __restrict__ woB, char* ws) {
    __shared__ __attribute__((aligned(16))) char tbuf[7 * 4096];
    __shared__ __attribute__((aligned(16))) float red[72 * 16];
    const int tid = threadIdx.x, bid = blockIdx.x, w = __builtin_amdgcn_readfirstlane(tid >> 6), l = tid & 63;
    STAMP_DECL; ST();
    const int lin = (bid & 7) * 27 + (bid >> 3);
    const bool jobW = (bid >> 3) < 27;
    const int cmat = lin >= 108, cblk = lin - 108 * cmat;
    u32x2 sv[8]; u32x4 xv[9];
    if (w < 5) {
        if (jobW) {
            const int T = min(tid, 287);
            const char* p = (const char*)((cmat ? wB : wA) + (size_t)(8 * (T >> 2)) * 1728 + 16 * cblk + 4 * (T & 3));
            gld8<0>(sv[0], p); gld8<3456>(sv[1], p); p += 6912; gld8<0>(sv[2], p); gld8<3456>(sv[3], p); p += 6912;
            gld8<0>(sv[4], p); gld8<3456>(sv[5], p); p += 6912; gld8<0>(sv[6], p); gld8<3456>(sv[7], p);
        }
    } else if (w < 7) {
        const int T = bid * 90 + min(tid - 320, 89), jmat = T / 11520, r = T - jmat * 11520, jsg = r / 160, jq = r - jsg * 160;
        const int jt = jq / 20, lc = min(4 * (jq - jt * 20), 68), ss = jsg >> 2, gg = jsg & 3;
        int r0, rb_, rc;
        if (ss >= 16) { r0 = (16 * (ss - 16) + 4 * gg) * 18 + 16; rb_ = 18; rc = 36; }
        else if (jmat == 0) { r0 = 36 * ss + 4 * gg; rb_ = 2; rc = 18; }
        else { r0 = (2 * ss + (gg >> 1)) * 18 + 4 * (gg & 1); rb_ = 2; rc = 8; }
        const char* p = (const char*)((jmat ? woB : woA) + (size_t)r0 * 576 + 72 * jt + lc);
        const char* p2 = p + rb_ * 1152; const char* p4 = p + rc * 1152; const char* p6 = p4 + rb_ * 1152;
        gld8<0>(sv[0], p); gld8<1152>(sv[1], p); gld8<0>(sv[2], p2); gld8<1152>(sv[3], p2);
        gld8<0>(sv[4], p4); gld8<1152>(sv[5], p4); gld8<0>(sv[6], p6); gld8<1152>(sv[7], p6);
    } else {
        const int T = bid * 64 + l, m = T & 15, j = (T >> 4) & 7, rb = T >> 7;
        const char* p = (const char*)(x0 + (size_t)(rb * 16 + m) * 576 + 72 * j);
        gld16<0>(xv[0], p); gld16<16>(xv[1], p); gld16<32>(xv[2], p); gld16<48>(xv[3], p); gld16<64>(xv[4], p);
        gld16<80>(xv[5], p); gld16<96>(xv[6], p); gld16<112>(xv[7], p); gld16<128>(xv[8], p);
    }
    unsigned tw = 0;
    if (w == 7 && (bid >> 3) < 11) asm volatile("global_load_dword %0, %1, off sc1" : "=v"(tw) : "v"(ws + ((size_t)(bid >> 3) * 64 + l) * 65536 + 61440) : "memory");
    int tct, tcb, tc; qkv_col_map(16 * cblk + (l & 15), tct, tcb, tc);
    ST();
    asm volatile("s_waitcnt vmcnt(0)" : "+v"(sv[0]), "+v"(sv[1]), "+v"(sv[2]), "+v"(sv[3]), "+v"(sv[4]), "+v"(sv[5]), "+v"(sv[6]), "+v"(sv[7]) :: "memory");
    asm volatile("" : "+v"(tw));
    asm volatile("" : "+v"(xv[0]), "+v"(xv[1]), "+v"(xv[2]), "+v"(xv[3]), "+v"(xv[4]), "+v"(xv[5]), "+v"(xv[6]), "+v"(xv[7]), "+v"(xv[8]));
    ST();
    u32x4 cc[4];
    if (w < 7) {
        transpose8x4(sv, cc);
        lds_ptr tb = (lds_ptr)tbuf + w * 4096;
#pragma unroll
        for (int i = 0; i < 4; ++i) *(lds_u4)(tb + (l * 4 + i) * 16) = cc[i];
#pragma unroll
        for (int i = 0; i < 4; ++i) cc[i] = *(lds_u4)(tb + (i * 64 + l) * 16);
        if (w < 5) {
#pragma unroll
            for (int i = 0; i < 4; ++i) {
                const int sg = 16 * w + 4 * i + (l >> 4);
                if (sg < 72) red[sg * 16 + (l & 15)] = dot_ones(cc[i][3], dot_ones(cc[i][2], dot_ones(cc[i][1], dot_ones(cc[i][0], 0.f))));
            }
        }
    }
    if (w < 5) {
        if (jobW) {
            char* dst = ws + (cmat ? OFF_WTB : OFF_WTA) + ((size_t)(tct * 18 * 7 + tcb) * 64 + (l >> 4) * 16 + tc) * 16;
#pragma unroll
            for (int i = 0; i < 4; ++i)
                if (4 * w + i < 18) gst16c(dst + (4 * w + i) * 7168, cc[i]);
        }
    } else if (w < 7) {
#pragma unroll
        for (int i = 0; i < 4; ++i) {
            const int loc = (w - 5) * 256 + i * 64 + l;
            const int X = bid * 360 + loc, mat = X / 46080, r = X - mat * 46080, sg = r / 640, n = r - sg * 640, jt = n / 80, lc = n - jt * 80;
            const int ss = sg >> 2, okh = ss < 16 ? (ss >> 3) : (ss - 16), oi = ss < 16 ? (ss & 7) : 8;
            if (loc < 360) gst16c(ws + (mat ? OFF_WOB : OFF_WOA) + (size_t)jt * WO_TILE + (size_t)((oi * 2 + okh) * 5 + (lc >> 4)) * 1024 + ((sg & 3) * 16 + (lc & 15)) * 16,
                                  lc < 72 ? cc[i] : (u32x4){0u, 0u, 0u, 0u});
        }
    } else {
        const int T = bid * 64 + l, m = T & 15, j = (T >> 4) & 7, rb = T >> 7;
        float sa = 0.f, sb = 0.f;
#pragma unroll
        for (int i = 0; i < 9; ++i) {
            const int q = 9 * j + i;
            gst16c(ws + OFF_XF + ((size_t)(rb * 18 + (q >> 2)) * 64 + (q & 3) * 16 + m) * 16, xv[i]);
            const float a = dot_ones(xv[i][3], dot_ones(xv[i][2], dot_ones(xv[i][1], dot_ones(xv[i][0], 0.f))));
            if (i < 4 || i == 8) sa += a; else sb += a;
        }
        const float ma = sa * (1.f / 40.f), mb = sb * (1.f / 32.f);
        float qa = 0.f, qb = 0.f;
#pragma unroll
        for (int i = 0; i < 9; ++i) {
            const bool pa = i < 4 || i == 8; const float mu = pa ? ma : mb; float a = 0.f;
#pragma unroll
            for (int k = 0; k < 4; ++k) { const float d0 = bf_lo(xv[i][k]) - mu, d1 = bf_hi(xv[i][k]) - mu; a += d0 * d0 + d1 * d1; }
            if (pa) qa += a; else qb += a;
        }
        gst16c(ws + OFF_ST + (size_t)(rb * 16 + m) * 128 + j * 16, __builtin_bit_cast(u32x4, (f32x4){sa, qa, sb, qb}));
    }
    if (w == 6) {
        if (l < 36) {
            const int Z = bid * 36 + l, zm = Z / 4608, z2 = Z - zm * 4608, zct = z2 / 288, r = z2 - zct * 288;
            gst16c(ws + (zm ? OFF_WTB : OFF_WTA) + ((size_t)((zct * 18 + (r >> 4)) * 7 + 6) * 64 + ((r >> 2) & 3) * 16 + 12 + (r & 3)) * 16, (u32x4){0u, 0u, 0u, 0u});
        } else if (l < 49) {
            const int Z = bid * 13 + (l - 36);
            if ((size_t)Z * 16 < ZERO_BYTES) gst16c(ws + (size_t)Z * 16, (u32x4){0u, 0u, 0u, 0u});
        } else if (l == 49 && bid < 32) gst16c(ws + ((bid & 16) ? OFF_CSB : OFF_CSA) + ((bid & 15) * 112 + 108) * 4, (u32x4){0u, 0u, 0u, 0u});
    }
    ST();
    asm volatile("s_waitcnt lgkmcnt(0)\n\ts_barrier" ::: "memory");
    if (jobW && w == 0) {
        float a = 0.f;
#pragma unroll
        for (int i = 0; i < 18; ++i) a += red[((l >> 4) * 18 + i) * 16 + (l & 15)];
        a = rowsum4(a);
        if (l < 16) __hip_atomic_store((gu32*)(ws + (cmat ? OFF_CSB : OFF_CSA)) + tct * 112 + tcb * 16 + tc, __float_as_uint(a), RLX_AGENT);
    }
}

template <int MASK>
__global__ __launch_bounds__(512, 2) void mega_kernel(char* ws, const unsigned short* x0, unsigned short* out, int h_lo, int h_hi) {
    __shared__ __attribute__((aligned(16))) char smem[160 * 1024];
    const int lane = threadIdx.x & 63, w = __builtin_amdgcn_readfirstlane(threadIdx.x >> 6);
    constexpr bool FUSED = MASK == 7;
    if (FUSED && w == 7 && (blockIdx.x >> 3) < 4) {
        unsigned long long pc; asm volatile("s_getpc_b64 %0" : "=s"(pc));
        const char* cp = (const char*)(pc & ~127ull) + (blockIdx.x >> 3) * 7168 + min(lane, 55) * 128;
        unsigned t; asm volatile("global_load_dword %0, %1, off\n\ts_waitcnt vmcnt(0)" : "=v"(t) : "v"(cp) : "memory");
    }
#pragma unroll 1
    for (int hl = h_lo; hl < h_hi; ++hl) {
        const int L = hl >> 1;
        if (hl & 1) {
            if (MASK & 1) qkv_phase<1, FUSED>(ws, smem, L, w, lane);
            if (MASK & 2) attn_phase<FUSED>(ws, smem, L, w, lane);
        } else {
            if (MASK & 1) qkv_phase<0, FUSED>(ws, smem, L, w, lane);
        }
        if (MASK & 4) wo_phase<FUSED>(ws, smem, x0, out, L, hl & 1, w, lane);
    }
}

extern "C" void kernel_launch(void* const* d_in, const int* in_sizes, int n_in, void* d_out, int out_size, void* d_ws, size_t ws_size,
                              hipStream_t stream) {
    const unsigned short* x0 = (const unsigned short*)d_in[0];
    const unsigned short* wA = (const unsigned short*)d_in[1];
    const unsigned short* woA = (const unsigned short*)d_in[2];
    const unsigned short* wB = (const unsigned short*)d_in[4];
    const unsigned short* woB = (const unsigned short*)d_in[5];
    char* ws = (char*)d_ws;
    unsigned short* out = (unsigned short*)d_out;
    prep_kernel<<<256, 512, 0, stream>>>(x0, wA, woA, wB, woB, ws);
    mega_kernel<7><<<256, 512, 0, stream>>>(ws, x0, out, 0, 4);
}
```

```cpp
#include <hip/hip_runtime.h>
#include <hip/hip_bf16.h>
#include <stdint.h>

typedef __attribute__((ext_vector_type(8))) short bf16x8;
typedef __attribute__((ext_vector_type(4))) short bf16x4;
typedef __attribute__((ext_vector_type(4))) float f32x4;
typedef __attribute__((ext_vector_type(2))) float f32x2;
typedef __attribute__((ext_vector_type(16))) float f32x16;
typedef __attribute__((ext_vector_type(4))) unsigned u32x4;
typedef __attribute__((ext_vector_type(2))) unsigned u32x2;
typedef __attribute__((ext_vector_type(2))) __bf16 bfx2;
#define LDSAS __attribute__((address_space(3)))
typedef LDSAS char* lds_ptr;
typedef const LDSAS char* lds_cptr;
typedef LDSAS f32x4* lds_f4;
typedef LDSAS f32x2* lds_f2;
typedef LDSAS float* lds_f1;
typedef LDSAS u32x4* lds_u4;
typedef const volatile LDSAS unsigned* lds_vu32;
typedef const LDSAS bf16x8* lds_f8;

constexpr int SEQ = 2048, DM = 576, NH = 32;

constexpr size_t WT_TILE = 126 * 1024;
constexpr size_t WO_TILE = 90 * 1024;
constexpr size_t IMG_CHUNK = 19 * 1024;
constexpr size_t IMG_HEAD = 8 * IMG_CHUNK;
constexpr int KV_HEAD = 19 * 20;
constexpr size_t XF_BYTES = (size_t)SEQ * DM * 2, ST_BYTES = (size_t)SEQ * 128;
constexpr size_t OFF_CNT = 0;
constexpr size_t CNT_BYTES = 2 * 96 * 256 + 256;
constexpr size_t ZERO_BYTES = 49664;
constexpr size_t OFF_KV  = 65536;
constexpr size_t OFF_PQ  = OFF_KV + 2 * 16 * 16 * 3072;
constexpr size_t OFF_WTA = OFF_PQ + 256 * 512 * 32;
constexpr size_t OFF_WTB = OFF_WTA + 16 * WT_TILE + 4096;
constexpr size_t OFF_CSA = OFF_WTB + 16 * WT_TILE + 4096;
constexpr size_t OFF_CSB = OFF_CSA + 8192;
constexpr size_t OFF_WOA = OFF_CSB + 8192;
constexpr size_t OFF_WOB = OFF_WOA + 8 * WO_TILE + 8192;
constexpr size_t OFF_XF  = OFF_WOB + 8 * WO_TILE + 8192;
constexpr size_t OFF_ST  = OFF_XF + 4 * XF_BYTES;
constexpr size_t OFF_LAY = OFF_ST + 4 * ST_BYTES;
constexpr size_t O_BYTES = (size_t)128 * 16 * 1024 + (size_t)128 * 32 * 64;
constexpr size_t O_TAIL = (size_t)128 * 16 * 1024;
constexpr size_t LO_O   = 0;
constexpr size_t LO_Q1  = LO_O + 2 * O_BYTES;
constexpr size_t LO_Q2  = LO_Q1 + (size_t)NH * SEQ * 32;
constexpr size_t LO_IMG = LO_Q2 + (size_t)NH * SEQ * 4;
constexpr size_t LAY_BYTES = LO_IMG + (size_t)NH * IMG_HEAD;
static_assert(ZERO_BYTES <= OFF_KV && CNT_BYTES <= ZERO_BYTES && ZERO_BYTES % 16 == 0, "layout");
constexpr int C_QA = 0, C_OA = 16, C_WA = 32, C_QB = 48, C_AT = 64, C_WB = 80, C_LAY = 96;

constexpr float QSCALE = 0.23570226039551584f * 1.4426950408889634f;

__device__ __forceinline__ unsigned pack2(float lo, float hi) {
    f32x2 v = {lo, hi};
    bfx2 r = __builtin_convertvector(v, bfx2);
    return __builtin_bit_cast(unsigned, r);
}
__device__ __forceinline__ float bf_lo(unsigned u) { return __uint_as_float(u << 16); }
__device__ __forceinline__ float bf_hi(unsigned u) { return __uint_as_float(u & 0xffff0000u); }
__device__ __forceinline__ float phi_f(float t) { return (t > 0.f ? t + 1.f : __expf(t)) + 1e-6f; }
__device__ __forceinline__ unsigned lds_off(const void* p) { return (unsigned)(size_t)(lds_cptr)p; }

#define MFMA16(a, b, c) __builtin_amdgcn_mfma_f32_16x16x32_bf16(a, b, c, 0, 0, 0)
#define MFMA32(a, b, c) __builtin_amdgcn_mfma_f32_32x32x16_bf16(a, b, c, 0, 0, 0)
#define MFMA16K16(a, b, c) __builtin_amdgcn_mfma_f32_16x16x16bf16_1k(a, b, c, 0, 0, 0)
__device__ __forceinline__ float rowsum4(float v) {
    auto a = __builtin_amdgcn_permlane16_swap(__float_as_uint(v), __float_as_uint(v), false, false);
    v = __uint_as_float(a[0]) + __uint_as_float(a[1]);
    auto b = __builtin_amdgcn_permlane32_swap(__float_as_uint(v), __float_as_uint(v), false, false);
    return __uint_as_float(b[0]) + __uint_as_float(b[1]);
}
#define WARM 3
#define PIN(x) asm volatile("" : "+v"(x))
#define LAUNDER() int bid = blockIdx.x; asm volatile("" : "+v"(lane)); asm volatile("" : "+s"(w), "+s"(ws), "+s"(bid), "+s"(L))
#define SBAR() __builtin_amdgcn_sched_barrier(0)
#define EXP2(x) __builtin_amdgcn_exp2f(x)

template <int N>
__device__ __forceinline__ void glds(const void* g, unsigned lds) {
    unsigned keep;
    if constexpr (N == 4)
        asm volatile("s_mov_b32 %0, m0\n\ts_mov_b32 m0, %2\n\ts_nop 0\n\t"
                     "global_load_lds_dwordx4 %1, off\n\tglobal_load_lds_dwordx4 %1, off offset:1024\n\t"
                     "global_load_lds_dwordx4 %1, off offset:2048\n\tglobal_load_lds_dwordx4 %1, off offset:3072\n\t"
                     "s_mov_b32 m0, %0" : "=&s"(keep) : "v"(g), "s"(lds) : "memory");
    else
        asm volatile("s_mov_b32 %0, m0\n\ts_mov_b32 m0, %2\n\ts_nop 0\n\t"
                     "global_load_lds_dwordx4 %1, off\n\tglobal_load_lds_dwordx4 %1, off offset:1024\n\t"
                     "s_mov_b32 m0, %0" : "=&s"(keep) : "v"(g), "s"(lds) : "memory");
}
__device__ __forceinline__ void glds1_sc1(const void* g, unsigned lds) {
    unsigned keep;
    asm volatile("s_mov_b32 %0, m0\n\ts_mov_b32 m0, %2\n\ts_nop 0\n\tglobal_load_lds_dwordx4 %1, off sc1\n\ts_mov_b32 m0, %0"
                 : "=&s"(keep) : "v"(g), "s"(lds) : "memory");
}
template <int OFF, class T> __device__ __forceinline__ void gld16(T& d, const void* p) {
    asm volatile("global_load_dwordx4 %0, %1, off offset:%2" : "=v"(d) : "v"(p), "i"(OFF) : "memory"); }
template <int OFF, class T> __device__ __forceinline__ void gld4(T& d, const void* p) {
    asm volatile("global_load_dword %0, %1, off offset:%2" : "=v"(d) : "v"(p), "i"(OFF) : "memory"); }
template <int OFF, class T> __device__ __forceinline__ void gld16c(T& d, const void* p) {
    asm volatile("global_load_dwordx4 %0, %1, off offset:%2 sc1" : "=v"(d) : "v"(p), "i"(OFF) : "memory"); }
template <int OFF, class T> __device__ __forceinline__ void gld8c(T& d, const void* p) {
    asm volatile("global_load_dwordx2 %0, %1, off offset:%2 sc1" : "=v"(d) : "v"(p), "i"(OFF) : "memory"); }
template <int OFF, class T> __device__ __forceinline__ void gld4c(T& d, const void* p) {
    asm volatile("global_load_dword %0, %1, off offset:%2 sc1" : "=v"(d) : "v"(p), "i"(OFF) : "memory"); }
__device__ __forceinline__ void gst16c(void* p, u32x4 v) { asm volatile("global_store_dwordx4 %0, %1, off sc1\n\ts_nop 1" :: "v"(p), "v"(v) : "memory"); }
__device__ __forceinline__ void gst8c(void* p, f32x2 v) { asm volatile("global_store_dwordx2 %0, %1, off sc1" :: "v"(p), "v"(v) : "memory"); }

#define STAMP_DECL
#define ST()
#define STAMP_OUT(kid)

typedef __attribute__((address_space(1))) unsigned gu32;
#define RLX_AGENT __ATOMIC_RELAXED, __HIP_MEMORY_SCOPE_AGENT
template <int N> __device__ __forceinline__ void signal_cnt(char* ws, int line) {
    asm volatile("s_waitcnt vmcnt(%0) lgkmcnt(0)\n\ts_barrier" :: "i"(N) : "memory");
    if (threadIdx.x == 0) __hip_atomic_fetch_add((gu32*)(ws + OFF_CNT + (size_t)line * 256), 1u, RLX_AGENT);
}
__device__ __forceinline__ void wait_cnt(char* ws, int line, unsigned want) {
    if (threadIdx.x < 64) {
        gu32* p = (gu32*)(ws + OFF_CNT + (size_t)line * 256);
        gu32* tmo = (gu32*)(ws + OFF_CNT + 2 * C_LAY * 256);
        for (unsigned spins = 0;;) {
            if ((unsigned)__builtin_amdgcn_readfirstlane(__hip_atomic_load(p, RLX_AGENT)) >= want) break;
            __builtin_amdgcn_s_sleep(1);
            if (++spins > (1u << 18)) { __hip_atomic_store(tmo, 1u, RLX_AGENT); break; }
            if ((spins & 255u) == 0u && __builtin_amdgcn_readfirstlane(__hip_atomic_load(tmo, RLX_AGENT))) break;
        }
    }
    asm volatile("s_waitcnt vmcnt(0) lgkmcnt(0)\n\ts_barrier" ::: "memory");
}

__device__ __forceinline__ void qkv_col_map(int n, int& ct, int& cb, int& c) {
    const int part = n / 576, rem = n - part * 576, head = rem / 18, d = rem - head * 18;
    ct = head >> 1; const int hb = head & 1;
    if (d < 16) { cb = part * 2 + hb; c = d; } else { cb = 6; c = part * 4 + hb * 2 + (d - 16); }
}
__device__ __forceinline__ float dot_ones(unsigned u, float acc) {
    return __builtin_amdgcn_fdot2_f32_bf16(__builtin_bit_cast(bfx2, u), __builtin_bit_cast(bfx2, 0x3f803f80u), acc, false); }
__device__ __forceinline__ float dot_self(unsigned u, float acc) {
    return __builtin_amdgcn_fdot2_f32_bf16(__builtin_bit_cast(bfx2, u), __builtin_bit_cast(bfx2, u), acc, false); }
__device__ __forceinline__ void transpose8x4(const u32x2 (&v)[8], u32x4 (&c)[4]) {
#pragma unroll
    for (int k = 0; k < 4; ++k) {
        c[0][k] = __builtin_amdgcn_perm(v[2 * k + 1][0], v[2 * k][0], 0x05040100u); c[1][k] = __builtin_amdgcn_perm(v[2 * k + 1][0], v[2 * k][0], 0x07060302u);
        c[2][k] = __builtin_amdgcn_perm(v[2 * k + 1][1], v[2 * k][1], 0x05040100u); c[3][k] = __builtin_amdgcn_perm(v[2 * k + 1][1], v[2 * k][1], 0x07060302u);
    }
}
template <int OFF, class T> __device__ __forceinline__ void gld8(T& d, const void* p) {
    asm volatile("global_load_dwordx2 %0, %1, off offset:%2" : "=v"(d) : "v"(p), "i"(OFF) : "memory"); }

constexpr int LDS_W = 34 * 1024, LDS_WO = 70 * 1024;
struct Pf { const char* src; unsigned dst; };
__device__ __forceinline__ Pf pf_qkv(char* ws, char* smem, int layer, int bid, int w, int lane) {
    const int ct = 8 * (bid & 1) + ((bid >> 3) & 7);
    return Pf{ws + (layer ? OFF_WTB : OFF_WTA) + (size_t)ct * WT_TILE + w * 2048 + lane * 16, lds_off(smem) + LDS_W + w * 2048};
}
__device__ __forceinline__ Pf pf_wo(char* ws, char* smem, int mode, int bid, int w, int lane) {
    const int jt = (bid >> 3) & 7;
    return Pf{ws + (mode ? OFF_WOB : OFF_WOA) + (size_t)jt * WO_TILE + w * 2048 + lane * 16, lds_off(smem) + LDS_WO + w * 2048};
}
#define PF(p, j) glds<2>((p).src + (j) * 16384, (p).dst + (j) * 16384)
#define PF_LAST(p, j, n) do { const int jj_ = w < (n) ? (j) : (j) - 1; PF(p, jj_); } while (0)
#define PFQ_LAST(p) PF_LAST(p, 7, 7)
#define PFO_LAST(p) PF_LAST(p, 5, 5)

__device__ __forceinline__ void lin_kv_part(char* ws, char* smem, int L, int panel, int ct, int w, int lane, const f32x4 (&acc)[7], const float (&rs4)[4], unsigned (&qt)[2]) {
    const int m = lane & 15, g = lane >> 4, tid = w * 64 + lane;
    {
        u32x2 fr[5];
#pragma unroll
        for (int c = 2; c < 7; ++c) {
            float v[4];
#pragma unroll
            for (int r = 0; r < 4; ++r) {
                float x = rs4[r] * acc[c][r];
                if (c < 4) x = phi_f(x);
                else if (c == 6) { const float p = phi_f(x); x = m < 8 ? p : x; x = m == 12 ? 1.f : x; }
                v[r] = x;
            }
            fr[c - 2] = (u32x2){pack2(v[0], v[1]), pack2(v[2], v[3])};
        }
        const bf16x4 k0 = __builtin_bit_cast(bf16x4, fr[0]), k1 = __builtin_bit_cast(bf16x4, fr[1]);
        const bf16x4 v0 = __builtin_bit_cast(bf16x4, fr[2]), v1 = __builtin_bit_cast(bf16x4, fr[3]), T = __builtin_bit_cast(bf16x4, fr[4]);
        const f32x4 z4 = {0.f, 0.f, 0.f, 0.f};
        const f32x4 d1 = MFMA16K16(k0, v0, z4), d2 = MFMA16K16(k1, v1, z4), d3 = MFMA16K16(T, v0, z4), d4 = MFMA16K16(T, v1, z4);
        const f32x4 d5 = MFMA16K16(k0, T, z4), d6 = MFMA16K16(k1, T, z4), d7 = MFMA16K16(T, T, z4);
        lds_ptr red = (lds_ptr)smem + (w ^ 4) * 5120;
        *(lds_f4)(red + (m * 20 + 4 * g) * 4) = d1;
        *(lds_f4)(red + (KV_HEAD + m * 20 + 4 * g) * 4) = d2;
        if (g == 1) {
            *(lds_f2)(red + (m * 20 + 16) * 4) = (f32x2){d3[0], d3[1]};
            *(lds_f2)(red + (KV_HEAD + m * 20 + 16) * 4) = (f32x2){d4[2], d4[3]};
        }
        if (m == 8 || m == 9 || m == 12) {
            const int e = m == 12 ? 18 : 8 + m;
            *(lds_f4)(red + (e * 20 + 4 * g) * 4) = d5;
            if (g == 1) *(lds_f2)(red + (e * 20 + 16) * 4) = (f32x2){d7[0], d7[1]};
        }
        if (m == 10 || m == 11 || m == 12) {
            const int e = m == 12 ? 18 : 6 + m;
            *(lds_f4)(red + (KV_HEAD + e * 20 + 4 * g) * 4) = d6;
            if (g == 1) *(lds_f2)(red + (KV_HEAD + e * 20 + 16) * 4) = (f32x2){d7[2], d7[3]};
        }
        {
            const unsigned o0 = fr[4][0], o1 = fr[4][1];
            const unsigned n0 = __builtin_amdgcn_mov_dpp(o0, 0xB1, 0xf, 0xf, true), n1 = __builtin_amdgcn_mov_dpp(o1, 0xB1, 0xf, 0xf, true);
            if (m == 0 || m == 2)
                *(lds_u4)(red + 4096 + (m >> 1) * 64 + g * 16) =
                    (u32x4){(o0 & 0xffffu) | (n0 << 16), (o0 >> 16) | (n0 & 0xffff0000u), (o1 & 0xffffu) | (n1 << 16), (o1 >> 16) | (n1 & 0xffff0000u)};
        }
        asm volatile("s_waitcnt lgkmcnt(0)\n\ts_barrier" ::: "memory");
        char* kvp = ws + OFF_KV + (size_t)(L * 16 + ct) * 16 * 3072;
        if (tid < 190) {
            f32x4 s4 = *(lds_f4)((lds_ptr)smem + tid * 16);
#pragma unroll
            for (int i = 1; i < 8; ++i) s4 += *(lds_f4)((lds_ptr)smem + i * 5120 + tid * 16);
            gst16c(kvp + panel * 3072 + tid * 16, __builtin_bit_cast(u32x4, s4));
        }
        qt[0] = *(lds_vu32)(red + 4096 + m * 4); qt[1] = *(lds_vu32)(red + 4096 + 64 + m * 4);
    }
}
__device__ __forceinline__ void lin_o_part(char* ws, char* smem, int L, int rb, int ct, int w, int lane, u32x4 (&bq)[2]) {
    const int m = lane & 15, g = lane >> 4, tid = w * 64 + lane, h0 = 2 * ct;
    char* lay = ws + OFF_LAY + L * LAY_BYTES;
    char* kvp = ws + OFF_KV + (size_t)(L * 16 + ct) * 16 * 3072;
    const f32x4 z4 = {0.f, 0.f, 0.f, 0.f};
    {
        {
            f32x4 pa[3], pb[3];
            const char* pp = kvp + w * 6144 + lane * 16;
            gld16c<0>(pa[0], pp); gld16c<1024>(pa[1], pp); gld16c<2048>(pa[2], pp);
            gld16c<0>(pb[0], pp + 3072); gld16c<1024>(pb[1], pp + 3072); gld16c<2048>(pb[2], pp + 3072);
            asm volatile("s_waitcnt vmcnt(0)" : "+v"(pa[0]), "+v"(pa[1]), "+v"(pa[2]), "+v"(pb[0]), "+v"(pb[1]), "+v"(pb[2]), "+v"(bq[0]), "+v"(bq[1]) :: "memory");
#pragma unroll
            for (int i = 0; i < 3; ++i) *(lds_f4)((lds_ptr)smem + w * 3072 + i * 1024 + lane * 16) = pa[i] + pb[i];
            asm volatile("s_waitcnt lgkmcnt(0)\n\ts_barrier" ::: "memory");
            if (tid < 190) {
                f32x4 s4 = *(lds_f4)((lds_ptr)smem + tid * 16);
#pragma unroll
                for (int i = 1; i < 8; ++i) s4 += *(lds_f4)((lds_ptr)smem + i * 3072 + tid * 16);
                *(lds_f4)((lds_ptr)smem + 24576 + tid * 16) = s4;
            }
            asm volatile("s_waitcnt lgkmcnt(0)\n\ts_barrier" ::: "memory");
        }
        f32x4 ka[2], kb[2]; f32x2 ka2[2], kb2[2];
        const int r3 = m & 3; const bool isz = r3 == 2;
        {
            lds_ptr p1 = (lds_ptr)smem + 24576 + m * 80, p2 = (lds_ptr)smem + 24576 + (isz ? 18 : 16 + (r3 & 1)) * 80;
#pragma unroll
            for (int h = 0; h < 2; ++h) {
                ka[h] = *(lds_f4)(p1 + h * KV_HEAD * 4 + 16 * g); ka2[h] = *(lds_f2)(p1 + h * KV_HEAD * 4 + 64);
                kb[h] = *(lds_f4)(p2 + h * KV_HEAD * 4 + 16 * g); kb2[h] = *(lds_f2)(p2 + h * KV_HEAD * 4 + 64);
            }
        }
        const bool use2 = isz || m < 2;
        unsigned om[2][2], ot[2];
#pragma unroll
        for (int h = 0; h < 2; ++h) {
            const u32x4 f1 = {pack2(ka[h][0], ka[h][1]), pack2(ka[h][2], ka[h][3]), g == 0 ? pack2(ka2[h][0], ka2[h][1]) : 0u, 0u};
            u32x4 f2 = {pack2(kb[h][0], kb[h][1]), pack2(kb[h][2], kb[h][3]), g == 0 ? pack2(kb2[h][0], kb2[h][1]) : 0u, 0u};
            if (!use2) f2 = (u32x4){0u, 0u, 0u, 0u};
            const bf16x8 b = __builtin_bit_cast(bf16x8, bq[h]);
            const f32x4 e1 = MFMA16(__builtin_bit_cast(bf16x8, f1), b, z4), e2 = MFMA16(__builtin_bit_cast(bf16x8, f2), b, z4);
            const float r = __builtin_amdgcn_rcpf(e2[2] + 1e-6f);
            om[h][0] = pack2(e1[0] * r, e1[1] * r); om[h][1] = pack2(e1[2] * r, e1[3] * r);
            ot[h] = pack2(e2[0] * r, e2[1] * r);
        }
        gst16c(lay + LO_O + ((size_t)(rb * 16 + ct) * 64 + lane) * 16, (u32x4){om[0][0], om[0][1], om[1][0], om[1][1]});
        {
            const unsigned a0 = __builtin_amdgcn_mov_dpp(ot[0], 0x00, 0xf, 0xf, true), a1 = __builtin_amdgcn_mov_dpp(ot[0], 0x55, 0xf, 0xf, true),
                           a2 = __builtin_amdgcn_mov_dpp(ot[0], 0xAA, 0xf, 0xf, true), a3 = __builtin_amdgcn_mov_dpp(ot[0], 0xFF, 0xf, 0xf, true);
            const unsigned b0 = __builtin_amdgcn_mov_dpp(ot[1], 0x00, 0xf, 0xf, true), b1 = __builtin_amdgcn_mov_dpp(ot[1], 0x55, 0xf, 0xf, true),
                           b2 = __builtin_amdgcn_mov_dpp(ot[1], 0xAA, 0xf, 0xf, true), b3 = __builtin_amdgcn_mov_dpp(ot[1], 0xFF, 0xf, 0xf, true);
            const bool h1 = r3 == 1;
            if (g == 0 && r3 < 2)
                gst16c(lay + LO_O + O_TAIL + ((size_t)(rb * 32 + h0 + r3) * 16 + (m & 12)) * 4, (u32x4){h1 ? b0 : a0, h1 ? b1 : a1, h1 ? b2 : a2, h1 ? b3 : a3});
        }
    }
}

template <int LAYER, bool FUSED>
__device__ __forceinline__ void qkv_phase(char* ws, char* smem, int L, int w, int lane) {
    LAUNDER();
    int m = lane & 15, g = lane >> 4; const int rp = w & 3, kh = w >> 2;
    const int xcd = bid & 7, idx = bid >> 3;
    const int panel = 4 * (xcd >> 1) + (idx >> 3), ct = 8 * (xcd & 1) + (idx & 7);
    const int rb = panel * 8 + 2 * rp + kh, rbo = panel * 8 + 2 * rp + 1 - kh;
    const int xv = 2 * L + LAYER;
    char* lay = ws + OFF_LAY + L * LAY_BYTES;
    STAMP_DECL; ST();
    f32x4 st[2], cs4[4]; float cs1[5];
    {
        const float* cs = (const float*)(ws + (LAYER ? OFF_CSB : OFF_CSA)) + ct * 112;
        const float* c4 = cs + 4 * g; const float* c1 = cs + m;
        if constexpr (LAYER == 0) {
            gld16<0>(cs4[0], c4); gld16<64>(cs4[1], c4);
            gld4<128>(cs1[0], c1); gld4<192>(cs1[1], c1); gld4<256>(cs1[2], c1); gld4<320>(cs1[3], c1); gld4<384>(cs1[4], c1);
        } else {
            gld16<0>(cs4[0], c4); gld16<64>(cs4[1], c4); gld16<128>(cs4[2], c4); gld16<192>(cs4[3], c4);
            gld4<256>(cs1[0], c1); gld4<320>(cs1[1], c1); gld4<384>(cs1[2], c1);
        }
    }
    if (!FUSED || xv == 0) {
        const Pf pf = pf_qkv(ws, smem, LAYER, bid, w, lane);
        PF(pf, 0); PF(pf, 1); PF(pf, 2); PF(pf, 3); PF(pf, 4); PF(pf, 5); PF(pf, 6); PFQ_LAST(pf);
    }
    if (xv) wait_cnt(ws, LAYER ? L * C_LAY + C_WA + panel : C_WB + panel, 16u);
    else asm volatile("s_waitcnt vmcnt(0)\n\ts_barrier" ::: "memory");
    ST();
    bf16x8 a[2][9];
    const char* xa0 = ws + OFF_XF + xv * XF_BYTES + (size_t)(rb * 18 + kh) * 1024 + lane * 16;
    const char* xa1 = ws + OFF_XF + xv * XF_BYTES + (size_t)(rbo * 18 + kh) * 1024 + lane * 16;
#define LDA(j) do { gld16c<((j) & 1) * 2048>(a[0][j], xa0 + ((j) >> 1) * 4096); gld16c<((j) & 1) * 2048>(a[1][j], xa1 + ((j) >> 1) * 4096); } while (0)
    {
        const char* sp = ws + OFF_ST + xv * ST_BYTES + (size_t)(rb * 16 + m) * 128 + g * 32;
        gld16c<0>(st[0], sp); gld16c<16>(st[1], sp);
    }
    LDA(0); LDA(1); LDA(2);
    const lds_cptr bl = (lds_cptr)smem + LDS_W + kh * 7 * 1024 + lane * 16;
    bf16x8 bb[2][7];
#pragma unroll
    for (int c = 0; c < 7; ++c) bb[0][c] = *(lds_f8)(bl + c * 1024);
    constexpr int NTR = LAYER == 0 ? 2 : 4;
    f32x4 acc[2][7];
#pragma unroll
    for (int c = 0; c < 7; ++c) { acc[0][c] = (f32x4){0.f, 0.f, 0.f, 0.f}; acc[1][c] = (f32x4){0.f, 0.f, 0.f, 0.f}; }
    float nmu, rs, nmu4[4], rs4[4];
#define MM(j) do { _Pragma("unroll") for (int c = 0; c < 7; ++c) { \
            if ((j) < 8 && !(LAYER == 0 && c < 2 && (j) >= 3)) bb[((j) + 1) & 1][c] = *(lds_f8)(bl + (((j) + 1) * 14 + c) * 1024); \
            SBAR(); \
            if (LAYER == 0 && c < 2 && (j) >= 4) {} else \
            if (c < NTR) { acc[1][c] = MFMA16(bb[(j) & 1][c], a[1][j], acc[1][c]); acc[0][c] = MFMA16(bb[(j) & 1][c], a[0][j], acc[0][c]); } \
            else         { acc[1][c] = MFMA16(a[1][j], bb[(j) & 1][c], acc[1][c]); acc[0][c] = MFMA16(a[0][j], bb[(j) & 1][c], acc[0][c]); } \
            SBAR(); \
        } } while (0)
#define WAITA(j, N) asm volatile("s_waitcnt vmcnt(" #N ")" : "+v"(a[0][j]), "+v"(a[1][j]) :: "memory")
    SBAR();
    WAITA(0, 4); ST(); LDA(3); LDA(4); MM(0);
    WAITA(1, 6); LDA(5); LDA(6); MM(1);
    {
        if constexpr (LAYER == 0) asm volatile("" : "+v"(st[0]), "+v"(st[1]), "+v"(cs4[0]), "+v"(cs4[1]), "+v"(cs1[0]), "+v"(cs1[1]), "+v"(cs1[2]), "+v"(cs1[3]), "+v"(cs1[4]));
        else asm volatile("" : "+v"(st[0]), "+v"(st[1]), "+v"(cs4[0]), "+v"(cs4[1]), "+v"(cs4[2]), "+v"(cs4[3]), "+v"(cs1[0]), "+v"(cs1[1]), "+v"(cs1[2]));
        float s = rowsum4(st[0][0] + st[0][2] + st[1][0] + st[1][2]);
        const float mean = s * (1.f / 576.f);
        const float d0 = st[0][0] * (1.f / 40.f) - mean, d1 = st[0][2] * (1.f / 32.f) - mean;
        const float d2 = st[1][0] * (1.f / 40.f) - mean, d3 = st[1][2] * (1.f / 32.f) - mean;
        const float q = rowsum4(st[0][1] + st[0][3] + st[1][1] + st[1][3] + 40.f * (d0 * d0 + d2 * d2) + 32.f * (d1 * d1 + d3 * d3));
        nmu = -mean; rs = __builtin_amdgcn_rsqf(q * (1.f / 576.f) + 1e-5f);
#pragma unroll
        for (int r = 0; r < 4; ++r) { nmu4[r] = __shfl(nmu, 4 * g + r); rs4[r] = __shfl(rs, 4 * g + r); }
#pragma unroll
        for (int c = 0; c < NTR; ++c) cs4[c] *= nmu;
        SBAR();
    }
    WAITA(2, 8); LDA(7); LDA(8); MM(2);
    WAITA(3, 10); MM(3);
    WAITA(4, 8); MM(4);
    asm volatile("s_barrier" ::: "memory");
    WAITA(5, 6); MM(5);
    WAITA(6, 4); MM(6);
    WAITA(7, 2); MM(7);
    WAITA(8, 0); MM(8);
    ST();
#undef MM
#undef WAITA
#undef LDA
    asm volatile("" : "+v"(lane)); m = lane & 15; g = lane >> 4;
    constexpr int C0 = LAYER == 0 ? 2 : 0, SLOT = (7 - C0) * 1024;
    {
        lds_ptr ex = (lds_ptr)smem + w * SLOT + lane * 16;
#pragma unroll
        for (int c = C0; c < 7; ++c) *(lds_f4)(ex + (c - C0) * 1024) = acc[1][c];
        asm volatile("s_waitcnt lgkmcnt(0)\n\ts_barrier" ::: "memory");
        lds_ptr ey = (lds_ptr)smem + (w ^ 4) * SLOT + lane * 16;
#pragma unroll
        for (int c = C0; c < 7; ++c) {
            acc[0][c] += *(lds_f4)(ey + (c - C0) * 1024);
            if (c < NTR) acc[0][c] += cs4[c];
            else { const float cv = cs1[c - NTR]; acc[0][c] += (f32x4){nmu4[0] * cv, nmu4[1] * cv, nmu4[2] * cv, nmu4[3] * cv}; }
        }
    }
    ST();
    const int h0 = 2 * ct;
    if constexpr (LAYER == 0) {
        unsigned qt[2];
        lin_kv_part(ws, smem, L, panel, ct, w, lane, acc[0], rs4, qt);
        ST();
        const lds_cptr bl2 = (lds_cptr)smem + LDS_W + kh * 7 * 1024 + lane * 16;
#pragma unroll
        for (int j = 4; j < 9; ++j) {
            const bf16x8 b0 = *(lds_f8)(bl2 + (j * 14) * 1024), b1 = *(lds_f8)(bl2 + (j * 14 + 1) * 1024);
            acc[1][0] = MFMA16(b0, a[1][j], acc[1][0]); acc[0][0] = MFMA16(b0, a[0][j], acc[0][0]);
            acc[1][1] = MFMA16(b1, a[1][j], acc[1][1]); acc[0][1] = MFMA16(b1, a[0][j], acc[0][1]);
            if (j == 5) signal_cnt<0>(ws, L * C_LAY + C_QA + ct);
        }
        lds_ptr e2 = (lds_ptr)smem + 40960 + w * 2048 + lane * 16;
        *(lds_f4)(e2) = acc[1][0]; *(lds_f4)(e2 + 1024) = acc[1][1];
        asm volatile("s_waitcnt lgkmcnt(0)\n\ts_barrier" ::: "memory");
        const Pf pf = pf_wo(ws, smem, 0, bid, w, lane);
        if (FUSED) { PF(pf, 0); PF(pf, 1); PF(pf, 2); }
        u32x4 bq[2];
        lds_ptr e3 = (lds_ptr)smem + 40960 + (w ^ 4) * 2048 + lane * 16;
#pragma unroll
        for (int c = 0; c < 2; ++c) {
            const f32x4 t = acc[0][c] + *(lds_f4)(e3 + c * 1024) + cs4[c];
            bq[c] = (u32x4){pack2(phi_f(rs * t[0]), phi_f(rs * t[1])), pack2(phi_f(rs * t[2]), phi_f(rs * t[3])), qt[c], 0u};
        }
        ST();
        wait_cnt(ws, L * C_LAY + C_QA + ct, 16);
        ST();
        lin_o_part(ws, smem, L, rb, ct, w, lane, bq);
        ST();
        if (FUSED) { PF(pf, 3); PF(pf, 4); signal_cnt<4>(ws, L * C_LAY + C_OA + panel); PFO_LAST(pf); }
        else signal_cnt<0>(ws, L * C_LAY + C_OA + panel);
    } else {
        const int tok = rb * 16 + m;
        char* img = lay + LO_IMG + (size_t)h0 * IMG_HEAD + (size_t)(rb >> 4) * IMG_CHUNK;
        const float rq = rs * QSCALE;
#pragma unroll
        for (int p = 0; p < 2; ++p) {
            const float sc = p == 0 ? rq : rs;
            const f32x4 A = acc[0][2 * p], B = acc[0][2 * p + 1];
            auto s0 = __builtin_amdgcn_permlane16_swap(pack2(sc * A[0], sc * A[1]), pack2(sc * B[0], sc * B[1]), false, false);
            auto s1 = __builtin_amdgcn_permlane16_swap(pack2(sc * A[2], sc * A[3]), pack2(sc * B[2], sc * B[3]), false, false);
            const u32x4 d = {s0[0], s1[0], s0[1], s1[1]};
            if (p == 0) gst16c(lay + LO_Q1 + ((size_t)(h0 + (g & 1)) * SEQ + tok) * 32 + (g >> 1) * 16, d);
            else gst16c(img + (g & 1) * IMG_HEAD + ((tok & 255) >> 5) * 1024 + (g >> 1) * 512 + (tok & 31) * 16, d);
        }
        {
            const f32x4 A = acc[0][4], B = acc[0][5];
            auto s0 = __builtin_amdgcn_permlane32_swap(pack2(rs4[0] * A[0], rs4[1] * A[1]), pack2(rs4[0] * B[0], rs4[1] * B[1]), false, false);
            auto s1 = __builtin_amdgcn_permlane32_swap(pack2(rs4[2] * A[2], rs4[3] * A[3]), pack2(rs4[2] * B[2], rs4[3] * B[3]), false, false);
            gst16c(img + (g >> 1) * IMG_HEAD + 9216 + ((rb & 15) * 2 + (g & 1)) * 320 + m * 16, (u32x4){s0[0], s1[0], s0[1], s1[1]});
        }
        {
            const float qs = m < 4 ? QSCALE : 1.f;
            const f32x4 A = acc[0][6];
            const unsigned o0 = pack2(qs * rs4[0] * A[0], qs * rs4[1] * A[1]), o1 = pack2(qs * rs4[2] * A[2], qs * rs4[3] * A[3]);
            const unsigned n0 = __builtin_amdgcn_mov_dpp(o0, 0xB1, 0xf, 0xf, true), n1 = __builtin_amdgcn_mov_dpp(o1, 0xB1, 0xf, 0xf, true);
            auto u0 = __builtin_amdgcn_permlane32_swap(o0, o0, false, false); auto u1 = __builtin_amdgcn_permlane32_swap(o1, o1, false, false);
            const int hsel = (m >> 1) & 1, tok0 = rb * 16 + 4 * g;
            u32x4 d; char* p;
            if (m < 8) {
                d = (u32x4){(o0 & 0xffffu) | (n0 << 16), (o0 >> 16) | (n0 & 0xffff0000u), (o1 & 0xffffu) | (n1 << 16), (o1 >> 16) | (n1 & 0xffff0000u)};
                p = m < 4 ? lay + LO_Q2 + ((size_t)(h0 + hsel) * SEQ + tok0) * 4 : img + hsel * IMG_HEAD + 8192 + (tok0 & 255) * 4;
            } else {
                const unsigned cst = (m & 1) ? 0u : 0x3f803f80u;
                d = m < 12 ? (u32x4){o0, o1, u0[1], u1[1]} : (u32x4){cst, cst, cst, cst};
                p = img + hsel * IMG_HEAD + 9216 + ((rb & 15) * 2 + (g & 1)) * 320 + (16 + (m & 1) + ((m >> 1) & 2)) * 16;
            }
            if (m < 8 ? !(m & 1) : g < 2) gst16c(p, d);
        }
        ST();
        signal_cnt<0>(ws, L * C_LAY + C_QB + ct);
    }
    ST();
    STAMP_OUT(L * 5 + (LAYER ? 2 : 0));
}

template <bool FUSED>
__device__ __forceinline__ void wo_phase(char* ws, char* smem, const unsigned short* x0, unsigned short* out, int L, int mode, int w, int lane) {
    LAUNDER();
    const int m = lane & 15, g = lane >> 4, rb4 = w & 3, kh = w >> 2;
    const int xcd = bid & 7, idx = bid >> 3;
    const int panel = 4 * xcd + (idx >> 3), jt = idx & 7;
    const int rb = panel * 4 + rb4;
    const int xv = 2 * L + mode;
    const char* ob = ws + OFF_LAY + L * LAY_BYTES + LO_O + mode * O_BYTES;
    const bool last = xv == 3;
    const bool dopf = FUSED && !last;
    const Pf pf = pf_qkv(ws, smem, mode ? 0 : 1, bid, w, lane);
    STAMP_DECL; ST();
    if (!FUSED) { const Pf po = pf_wo(ws, smem, mode, bid, w, lane); PF(po, 0); PF(po, 1); PF(po, 2); PF(po, 3); PF(po, 4); PFO_LAST(po); }
    u32x2 xr[3];
#pragma unroll
    for (int c = 0; c < 3; ++c) {
        const int cb = c == 2 ? 4 : 2 * kh + c, gg = c == 2 ? (g & 1) : g;
        const int n0 = 72 * jt + 16 * cb + 4 * gg;
        const char* xp = xv == 0 ? (const char*)(x0 + (size_t)(rb * 16 + m) * 576 + n0)
                                 : ws + OFF_XF + xv * XF_BYTES + ((size_t)(rb * 18 + (n0 >> 5)) * 64 + ((n0 >> 3) & 3) * 16 + m) * 16 + ((n0 >> 2) & 1) * 8;
        gld8c<0>(xr[c], xp);
    }
    if (dopf) { PF(pf, 0); PF(pf, 1); }
    wait_cnt(ws, L * C_LAY + (mode ? C_AT + (panel >> 2) : C_OA + (panel >> 1)), mode ? 32u : 16u);
    asm volatile("" : "+v"(xr[0]), "+v"(xr[1]), "+v"(xr[2]));
    ST();
    bf16x8 af[9]; unsigned pt[4];
    {
        const char* op = ob + ((size_t)(rb * 16 + 8 * kh) * 64 + lane) * 16;
        gld16c<0>(af[0], op); gld16c<1024>(af[1], op); gld16c<2048>(af[2], op); gld16c<3072>(af[3], op);
        gld16c<0>(af[4], op + 4096); gld16c<1024>(af[5], op + 4096); gld16c<2048>(af[6], op + 4096); gld16c<3072>(af[7], op + 4096);
        const char* tp = ob + O_TAIL + ((size_t)(rb * 32 + 16 * kh + 4 * g) * 16 + m) * 4;
        gld4c<0>(pt[0], tp); gld4c<64>(pt[1], tp); gld4c<128>(pt[2], tp); gld4c<192>(pt[3], tp);
    }
    f32x4 acc[5];
#pragma unroll
    for (int c = 0; c < 5; ++c) acc[c] = (f32x4){0.f, 0.f, 0.f, 0.f};
    const lds_cptr bl = (lds_cptr)smem + LDS_WO + kh * 5 * 1024 + lane * 16;
    bf16x8 bb[3][5];
#pragma unroll
    for (int c = 0; c < 5; ++c) { bb[0][c] = *(lds_f8)(bl + c * 1024); bb[1][c] = *(lds_f8)(bl + (10 + c) * 1024); }
#define KSTEP(i_) do { _Pragma("unroll") for (int c = 0; c < 5; ++c) { \
        if ((i_) < 7) bb[((i_) + 2) % 3][c] = *(lds_f8)(bl + (((i_) + 2) * 10 + c) * 1024); \
        SBAR(); acc[c] = MFMA16(bb[(i_) % 3][c], af[i_], acc[c]); SBAR(); } } while (0)
#define WAITF(i_, N) asm volatile("s_waitcnt vmcnt(" #N ")" : "+v"(af[i_]) :: "memory")
    SBAR();
    WAITF(0, 11); ST(); KSTEP(0); WAITF(1, 10); KSTEP(1); WAITF(2, 9); KSTEP(2); WAITF(3, 8); KSTEP(3);
    WAITF(4, 7); KSTEP(4); WAITF(5, 6); KSTEP(5); WAITF(6, 5); KSTEP(6); WAITF(7, 4); KSTEP(7);
    asm volatile("s_waitcnt vmcnt(0)" : "+v"(pt[0]), "+v"(pt[1]), "+v"(pt[2]), "+v"(pt[3]) :: "memory");
    af[8] = __builtin_bit_cast(bf16x8, (u32x4){pt[0], pt[1], pt[2], pt[3]});
    KSTEP(8);
#undef KSTEP
#undef WAITF
    ST();
    lds_ptr ex = (lds_ptr)smem + rb4 * 5 * 1024 + lane * 16;
    if (kh == 0) { *(lds_f4)(ex + 2 * 1024) = acc[2]; *(lds_f4)(ex + 3 * 1024) = acc[3]; }
    else { *(lds_f4)(ex) = acc[0]; *(lds_f4)(ex + 1024) = acc[1]; *(lds_f4)(ex + 4 * 1024) = acc[4]; }
    asm volatile("s_waitcnt lgkmcnt(0)\n\ts_barrier" ::: "memory");
    unsigned pk[3][2];
    {
        const f32x4 o0 = (kh ? acc[2] : acc[0]) + *(lds_f4)(ex + (2 * kh) * 1024), o1 = (kh ? acc[3] : acc[1]) + *(lds_f4)(ex + (2 * kh + 1) * 1024);
        const f32x4 o2 = acc[4] + *(lds_f4)(ex + 4 * 1024);
        pk[0][0] = pack2(o0[0] + bf_lo(xr[0][0]), o0[1] + bf_hi(xr[0][0])); pk[0][1] = pack2(o0[2] + bf_lo(xr[0][1]), o0[3] + bf_hi(xr[0][1]));
        pk[1][0] = pack2(o1[0] + bf_lo(xr[1][0]), o1[1] + bf_hi(xr[1][0])); pk[1][1] = pack2(o1[2] + bf_lo(xr[1][1]), o1[3] + bf_hi(xr[1][1]));
        pk[2][0] = pack2(o2[0] + bf_lo(xr[2][0]), o2[1] + bf_hi(xr[2][0])); pk[2][1] = pack2(o2[2] + bf_lo(xr[2][1]), o2[3] + bf_hi(xr[2][1]));
    }
    {
        auto s0 = __builtin_amdgcn_permlane16_swap(pk[0][0], pk[1][0], false, false), s1 = __builtin_amdgcn_permlane16_swap(pk[0][1], pk[1][1], false, false);
        auto t0 = __builtin_amdgcn_permlane16_swap(pk[2][0], pk[2][0], false, false), t1 = __builtin_amdgcn_permlane16_swap(pk[2][1], pk[2][1], false, false);
        const u32x4 d = {s0[0], s1[0], s0[1], s1[1]}, e = {t0[0], t1[0], t0[1], t1[1]};
        const int n0 = 72 * jt + 32 * kh + 16 * (g & 1) + 8 * (g >> 1), n1 = 72 * jt + 64;
        if (last) {
            typedef __attribute__((address_space(1))) u32x4* g_u4;
            unsigned short* orow = out + (size_t)(rb * 16 + m) * 576;
            *(g_u4)(orow + n0) = d;
            if (kh == 0 && g == 0) *(g_u4)(orow + n1) = e;
        } else {
            char* xo = ws + OFF_XF + (xv + 1) * XF_BYTES + (size_t)rb * 18 * 1024 + m * 16;
            gst16c(xo + (n0 >> 5) * 1024 + ((n0 >> 3) & 3) * 256, d);
            if (kh == 0 && g == 0) gst16c(xo + (n1 >> 5) * 1024 + ((n1 >> 3) & 3) * 256, e);
        }
    }
    if (!last) {
        const bool v2 = kh == 0 && g < 2;
        float v[12];
#pragma unroll
        for (int c = 0; c < 3; ++c) { v[4 * c] = bf_lo(pk[c][0]); v[4 * c + 1] = bf_hi(pk[c][0]); v[4 * c + 2] = bf_lo(pk[c][1]); v[4 * c + 3] = bf_hi(pk[c][1]); }
        float s = ((v[0] + v[1]) + (v[2] + v[3])) + ((v[4] + v[5]) + (v[6] + v[7])) + (v2 ? (v[8] + v[9]) + (v[10] + v[11]) : 0.f);
        s = rowsum4(s);
        const float mean = s * (kh ? (1.f / 32.f) : (1.f / 40.f));
        float q = 0.f, q2 = 0.f;
#pragma unroll
        for (int r = 0; r < 8; ++r) { const float dd = v[r] - mean; q += dd * dd; }
#pragma unroll
        for (int r = 8; r < 12; ++r) { const float dd = v[r] - mean; q2 += dd * dd; }
        q += v2 ? q2 : 0.f;
        q = rowsum4(q);
        if (g == 0) gst8c(ws + OFF_ST + (xv + 1) * ST_BYTES + (size_t)(rb * 16 + m) * 128 + jt * 16 + kh * 8, (f32x2){s, q});
        ST();
        const int line = L * C_LAY + (mode ? C_WB : C_WA) + (panel >> 1);
        if (FUSED) { PF(pf, 2); PF(pf, 3); signal_cnt<4>(ws, line); ST(); PF(pf, 4); PF(pf, 5); PF(pf, 6); PFQ_LAST(pf); }
        else signal_cnt<0>(ws, line);
    }
    ST();
    STAMP_OUT(L * 5 + (mode ? 4 : 1));
}

__device__ __forceinline__ float half_max(float v) {
    auto r = __builtin_amdgcn_permlane32_swap(__float_as_uint(v), __float_as_uint(v), false, false);
    return fmaxf(__uint_as_float(r[0]), __uint_as_float(r[1]));
}
#define STR_(x) #x
#define STR(x) STR_(x)
#define AEXP(r) "v_exp_f32 v" STR(r) ", v" STR(r) "\n\t"
#define ACVT(d, r0, r1) "v_cvt_pk_bf16_f32 v" STR(d) ", v" STR(r0) ", v" STR(r1) "\n\t"
#define AE4(r0, r1, r2, r3, d0, d1) AEXP(r0) AEXP(r1) AEXP(r2) AEXP(r3) ACVT(d0, r0, r1) ACVT(d1, r2, r3)
#define ATILE(KO, K2O, V1O, V0O, XK, X0, X1, XV) \
    "v_mfma_f32_32x32x16_bf16 v[48:63], v[80:83], v[72:75], 0\n\t" XK \
    "ds_read_b128 v[80:83], v104 offset:" STR(KO) "\n\t" \
    AE4(32, 33, 34, 35, 96, 97) \
    "v_mfma_f32_32x32x16_bf16 v[16:31], v[92:95], v[100:103], v[16:31]\n\t" \
    "ds_read_b128 v[92:95], v106 offset:" STR(V1O) "\n\t" X0 \
    AE4(36, 37, 38, 39, 98, 99) \
    "v_mfma_f32_32x32x16_bf16 v[48:63], v[84:87], v[76:79], v[48:63]\n\t" \
    "ds_read_b32 v84, v105 offset:" STR(K2O) "\n\t" \
    AE4(40, 41, 42, 43, 100, 101) \
    "s_waitcnt lgkmcnt(3)\n\t" \
    "v_mfma_f32_32x32x16_bf16 v[0:15], v[88:91], v[96:99], v[0:15]\n\t" \
    AE4(44, 45, 46, 47, 102, 103) \
    "s_waitcnt lgkmcnt(1)\n\t" \
    "v_mfma_f32_32x32x16_bf16 v[32:47], v[80:83], v[64:67], 0\n\t" \
    AE4(48, 49, 50, 51, 96, 97) \
    "v_mfma_f32_32x32x16_bf16 v[0:15], v[92:95], v[100:103], v[0:15]\n\t" X1 \
    AE4(52, 53, 54, 55, 98, 99) \
    "s_waitcnt lgkmcnt(0)\n\t" \
    "v_mfma_f32_32x32x16_bf16 v[32:47], v[84:87], v[68:71], v[32:47]\n\t" \
    AE4(56, 57, 58, 59, 100, 101) \
    "v_mfma_f32_32x32x16_bf16 v[16:31], v[88:91], v[96:99], v[16:31]\n\t" XV \
    "ds_read_b128 v[88:91], v106 offset:" STR(V0O) "\n\t" \
    AE4(60, 61, 62, 63, 102, 103)
#define ACHUNK \
    "s_mov_b32 m0, s22\n\ts_add_u32 s28, s20, 0x2000\n\ts_addc_u32 s29, s21, 0\n\tglobal_load_lds_dwordx4 v107, s[20:21] sc1\n\t" \
    "s_add_u32 s27, s22, 0x2000\n\ts_mov_b32 m0, s27\n\ts_cmp_lt_u32 %[w], 3\n\tglobal_load_lds_dwordx4 v107, s[28:29] sc1\n\t" \
    "s_cbranch_scc0 7f\n\t" \
    "s_add_u32 s27, s22, 0x4000\n\ts_mov_b32 m0, s27\n\ts_add_u32 s28, s20, 0x4000\n\ts_addc_u32 s29, s21, 0\n\tglobal_load_lds_dwordx4 v107, s[28:29] sc1\n\t" \
    "7:\n\t" \
    "s_add_u32 s20, s20, 0x4c00\n\ts_addc_u32 s21, s21, 0\n\t"
#define AWPF \
    "s_mov_b32 m0, s25\n\ts_add_u32 s25, s25, 0x4000\n\tglobal_load_lds_dwordx4 v107, s[30:31]\n\tglobal_load_lds_dwordx4 v107, s[30:31] offset:1024\n\t" \
    "s_add_u32 s30, s30, 0x4000\n\ts_addc_u32 s31, s31, 0\n\t"
#define AKADV "v_add_u32 v104, s23, v104\n\tv_add_u32 v105, s23, v105\n\t"
#define AVADV "v_add_u32 v106, s23, v106\n\t"

template <bool FUSED>
__device__ __forceinline__ void attn_phase(char* ws, char* smem, int L, int w, int lane) {
    LAUNDER();
    const int r32 = lane & 31, hh = lane >> 5, kh = w >> 2;
    const int xcd = bid & 7, idx = bid >> 3;
    const int head = 4 * xcd + (idx >> 3), qb = idx & 7;
    const int qA = qb * 256 + (w & 3) * 64 + r32;
    char* lay = ws + OFF_LAY + L * LAY_BYTES;
    const unsigned lds0 = lds_off(smem) + w * 1024;
    STAMP_DECL; ST();
    wait_cnt(ws, L * C_LAY + C_QB + (head >> 1), 16);
    ST();
    bf16x8 q1a, q1b; unsigned q2wa, q2wb;
    {
        const char* qp = lay + LO_Q1 + ((size_t)head * SEQ + qA) * 32 + hh * 16;
        const char* q2p = lay + LO_Q2 + ((size_t)head * SEQ + qA) * 4;
        gld16c<0>(q1a, qp); gld16c<1024>(q1b, qp); gld4c<0>(q2wa, q2p); gld4c<128>(q2wb, q2p);
    }
    const char* srcw = lay + LO_IMG + (size_t)head * IMG_HEAD + w * 1024;
    {
        const char* src = srcw + lane * 16;
#pragma unroll
        for (int k = 0; k < 4; ++k) glds1_sc1(src + k * 8192, lds0 + k * 8192);
        glds1_sc1(src + (w < 6 ? 4 : 3) * 8192, lds0 + (w < 6 ? 4 : 3) * 8192);
    }
    lds_cptr kp1 = (lds_cptr)smem + lane * 16;
    lds_cptr kp2 = (lds_cptr)smem + 8192 + r32 * 4;
    const int dmap = r32 < 19 ? r32 : 19;
    lds_cptr vp = (lds_cptr)smem + 9216 + hh * 320 + dmap * 16;
    asm volatile("s_waitcnt vmcnt(2)\n\ts_barrier" : "+v"(q1a), "+v"(q1b), "+v"(q2wa), "+v"(q2wb) :: "memory");
    ST();
#define LDK1(t) (*(lds_f8)(kp1 + (t) * 1024))
#define LDK2(t) (__builtin_bit_cast(bf16x8, (u32x4){*(lds_vu32)(kp2 + (t) * 128), 0x3f80u, 0u, 0u}))
#define LDV(u) (*(lds_f8)(vp + (u) * 640))
    const f32x16 z16 = {};
    f32x16 s0, s1, oa = {}, ob = {};
    bf16x8 k1 = LDK1(0), k2 = LDK2(0), v0, v1, q2a, q2b;
    u32x4 p0, p1 = {0u, 0u, 0u, 0u};
    {
        q2a = __builtin_bit_cast(bf16x8, (u32x4){hh ? 0u : q2wa, 0u, 0u, 0u});
        q2b = __builtin_bit_cast(bf16x8, (u32x4){hh ? 0u : q2wb, 0u, 0u, 0u});
        f32x16 sa = MFMA32(k1, q1a, z16), sb = MFMA32(k1, q1b, z16); sa = MFMA32(k2, q2a, sa); sb = MFMA32(k2, q2b, sb);
        kp1 += kh * 4096; kp2 += kh * 512; vp += kh * 5120;
        k1 = LDK1(0); k2 = LDK2(0); v0 = LDV(0); v1 = v0;
        float ma = fmaxf(sa[0], sa[1]), mb = fmaxf(sb[0], sb[1]);
#pragma unroll
        for (int i = 2; i < 16; ++i) { ma = fmaxf(ma, sa[i]); mb = fmaxf(mb, sb[i]); }
        ma = half_max(ma); mb = half_max(mb);
        q2a = __builtin_bit_cast(bf16x8, (u32x4){hh ? 0u : q2wa, hh ? 0u : (pack2(-ma, 0.f) & 0xffffu), 0u, 0u});
        q2b = __builtin_bit_cast(bf16x8, (u32x4){hh ? 0u : q2wb, hh ? 0u : (pack2(-mb, 0.f) & 0xffffu), 0u, 0u});
    }
    s0 = MFMA32(k1, q1a, z16); s0 = MFMA32(k2, q2a, s0);
    ST();
    {
        unsigned a1 = (unsigned)(size_t)kp1, a2 = (unsigned)(size_t)kp2, a3 = (unsigned)(size_t)vp;
        const unsigned loff = lane * 16;
        const char* dsrc = srcw + 2 * IMG_CHUNK; const unsigned dbase = lds0;
        const int jt = (bid >> 3) & 7;
        const char* wsrc = ws + OFF_WOB + (size_t)jt * WO_TILE + w * 2048; const unsigned wdst = lds_off(smem) + LDS_WO + w * 2048;
        asm volatile(
            "s_mov_b32 s26, m0\n\t"
            "s_mov_b64 s[20:21], %[dsrc]\n\t"
            "s_add_u32 s22, %[dbase], 0x9800\n\t"
            "s_mov_b64 s[30:31], %[wsrc]\n\t"
            "s_mov_b32 s25, %[wdst]\n\t"
            "s_mov_b32 s24, 0\n\t"
            "s_mov_b32 s33, 0\n\t"
            "s_mov_b32 s34, 0x4c00\n\t"
            "s_waitcnt lgkmcnt(0)\n\t"
            "s_nop 7\n\t"
            "s_nop 3\n\t"
            "2:\n\t"
            "s_setprio 2\n\t"
            ATILE(1024, 128, 640, 1280, "", "", "", "")
            "s_setprio 1\n\t"
            "s_cmp_eq_u32 s33, 2\n\t"
            "s_cselect_b32 s23, 0xffff6800, s34\n\t"
            "s_sub_u32 s35, s24, 1\n\t"
            "s_cmp_lt_u32 s35, %[npf]\n\t"
            "s_cbranch_scc0 3f\n\t"
            AWPF
            "3:\n\t"
            ATILE(2048, 256, 1920, 2560, "", "", "", "")
            "s_setprio 0\n\t"
            ATILE(3072, 384, 3200, 3840, "", "", "", "")
            "s_waitcnt vmcnt(2)\n\t"
            "s_cmp_lt_u32 s35, %[npf]\n\t"
            "s_cbranch_scc1 4f\n\t"
            "s_waitcnt vmcnt(0)\n\t"
            "4:\n\t"
            "s_barrier\n\t"
            "s_cmp_lt_u32 s24, 6\n\t"
            "s_cbranch_scc0 5f\n\t"
            ACHUNK
            "s_cmp_eq_u32 s33, 0\n\t"
            "s_cselect_b32 s27, 0xffff6800, s34\n\t"
            "s_add_u32 s22, s22, s27\n\t"
            "5:\n\t"
            "s_setprio 3\n\t"
            ATILE(0, 0, 4480, 0, AKADV, "", "", AVADV)
            "s_add_u32 s24, s24, 1\n\t"
            "s_add_u32 s33, s33, 1\n\t"
            "s_cmp_eq_u32 s33, 3\n\t"
            "s_cselect_b32 s33, 0, s33\n\t"
            "s_cmp_lt_u32 s24, 8\n\t"
            "s_cbranch_scc1 2b\n\t"
            "s_waitcnt lgkmcnt(0)\n\t"
            "v_mfma_f32_32x32x16_bf16 v[16:31], v[92:95], v[100:103], v[16:31]\n\t"
            "s_mov_b32 m0, s26\n\t"
            "s_setprio 0\n\t"
            "s_nop 7\n\t"
            "s_nop 3\n\t"
            : "+{v[0:15]}"(oa), "+{v[16:31]}"(ob), "+{v[32:47]}"(s0), "={v[48:63]}"(s1), "+{v[80:83]}"(k1), "+{v[84:87]}"(k2), "+{v[88:91]}"(v0), "+{v[92:95]}"(v1),
              "={v[96:99]}"(p0), "+{v[100:103]}"(p1), "+{v104}"(a1), "+{v105}"(a2), "+{v106}"(a3)
            : "{v[64:67]}"(q1a), "{v[68:71]}"(q2a), "{v[72:75]}"(q1b), "{v[76:79]}"(q2b), "{v107}"(loff), [dsrc] "s"(dsrc), [dbase] "s"(dbase),
              [wsrc] "s"(wsrc), [wdst] "s"(wdst), [w] "s"(w), [npf] "s"(FUSED ? (w < 5 ? 6 : 5) : 0)
            : "s20", "s21", "s22", "s23", "s24", "s25", "s26", "s27", "s28", "s29", "s30", "s31", "s33", "s34", "s35", "scc", "memory");
    }
    ST();
    lds_ptr ex = (lds_ptr)smem + (w < 6 ? w * 3072 : 2 * (int)IMG_CHUNK + 12288 + (w - 6) * 3072) + lane * 16;
    const int wo_ = w ^ 4;
    lds_ptr ey = (lds_ptr)smem + (wo_ < 6 ? wo_ * 3072 : 2 * (int)IMG_CHUNK + 12288 + (wo_ - 6) * 3072) + lane * 16;
    f32x16 o;
    {
        const f32x16 snd = kh ? oa : ob; o = kh ? ob : oa;
        *(lds_f4)(ex) = (f32x4){snd[0], snd[1], snd[2], snd[3]}; *(lds_f4)(ex + 1024) = (f32x4){snd[4], snd[5], snd[6], snd[7]};
        *(lds_f4)(ex + 2048) = (f32x4){snd[8], snd[9], snd[10], snd[11]};
    }
    asm volatile("s_waitcnt lgkmcnt(0)\n\ts_barrier" ::: "memory");
    {
        const f32x4 a = *(lds_f4)(ey), b = *(lds_f4)(ey + 1024), c = *(lds_f4)(ey + 2048);
#pragma unroll
        for (int i = 0; i < 4; ++i) { o[i] += a[i]; o[4 + i] += b[i]; o[8 + i] += c[i]; }
    }
    const int q = qA + 32 * kh;
    bool bad = !(fabsf(o[10]) < 1.0e30f);
#pragma unroll
    for (int i = 0; i < 10; ++i) bad = bad || !(fabsf(o[i]) < 1.0e38f);
    if (__any(bad)) {
        o = (f32x16){};
        float mrun = -INFINITY;
        const bf16x8 q1 = kh ? q1b : q1a;
        const bf16x8 q2 = __builtin_bit_cast(bf16x8, (u32x4){hh ? 0u : (kh ? q2wb : q2wa), 0u, 0u, 0u});
        const char* img = lay + LO_IMG + (size_t)head * IMG_HEAD;
#pragma unroll 1
        for (int t = 0; t < 64; ++t) {
            const char* cb = img + (t >> 3) * IMG_CHUNK; const int tt = t & 7;
            const char* vb = cb + 9216 + tt * 1280 + hh * 320 + dmap * 16;
            bf16x8 fk, fv0, fv1; unsigned fk2;
            gld16c<0>(fk, cb + tt * 1024 + lane * 16); gld4c<0>(fk2, cb + 8192 + tt * 128 + r32 * 4); gld16c<0>(fv0, vb); gld16c<640>(fv1, vb);
            asm volatile("s_waitcnt vmcnt(0)" : "+v"(fk), "+v"(fk2), "+v"(fv0), "+v"(fv1) :: "memory");
            f32x16 s = MFMA32(fk, q1, z16);
            s = MFMA32(__builtin_bit_cast(bf16x8, (u32x4){fk2, 0u, 0u, 0u}), q2, s);
            float mx = fmaxf(s[0], s[1]);
#pragma unroll
            for (int i = 2; i < 16; ++i) mx = fmaxf(mx, s[i]);
            mx = fmaxf(half_max(mx), mrun);
            const float al = EXP2(mrun - mx);
            mrun = mx;
#pragma unroll
            for (int i = 0; i < 16; ++i) o[i] *= al;
#pragma unroll
            for (int i = 0; i < 4; ++i) {
                p0[i] = pack2(EXP2(s[2 * i] - mx), EXP2(s[2 * i + 1] - mx));
                p1[i] = pack2(EXP2(s[8 + 2 * i] - mx), EXP2(s[8 + 2 * i + 1] - mx));
            }
            o = MFMA32(fv0, __builtin_bit_cast(bf16x8, p0), o);
            o = MFMA32(fv1, __builtin_bit_cast(bf16x8, p1), o);
        }
    }
    {
        auto r = __builtin_amdgcn_permlane32_swap(__float_as_uint(o[10]), __float_as_uint(o[10]), false, false);
        const float rl = __builtin_amdgcn_rcpf(__uint_as_float(r[0]));
        const int rb = q >> 4, m = q & 15;
        gst16c(lay + LO_O + O_BYTES + ((size_t)(rb * 16 + (head >> 1)) * 64 + (2 * (head & 1) + hh) * 16 + m) * 16,
               (u32x4){pack2(o[0] * rl, o[1] * rl), pack2(o[2] * rl, o[3] * rl), pack2(o[4] * rl, o[5] * rl), pack2(o[6] * rl, o[7] * rl)});
        const unsigned t0 = pack2(o[8] * rl, o[9] * rl);
        const unsigned t1 = __builtin_amdgcn_mov_dpp(t0, 0x55, 0xf, 0xf, true), t2 = __builtin_amdgcn_mov_dpp(t0, 0xAA, 0xf, 0xf, true),
                       t3 = __builtin_amdgcn_mov_dpp(t0, 0xFF, 0xf, 0xf, true);
        if (hh == 0 && (r32 & 3) == 0) gst16c(lay + LO_O + O_BYTES + O_TAIL + ((size_t)(rb * 32 + head) * 16 + m) * 4, (u32x4){t0, t1, t2, t3});
    }
    ST();
    signal_cnt<0>(ws, L * C_LAY + C_AT + qb);
    ST();
    STAMP_OUT(L * 5 + 3);
#undef LDK1
#undef LDK2
#undef LDV
}

__global__ __launch_bounds__(512) void prep_kernel(const unsigned short* __restrict__ x0, const unsigned short* __restrict__ wA,
                                                   const unsigned short* __restrict__ woA, const unsigned short* __restrict__ wB,
                                                   const unsigned short* __restrict__ woB, char* ws) {
    __shared__ __attribute__((aligned(16))) char tbuf[7 * 4096];
    __shared__ __attribute__((aligned(16))) float red[72 * 16];
    const int tid = threadIdx.x, bid = blockIdx.x, w = __builtin_amdgcn_readfirstlane(tid >> 6), l = tid & 63;
    STAMP_DECL; ST();
    const int lin = (bid & 7) * 27 + (bid >> 3);
    const bool jobW = (bid >> 3) < 27;
    const int cmat = lin >= 108, cblk = lin - 108 * cmat;
    u32x2 sv[8]; u32x4 xv[9];
    if (w < 5) {
        if (jobW) {
            const int T = min(tid, 287);
            const char* p = (const char*)((cmat ? wB : wA) + (size_t)(8 * (T >> 2)) * 1728 + 16 * cblk + 4 * (T & 3));
            gld8<0>(sv[0], p); gld8<3456>(sv[1], p); p += 6912; gld8<0>(sv[2], p); gld8<3456>(sv[3], p); p += 6912;
            gld8<0>(sv[4], p); gld8<3456>(sv[5], p); p += 6912; gld8<0>(sv[6], p); gld8<3456>(sv[7], p);
        }
    } else if (w < 7) {
        const int T = bid * 90 + min(tid - 320, 89), jmat = T / 11520, r = T - jmat * 11520, jsg = r / 160, jq = r - jsg * 160;
        const int jt = jq / 20, lc = min(4 * (jq - jt * 20), 68), ss = jsg >> 2, gg = jsg & 3;
        int r0, rb_, rc;
        if (ss >= 16) { r0 = (16 * (ss - 16) + 4 * gg) * 18 + 16; rb_ = 18; rc = 36; }
        else if (jmat == 0) { r0 = 36 * ss + 4 * gg; rb_ = 2; rc = 18; }
        else { r0 = (2 * ss + (gg >> 1)) * 18 + 4 * (gg & 1); rb_ = 2; rc = 8; }
        const char* p = (const char*)((jmat ? woB : woA) + (size_t)r0 * 576 + 72 * jt + lc);
        const char* p2 = p + rb_ * 1152; const char* p4 = p + rc * 1152; const char* p6 = p4 + rb_ * 1152;
        gld8<0>(sv[0], p); gld8<1152>(sv[1], p); gld8<0>(sv[2], p2); gld8<1152>(sv[3], p2);
        gld8<0>(sv[4], p4); gld8<1152>(sv[5], p4); gld8<0>(sv[6], p6); gld8<1152>(sv[7], p6);
    } else {
        const int T = bid * 64 + l, m = T & 15, j = (T >> 4) & 7, rb = T >> 7;
        const char* p = (const char*)(x0 + (size_t)(rb * 16 + m) * 576 + 72 * j);
        gld16<0>(xv[0], p); gld16<16>(xv[1], p); gld16<32>(xv[2], p); gld16<48>(xv[3], p); gld16<64>(xv[4], p);
        gld16<80>(xv[5], p); gld16<96>(xv[6], p); gld16<112>(xv[7], p); gld16<128>(xv[8], p);
    }
    unsigned tw = 0;
    if (w == 7 && (bid >> 3) < 11) asm volatile("global_load_dword %0, %1, off sc1" : "=v"(tw) : "v"(ws + ((size_t)(bid >> 3) * 64 + l) * 65536 + 61440) : "memory");
    int tct, tcb, tc; qkv_col_map(16 * cblk + (l & 15), tct, tcb, tc);
    ST();
    asm volatile("s_waitcnt vmcnt(0)" : "+v"(sv[0]), "+v"(sv[1]), "+v"(sv[2]), "+v"(sv[3]), "+v"(sv[4]), "+v"(sv[5]), "+v"(sv[6]), "+v"(sv[7]) :: "memory");
    asm volatile("" : "+v"(tw));
    asm volatile("" : "+v"(xv[0]), "+v"(xv[1]), "+v"(xv[2]), "+v"(xv[3]), "+v"(xv[4]), "+v"(xv[5]), "+v"(xv[6]), "+v"(xv[7]), "+v"(xv[8]));
    ST();
    u32x4 cc[4];
    if (w < 7) {
        transpose8x4(sv, cc);
        lds_ptr tb = (lds_ptr)tbuf + w * 4096;
#pragma unroll
        for (int i = 0; i < 4; ++i) *(lds_u4)(tb + (l * 4 + i) * 16) = cc[i];
#pragma unroll
        for (int i = 0; i < 4; ++i) cc[i] = *(lds_u4)(tb + (i * 64 + l) * 16);
        if (w < 5) {
#pragma unroll
            for (int i = 0; i < 4; ++i) {
                const int sg = 16 * w + 4 * i + (l >> 4);
                if (sg < 72) red[sg * 16 + (l & 15)] = dot_ones(cc[i][3], dot_ones(cc[i][2], dot_ones(cc[i][1], dot_ones(cc[i][0], 0.f))));
            }
        }
    }
    if (w < 5) {
        if (jobW) {
            char* dst = ws + (cmat ? OFF_WTB : OFF_WTA) + ((size_t)(tct * 18 * 7 + tcb) * 64 + (l >> 4) * 16 + tc) * 16;
#pragma unroll
            for (int i = 0; i < 4; ++i)
                if (4 * w + i < 18) gst16c(dst + (4 * w + i) * 7168, cc[i]);
        }
    } else if (w < 7) {
#pragma unroll
        for (int i = 0; i < 4; ++i) {
            const int loc = (w - 5) * 256 + i * 64 + l;
            const int X = bid * 360 + loc, mat = X / 46080, r = X - mat * 46080, sg = r / 640, n = r - sg * 640, jt = n / 80, lc = n - jt * 80;
            const int ss = sg >> 2, okh = ss < 16 ? (ss >> 3) : (ss - 16), oi = ss < 16 ? (ss & 7) : 8;
            if (loc < 360) gst16c(ws + (mat ? OFF_WOB : OFF_WOA) + (size_t)jt * WO_TILE + (size_t)((oi * 2 + okh) * 5 + (lc >> 4)) * 1024 + ((sg & 3) * 16 + (lc & 15)) * 16,
                                  lc < 72 ? cc[i] : (u32x4){0u, 0u, 0u, 0u});
        }
    } else {
        const int T = bid * 64 + l, m = T & 15, j = (T >> 4) & 7, rb = T >> 7;
        float sa = 0.f, sb = 0.f;
#pragma unroll
        for (int i = 0; i < 9; ++i) {
            const int q = 9 * j + i;
            gst16c(ws + OFF_XF + ((size_t)(rb * 18 + (q >> 2)) * 64 + (q & 3) * 16 + m) * 16, xv[i]);
            const float a = dot_ones(xv[i][3], dot_ones(xv[i][2], dot_ones(xv[i][1], dot_ones(xv[i][0], 0.f))));
            if (i < 4 || i == 8) sa += a; else sb += a;
        }
        const float ma = sa * (1.f / 40.f), mb = sb * (1.f / 32.f);
        float qa = 0.f, qb = 0.f;
#pragma unroll
        for (int i = 0; i < 9; ++i) {
            const bool pa = i < 4 || i == 8; const float mu = pa ? ma : mb; float a = 0.f;
#pragma unroll
            for (int k = 0; k < 4; ++k) { const float d0 = bf_lo(xv[i][k]) - mu, d1 = bf_hi(xv[i][k]) - mu; a += d0 * d0 + d1 * d1; }
            if (pa) qa += a; else qb += a;
        }
        gst16c(ws + OFF_ST + (size_t)(rb * 16 + m) * 128 + j * 16, __builtin_bit_cast(u32x4, (f32x4){sa, qa, sb, qb}));
    }
    if (w == 6) {
        if (l < 36) {
            const int Z = bid * 36 + l, zm = Z / 4608, z2 = Z - zm * 4608, zct = z2 / 288, r = z2 - zct * 288;
            gst16c(ws + (zm ? OFF_WTB : OFF_WTA) + ((size_t)((zct * 18 + (r >> 4)) * 7 + 6) * 64 + ((r >> 2) & 3) * 16 + 12 + (r & 3)) * 16, (u32x4){0u, 0u, 0u, 0u});
        } else if (l < 49) {
            const int Z = bid * 13 + (l - 36);
            if ((size_t)Z * 16 < ZERO_BYTES) gst16c(ws + (size_t)Z * 16, (u32x4){0u, 0u, 0u, 0u});
        } else if (l == 49 && bid < 32) gst16c(ws + ((bid & 16) ? OFF_CSB : OFF_CSA) + ((bid & 15) * 112 + 108) * 4, (u32x4){0u, 0u, 0u, 0u});
    }
    ST();
    asm volatile("s_waitcnt lgkmcnt(0)\n\ts_barrier" ::: "memory");
    if (jobW && w == 0) {
        float a = 0.f;
#pragma unroll
        for (int i = 0; i < 18; ++i) a += red[((l >> 4) * 18 + i) * 16 + (l & 15)];
        a = rowsum4(a);
        if (l < 16) __hip_atomic_store((gu32*)(ws + (cmat ? OFF_CSB : OFF_CSA)) + tct * 112 + tcb * 16 + tc, __float_as_uint(a), RLX_AGENT);
    }
}

template <int MASK>
__global__ __launch_bounds__(512, 2) void mega_kernel(char* ws, const unsigned short* x0, unsigned short* out, int h_lo, int h_hi) {
    __shared__ __attribute__((aligned(16))) char smem[160 * 1024];
    const int lane = threadIdx.x & 63, w = __builtin_amdgcn_readfirstlane(threadIdx.x >> 6);
    constexpr bool FUSED = MASK == 7;
    if (FUSED && w == 7 && (blockIdx.x >> 3) < 4) {
        unsigned long long pc; asm volatile("s_getpc_b64 %0" : "=s"(pc));
        const char* cp = (const char*)(pc & ~127ull) + (blockIdx.x >> 3) * 7168 + min(lane, 55) * 128;
        unsigned t; asm volatile("global_load_dword %0, %1, off\n\ts_waitcnt vmcnt(0)" : "=v"(t) : "v"(cp) : "memory");
    }
#pragma unroll 1
    for (int hl = h_lo; hl < h_hi; ++hl) {
        const int L = hl >> 1;
        if (hl & 1) {
            if (MASK & 1) qkv_phase<1, FUSED>(ws, smem, L, w, lane);
            if (MASK & 2) attn_phase<FUSED>(ws, smem, L, w, lane);
        } else {
            if (MASK & 1) qkv_phase<0, FUSED>(ws, smem, L, w, lane);
        }
        if (MASK & 4) wo_phase<FUSED>(ws, smem, x0, out, L, hl & 1, w, lane);
    }
}

extern "C" void kernel_launch(void* const* d_in, const int* in_sizes, int n_in, void* d_out, int out_size, void* d_ws, size_t ws_size,
                              hipStream_t stream) {
    const unsigned short* x0 = (const unsigned short*)d_in[0];
    const unsigned short* wA = (const unsigned short*)d_in[1];
    const unsigned short* woA = (const unsigned short*)d_in[2];
    const unsigned short* wB = (const unsigned short*)d_in[4];
    const unsigned short* woB = (const unsigned short*)d_in[5];
    char* ws = (char*)d_ws;
    unsigned short* out = (unsigned short*)d_out;
    prep_kernel<<<256, 512, 0, stream>>>(x0, wA, woA, wB, woB, ws);
    mega_kernel<7><<<256, 512, 0, stream>>>(ws, x0, out, 0, 4);
}
```

```cpp
#include <hip/hip_runtime.h>
#include <hip/hip_bf16.h>
#include <stdint.h>

typedef __attribute__((ext_vector_type(8))) short bf16x8;
typedef __attribute__((ext_vector_type(4))) short bf16x4;
typedef __attribute__((ext_vector_type(4))) float f32x4;
typedef __attribute__((ext_vector_type(2))) float f32x2;
typedef __attribute__((ext_vector_type(16))) float f32x16;
typedef __attribute__((ext_vector_type(4))) unsigned u32x4;
typedef __attribute__((ext_vector_type(2))) unsigned u32x2;
typedef __attribute__((ext_vector_type(2))) __bf16 bfx2;
#define LDSAS __attribute__((address_space(3)))
typedef LDSAS char* lds_ptr;
typedef const LDSAS char* lds_cptr;
typedef LDSAS f32x4* lds_f4;
typedef LDSAS f32x2* lds_f2;
typedef LDSAS float* lds_f1;
typedef LDSAS u32x4* lds_u4;
typedef const volatile LDSAS unsigned* lds_vu32;
typedef const LDSAS bf16x8* lds_f8;

constexpr int SEQ = 2048, DM = 576, NH = 32;

constexpr size_t WT_TILE = 126 * 1024;
constexpr size_t WO_TILE = 90 * 1024;
constexpr size_t IMG_CHUNK = 19 * 1024;
constexpr size_t IMG_HEAD = 8 * IMG_CHUNK;
constexpr int KV_HEAD = 19 * 20;
constexpr size_t XF_BYTES = (size_t)SEQ * DM * 2, ST_BYTES = (size_t)SEQ * 128;
constexpr size_t OFF_CNT = 0;
constexpr size_t CNT_BYTES = 2 * 96 * 256 + 256;
constexpr size_t ZERO_BYTES = 49664;
constexpr size_t OFF_KV  = 65536;
constexpr size_t OFF_WTA = OFF_KV + 2 * 16 * 16 * 3072;
constexpr size_t OFF_WTB = OFF_WTA + 16 * WT_TILE + 4096;
constexpr size_t OFF_CSA = OFF_WTB + 16 * WT_TILE + 4096;
constexpr size_t OFF_CSB = OFF_CSA + 8192;
constexpr size_t OFF_WOA = OFF_CSB + 8192;
constexpr size_t OFF_WOB = OFF_WOA + 8 * WO_TILE + 8192;
constexpr size_t OFF_XF  = OFF_WOB + 8 * WO_TILE + 8192;
constexpr size_t OFF_ST  = OFF_XF + 4 * XF_BYTES;
constexpr size_t OFF_LAY = OFF_ST + 4 * ST_BYTES;
constexpr size_t O_BYTES = (size_t)128 * 16 * 1024 + (size_t)128 * 32 * 64;
constexpr size_t O_TAIL = (size_t)128 * 16 * 1024;
constexpr size_t LO_O   = 0;
constexpr size_t LO_Q1  = LO_O + 2 * O_BYTES;
constexpr size_t LO_Q2  = LO_Q1 + (size_t)NH * SEQ * 32;
constexpr size_t LO_IMG = LO_Q2 + (size_t)NH * SEQ * 4;
constexpr size_t LAY_BYTES = LO_IMG + (size_t)NH * IMG_HEAD;
static_assert(ZERO_BYTES <= OFF_KV && CNT_BYTES <= ZERO_BYTES && ZERO_BYTES % 16 == 0, "layout");
constexpr int C_QA = 0, C_OA = 16, C_WA = 32, C_QB = 48, C_AT = 64, C_WB = 80, C_LAY = 96;

constexpr float QSCALE = 0.23570226039551584f * 1.4426950408889634f;

__device__ __forceinline__ unsigned pack2(float lo, float hi) {
    f32x2 v = {lo, hi};
    bfx2 r = __builtin_convertvector(v, bfx2);
    return __builtin_bit_cast(unsigned, r);
}
__device__ __forceinline__ float bf_lo(unsigned u) { return __uint_as_float(u << 16); }
__device__ __forceinline__ float bf_hi(unsigned u) { return __uint_as_float(u & 0xffff0000u); }
__device__ __forceinline__ float phi_f(float t) { return (t > 0.f ? t + 1.f : __expf(t)) + 1e-6f; }
__device__ __forceinline__ unsigned lds_off(const void* p) { return (unsigned)(size_t)(lds_cptr)p; }

#define MFMA16(a, b, c) __builtin_amdgcn_mfma_f32_16x16x32_bf16(a, b, c, 0, 0, 0)
#define MFMA32(a, b, c) __builtin_amdgcn_mfma_f32_32x32x16_bf16(a, b, c, 0, 0, 0)
#define MFMA16K16(a, b, c) __builtin_amdgcn_mfma_f32_16x16x16bf16_1k(a, b, c, 0, 0, 0)
__device__ __forceinline__ float rowsum4(float v) {
    auto a = __builtin_amdgcn_permlane16_swap(__float_as_uint(v), __float_as_uint(v), false, false);
    v = __uint_as_float(a[0]) + __uint_as_float(a[1]);
    auto b = __builtin_amdgcn_permlane32_swap(__float_as_uint(v), __float_as_uint(v), false, false);
    return __uint_as_float(b[0]) + __uint_as_float(b[1]);
}
#define PIN(x) asm volatile("" : "+v"(x))
#define LAUNDER() int bid = blockIdx.x; asm volatile("" : "+v"(lane)); asm volatile("" : "+s"(w), "+s"(ws), "+s"(bid), "+s"(L))
#define SBAR() __builtin_amdgcn_sched_barrier(0)
#define EXP2(x) __builtin_amdgcn_exp2f(x)

template <int N>
__device__ __forceinline__ void glds(const void* g, unsigned lds) {
    unsigned keep;
    if constexpr (N == 4)
        asm volatile("s_mov_b32 %0, m0\n\ts_mov_b32 m0, %2\n\ts_nop 0\n\t"
                     "global_load_lds_dwordx4 %1, off\n\tglobal_load_lds_dwordx4 %1, off offset:1024\n\t"
                     "global_load_lds_dwordx4 %1, off offset:2048\n\tglobal_load_lds_dwordx4 %1, off offset:3072\n\t"
                     "s_mov_b32 m0, %0" : "=&s"(keep) : "v"(g), "s"(lds) : "memory");
    else
        asm volatile("s_mov_b32 %0, m0\n\ts_mov_b32 m0, %2\n\ts_nop 0\n\t"
                     "global_load_lds_dwordx4 %1, off\n\tglobal_load_lds_dwordx4 %1, off offset:1024\n\t"
                     "s_mov_b32 m0, %0" : "=&s"(keep) : "v"(g), "s"(lds) : "memory");
}
__device__ __forceinline__ void glds1_sc1(const void* g, unsigned lds) {
    unsigned keep;
    asm volatile("s_mov_b32 %0, m0\n\ts_mov_b32 m0, %2\n\ts_nop 0\n\tglobal_load_lds_dwordx4 %1, off sc1\n\ts_mov_b32 m0, %0"
                 : "=&s"(keep) : "v"(g), "s"(lds) : "memory");
}
template <int OFF, class T> __device__ __forceinline__ void gld16(T& d, const void* p) {
    asm volatile("global_load_dwordx4 %0, %1, off offset:%2" : "=v"(d) : "v"(p), "i"(OFF) : "memory"); }
template <int OFF, class T> __device__ __forceinline__ void gld4(T& d, const void* p) {
    asm volatile("global_load_dword %0, %1, off offset:%2" : "=v"(d) : "v"(p), "i"(OFF) : "memory"); }
template <int OFF, class T> __device__ __forceinline__ void gld16c(T& d, const void* p) {
    asm volatile("global_load_dwordx4 %0, %1, off offset:%2 sc1" : "=v"(d) : "v"(p), "i"(OFF) : "memory"); }
template <int OFF, class T> __device__ __forceinline__ void gld8c(T& d, const void* p) {
    asm volatile("global_load_dwordx2 %0, %1, off offset:%2 sc1" : "=v"(d) : "v"(p), "i"(OFF) : "memory"); }
template <int OFF, class T> __device__ __forceinline__ void gld4c(T& d, const void* p) {
    asm volatile("global_load_dword %0, %1, off offset:%2 sc1" : "=v"(d) : "v"(p), "i"(OFF) : "memory"); }
__device__ __forceinline__ void gst16c(void* p, u32x4 v) { asm volatile("global_store_dwordx4 %0, %1, off sc1\n\ts_nop 1" :: "v"(p), "v"(v) : "memory"); }
__device__ __forceinline__ void gst8c(void* p, f32x2 v) { asm volatile("global_store_dwordx2 %0, %1, off sc1" :: "v"(p), "v"(v) : "memory"); }

typedef __attribute__((address_space(1))) unsigned gu32;
#define RLX_AGENT __ATOMIC_RELAXED, __HIP_MEMORY_SCOPE_AGENT
template <int N> __device__ __forceinline__ void signal_cnt(char* ws, int line) {
    asm volatile("s_waitcnt vmcnt(%0) lgkmcnt(0)\n\ts_barrier" :: "i"(N) : "memory");
    if (threadIdx.x == 0) __hip_atomic_fetch_add((gu32*)(ws + OFF_CNT + (size_t)line * 256), 1u, RLX_AGENT);
}
struct Polls { unsigned v0, v1, v2, v3; };
#define RETIRE(pl) asm volatile("" :: "v"((pl).v0), "v"((pl).v1), "v"((pl).v2), "v"((pl).v3))
__device__ __forceinline__ Polls wait_cnt(char* ws, int line, unsigned want) {
    unsigned v0, v1, v2, v3, r, n;
    const char* p = ws + OFF_CNT + (size_t)line * 256;
    const unsigned poller = __builtin_amdgcn_readfirstlane(threadIdx.x < 64 ? 1u : 0u);
    asm volatile(
        "s_cmp_eq_u32 %[poller], 0\n\t"
        "s_cbranch_scc1 3f\n\t"
        "global_load_dword %[v0], %[p], off sc1\n\ts_sleep 1\n\tglobal_load_dword %[v1], %[p], off sc1\n\ts_sleep 1\n\t"
        "global_load_dword %[v2], %[p], off sc1\n\ts_sleep 1\n\tglobal_load_dword %[v3], %[p], off sc1\n\t"
        "s_mov_b32 %[n], 0x20000\n\t"
        "1:\n\t"
        "s_waitcnt vmcnt(3)\n\tv_readfirstlane_b32 %[r], %[v0]\n\ts_nop 0\n\tglobal_load_dword %[v0], %[p], off sc1\n\ts_cmp_ge_u32 %[r], %[want]\n\ts_cbranch_scc1 4f\n\t"
        "s_waitcnt vmcnt(3)\n\tv_readfirstlane_b32 %[r], %[v1]\n\ts_nop 0\n\tglobal_load_dword %[v1], %[p], off sc1\n\ts_cmp_ge_u32 %[r], %[want]\n\ts_cbranch_scc1 4f\n\t"
        "s_waitcnt vmcnt(3)\n\tv_readfirstlane_b32 %[r], %[v2]\n\ts_nop 0\n\tglobal_load_dword %[v2], %[p], off sc1\n\ts_cmp_ge_u32 %[r], %[want]\n\ts_cbranch_scc1 4f\n\t"
        "s_waitcnt vmcnt(3)\n\tv_readfirstlane_b32 %[r], %[v3]\n\ts_nop 0\n\tglobal_load_dword %[v3], %[p], off sc1\n\ts_cmp_ge_u32 %[r], %[want]\n\ts_cbranch_scc1 4f\n\t"
        "s_sub_u32 %[n], %[n], 1\n\t"
        "s_cmp_lg_u32 %[n], 0\n\t"
        "s_cbranch_scc1 1b\n\t"
        "3:\n\t"
        "s_waitcnt vmcnt(0)\n\t"
        "4:\n\t"
        "s_waitcnt lgkmcnt(0)\n\t"
        "s_barrier"
        : [v0] "=&v"(v0), [v1] "=&v"(v1), [v2] "=&v"(v2), [v3] "=&v"(v3), [r] "=&s"(r), [n] "=&s"(n)
        : [p] "v"(p), [want] "s"(want), [poller] "s"(poller) : "scc", "memory");
    return Polls{v0, v1, v2, v3};
}

__device__ __forceinline__ void qkv_col_map(int n, int& ct, int& cb, int& c) {
    const int part = n / 576, rem = n - part * 576, head = rem / 18, d = rem - head * 18;
    ct = head >> 1; const int hb = head & 1;
    if (d < 16) { cb = part * 2 + hb; c = d; } else { cb = 6; c = part * 4 + hb * 2 + (d - 16); }
}
__device__ __forceinline__ float dot_ones(unsigned u, float acc) {
    return __builtin_amdgcn_fdot2_f32_bf16(__builtin_bit_cast(bfx2, u), __builtin_bit_cast(bfx2, 0x3f803f80u), acc, false); }
__device__ __forceinline__ float dot_self(unsigned u, float acc) {
    return __builtin_amdgcn_fdot2_f32_bf16(__builtin_bit_cast(bfx2, u), __builtin_bit_cast(bfx2, u), acc, false); }
__device__ __forceinline__ void transpose8x4(const u32x2 (&v)[8], u32x4 (&c)[4]) {
#pragma unroll
    for (int k = 0; k < 4; ++k) {
        c[0][k] = __builtin_amdgcn_perm(v[2 * k + 1][0], v[2 * k][0], 0x05040100u); c[1][k] = __builtin_amdgcn_perm(v[2 * k + 1][0], v[2 * k][0], 0x07060302u);
        c[2][k] = __builtin_amdgcn_perm(v[2 * k + 1][1], v[2 * k][1], 0x05040100u); c[3][k] = __builtin_amdgcn_perm(v[2 * k + 1][1], v[2 * k][1], 0x07060302u);
    }
}
template <int OFF, class T> __device__ __forceinline__ void gld8(T& d, const void* p) {
    asm volatile("global_load_dwordx2 %0, %1, off offset:%2" : "=v"(d) : "v"(p), "i"(OFF) : "memory"); }

constexpr int LDS_W = 34 * 1024, LDS_WO = 70 * 1024;
struct Pf { const char* src; unsigned dst; };
__device__ __forceinline__ Pf pf_qkv(char* ws, char* smem, int layer, int bid, int w, int lane) {
    const int ct = 8 * (bid & 1) + ((bid >> 3) & 7);
    return Pf{ws + (layer ? OFF_WTB : OFF_WTA) + (size_t)ct * WT_TILE + w * 2048 + lane * 16, lds_off(smem) + LDS_W + w * 2048};
}
__device__ __forceinline__ Pf pf_wo(char* ws, char* smem, int mode, int bid, int w, int lane) {
    const int jt = (bid >> 3) & 7;
    return Pf{ws + (mode ? OFF_WOB : OFF_WOA) + (size_t)jt * WO_TILE + w * 2048 + lane * 16, lds_off(smem) + LDS_WO + w * 2048};
}
#define PF(p, j) glds<2>((p).src + (j) * 16384, (p).dst + (j) * 16384)
#define PF_LAST(p, j, n) do { const int jj_ = w < (n) ? (j) : (j) - 1; PF(p, jj_); } while (0)
#define PFQ_LAST(p) PF_LAST(p, 7, 7)
#define PFO_LAST(p) PF_LAST(p, 5, 5)

__device__ __forceinline__ void lin_kv_part(char* ws, char* smem, int L, int panel, int ct, int w, int lane, const f32x4 (&acc)[7], const float (&rs4)[4], unsigned (&qt)[2]) {
    const int m = lane & 15, g = lane >> 4, tid = w * 64 + lane;
    {
        u32x2 fr[5];
#pragma unroll
        for (int c = 2; c < 7; ++c) {
            float v[4];
#pragma unroll
            for (int r = 0; r < 4; ++r) {
                float x = rs4[r] * acc[c][r];
                if (c < 4) x = phi_f(x);
                else if (c == 6) { const float p = phi_f(x); x = m < 8 ? p : x; x = m == 12 ? 1.f : x; }
                v[r] = x;
            }
            fr[c - 2] = (u32x2){pack2(v[0], v[1]), pack2(v[2], v[3])};
        }
        const bf16x4 k0 = __builtin_bit_cast(bf16x4, fr[0]), k1 = __builtin_bit_cast(bf16x4, fr[1]);
        const bf16x4 v0 = __builtin_bit_cast(bf16x4, fr[2]), v1 = __builtin_bit_cast(bf16x4, fr[3]), T = __builtin_bit_cast(bf16x4, fr[4]);
        const f32x4 z4 = {0.f, 0.f, 0.f, 0.f};
        const f32x4 d1 = MFMA16K16(k0, v0, z4), d2 = MFMA16K16(k1, v1, z4), d3 = MFMA16K16(T, v0, z4), d4 = MFMA16K16(T, v1, z4);
        const f32x4 d5 = MFMA16K16(k0, T, z4), d6 = MFMA16K16(k1, T, z4), d7 = MFMA16K16(T, T, z4);
        lds_ptr red = (lds_ptr)smem + (w ^ 4) * 5120;
        *(lds_f4)(red + (m * 20 + 4 * g) * 4) = d1;
        *(lds_f4)(red + (KV_HEAD + m * 20 + 4 * g) * 4) = d2;
        if (g == 1) {
            *(lds_f2)(red + (m * 20 + 16) * 4) = (f32x2){d3[0], d3[1]};
            *(lds_f2)(red + (KV_HEAD + m * 20 + 16) * 4) = (f32x2){d4[2], d4[3]};
        }
        if (m == 8 || m == 9 || m == 12) {
            const int e = m == 12 ? 18 : 8 + m;
            *(lds_f4)(red + (e * 20 + 4 * g) * 4) = d5;
            if (g == 1) *(lds_f2)(red + (e * 20 + 16) * 4) = (f32x2){d7[0], d7[1]};
        }
        if (m == 10 || m == 11 || m == 12) {
            const int e = m == 12 ? 18 : 6 + m;
            *(lds_f4)(red + (KV_HEAD + e * 20 + 4 * g) * 4) = d6;
            if (g == 1) *(lds_f2)(red + (KV_HEAD + e * 20 + 16) * 4) = (f32x2){d7[2], d7[3]};
        }
        {
            const unsigned o0 = fr[4][0], o1 = fr[4][1];
            const unsigned n0 = __builtin_amdgcn_mov_dpp(o0, 0xB1, 0xf, 0xf, true), n1 = __builtin_amdgcn_mov_dpp(o1, 0xB1, 0xf, 0xf, true);
            if (m == 0 || m == 2)
                *(lds_u4)(red + 4096 + (m >> 1) * 64 + g * 16) =
                    (u32x4){(o0 & 0xffffu) | (n0 << 16), (o0 >> 16) | (n0 & 0xffff0000u), (o1 & 0xffffu) | (n1 << 16), (o1 >> 16) | (n1 & 0xffff0000u)};
        }
        asm volatile("s_waitcnt lgkmcnt(0)\n\ts_barrier" ::: "memory");
        char* kvp = ws + OFF_KV + (size_t)(L * 16 + ct) * 16 * 3072;
        if (tid < 190) {
            f32x4 s4 = *(lds_f4)((lds_ptr)smem + tid * 16);
#pragma unroll
            for (int i = 1; i < 8; ++i) s4 += *(lds_f4)((lds_ptr)smem + i * 5120 + tid * 16);
            gst16c(kvp + panel * 3072 + tid * 16, __builtin_bit_cast(u32x4, s4));
        }
        qt[0] = *(lds_vu32)(red + 4096 + m * 4); qt[1] = *(lds_vu32)(red + 4096 + 64 + m * 4);
    }
}
__device__ __forceinline__ void lin_o_part(char* ws, char* smem, int L, int rb, int ct, int w, int lane, u32x4 (&bq)[2], const Polls& pl) {
    const int m = lane & 15, g = lane >> 4, tid = w * 64 + lane, h0 = 2 * ct;
    char* lay = ws + OFF_LAY + L * LAY_BYTES;
    char* kvp = ws + OFF_KV + (size_t)(L * 16 + ct) * 16 * 3072;
    const f32x4 z4 = {0.f, 0.f, 0.f, 0.f};
    {
        {
            f32x4 pa[3], pb[3];
            const char* pp = kvp + w * 6144 + lane * 16;
            gld16c<0>(pa[0], pp); gld16c<1024>(pa[1], pp); gld16c<2048>(pa[2], pp);
            gld16c<0>(pb[0], pp + 3072); gld16c<1024>(pb[1], pp + 3072); gld16c<2048>(pb[2], pp + 3072);
            asm volatile("s_waitcnt vmcnt(0)" : "+v"(pa[0]), "+v"(pa[1]), "+v"(pa[2]), "+v"(pb[0]), "+v"(pb[1]), "+v"(pb[2]), "+v"(bq[0]), "+v"(bq[1]) :: "memory");
            RETIRE(pl);
#pragma unroll
            for (int i = 0; i < 3; ++i) *(lds_f4)((lds_ptr)smem + w * 3072 + i * 1024 + lane * 16) = pa[i] + pb[i];
            asm volatile("s_waitcnt lgkmcnt(0)\n\ts_barrier" ::: "memory");
            if (tid < 190) {
                f32x4 s4 = *(lds_f4)((lds_ptr)smem + tid * 16);
#pragma unroll
                for (int i = 1; i < 8; ++i) s4 += *(lds_f4)((lds_ptr)smem + i * 3072 + tid * 16);
                *(lds_f4)((lds_ptr)smem + 24576 + tid * 16) = s4;
            }
            asm volatile("s_waitcnt lgkmcnt(0)\n\ts_barrier" ::: "memory");
        }
        f32x4 ka[2], kb[2]; f32x2 ka2[2], kb2[2];
        const int r3 = m & 3; const bool isz = r3 == 2;
        {
            lds_ptr p1 = (lds_ptr)smem + 24576 + m * 80, p2 = (lds_ptr)smem + 24576 + (isz ? 18 : 16 + (r3 & 1)) * 80;
#pragma unroll
            for (int h = 0; h < 2; ++h) {
                ka[h] = *(lds_f4)(p1 + h * KV_HEAD * 4 + 16 * g); ka2[h] = *(lds_f2)(p1 + h * KV_HEAD * 4 + 64);
                kb[h] = *(lds_f4)(p2 + h * KV_HEAD * 4 + 16 * g); kb2[h] = *(lds_f2)(p2 + h * KV_HEAD * 4 + 64);
            }
        }
        const bool use2 = isz || m < 2;
        unsigned om[2][2], ot[2];
#pragma unroll
        for (int h = 0; h < 2; ++h) {
            const u32x4 f1 = {pack2(ka[h][0], ka[h][1]), pack2(ka[h][2], ka[h][3]), g == 0 ? pack2(ka2[h][0], ka2[h][1]) : 0u, 0u};
            u32x4 f2 = {pack2(kb[h][0], kb[h][1]), pack2(kb[h][2], kb[h][3]), g == 0 ? pack2(kb2[h][0], kb2[h][1]) : 0u, 0u};
            if (!use2) f2 = (u32x4){0u, 0u, 0u, 0u};
            const bf16x8 b = __builtin_bit_cast(bf16x8, bq[h]);
            const f32x4 e1 = MFMA16(__builtin_bit_cast(bf16x8, f1), b, z4), e2 = MFMA16(__builtin_bit_cast(bf16x8, f2), b, z4);
            const float r = __builtin_amdgcn_rcpf(e2[2] + 1e-6f);
            om[h][0] = pack2(e1[0] * r, e1[1] * r); om[h][1] = pack2(e1[2] * r, e1[3] * r);
            ot[h] = pack2(e2[0] * r, e2[1] * r);
        }
        gst16c(lay + LO_O + ((size_t)(rb * 16 + ct) * 64 + lane) * 16, (u32x4){om[0][0], om[0][1], om[1][0], om[1][1]});
        {
            const unsigned a0 = __builtin_amdgcn_mov_dpp(ot[0], 0x00, 0xf, 0xf, true), a1 = __builtin_amdgcn_mov_dpp(ot[0], 0x55, 0xf, 0xf, true),
                           a2 = __builtin_amdgcn_mov_dpp(ot[0], 0xAA, 0xf, 0xf, true), a3 = __builtin_amdgcn_mov_dpp(ot[0], 0xFF, 0xf, 0xf, true);
            const unsigned b0 = __builtin_amdgcn_mov_dpp(ot[1], 0x00, 0xf, 0xf, true), b1 = __builtin_amdgcn_mov_dpp(ot[1], 0x55, 0xf, 0xf, true),
                           b2 = __builtin_amdgcn_mov_dpp(ot[1], 0xAA, 0xf, 0xf, true), b3 = __builtin_amdgcn_mov_dpp(ot[1], 0xFF, 0xf, 0xf, true);
            const bool h1 = r3 == 1;
            if (g == 0 && r3 < 2)
                gst16c(lay + LO_O + O_TAIL + ((size_t)(rb * 32 + h0 + r3) * 16 + (m & 12)) * 4, (u32x4){h1 ? b0 : a0, h1 ? b1 : a1, h1 ? b2 : a2, h1 ? b3 : a3});
        }
    }
}

template <int LAYER, bool FUSED>
__device__ __forceinline__ void qkv_phase(char* ws, char* smem, int L, int w, int lane) {
    LAUNDER();
    int m = lane & 15, g = lane >> 4; const int rp = w & 3, kh = w >> 2;
    const int xcd = bid & 7, idx = bid >> 3;
    const int panel = 4 * (xcd >> 1) + (idx >> 3), ct = 8 * (xcd & 1) + (idx & 7);
    const int rb = panel * 8 + 2 * rp + kh, rbo = panel * 8 + 2 * rp + 1 - kh;
    const int xv = 2 * L + LAYER;
    char* lay = ws + OFF_LAY + L * LAY_BYTES;
    f32x4 st[2], cs4[4]; float cs1[5];
    {
        const float* cs = (const float*)(ws + (LAYER ? OFF_CSB : OFF_CSA)) + ct * 112;
        const float* c4 = cs + 4 * g; const float* c1 = cs + m;
        if constexpr (LAYER == 0) {
            gld16<0>(cs4[0], c4); gld16<64>(cs4[1], c4);
            gld4<128>(cs1[0], c1); gld4<192>(cs1[1], c1); gld4<256>(cs1[2], c1); gld4<320>(cs1[3], c1); gld4<384>(cs1[4], c1);
        } else {
            gld16<0>(cs4[0], c4); gld16<64>(cs4[1], c4); gld16<128>(cs4[2], c4); gld16<192>(cs4[3], c4);
            gld4<256>(cs1[0], c1); gld4<320>(cs1[1], c1); gld4<384>(cs1[2], c1);
        }
    }
    if (!FUSED || xv == 0) {
        const Pf pf = pf_qkv(ws, smem, LAYER, bid, w, lane);
        PF(pf, 0); PF(pf, 1); PF(pf, 2); PF(pf, 3); PF(pf, 4); PF(pf, 5); PF(pf, 6); PFQ_LAST(pf);
    }
    const Polls pl = wait_cnt(ws, LAYER ? L * C_LAY + C_WA + panel : C_WB + panel, xv ? 16u : 0u);
    bf16x8 a[2][9];
    const char* xa0 = ws + OFF_XF + xv * XF_BYTES + (size_t)(rb * 18 + kh) * 1024 + lane * 16;
    const char* xa1 = ws + OFF_XF + xv * XF_BYTES + (size_t)(rbo * 18 + kh) * 1024 + lane * 16;
#define LDA(j) do { gld16c<((j) & 1) * 2048>(a[0][j], xa0 + ((j) >> 1) * 4096); gld16c<((j) & 1) * 2048>(a[1][j], xa1 + ((j) >> 1) * 4096); } while (0)
    {
        const char* sp = ws + OFF_ST + xv * ST_BYTES + (size_t)(rb * 16 + m) * 128 + g * 32;
        gld16c<0>(st[0], sp); gld16c<16>(st[1], sp);
    }
    LDA(0); LDA(1); LDA(2);
    const lds_cptr bl = (lds_cptr)smem + LDS_W + kh * 7 * 1024 + lane * 16;
    bf16x8 bb[2][7];
#pragma unroll
    for (int c = 0; c < 7; ++c) bb[0][c] = *(lds_f8)(bl + c * 1024);
    constexpr int NTR = LAYER == 0 ? 2 : 4;
    f32x4 acc[2][7];
#pragma unroll
    for (int c = 0; c < 7; ++c) { acc[0][c] = (f32x4){0.f, 0.f, 0.f, 0.f}; acc[1][c] = (f32x4){0.f, 0.f, 0.f, 0.f}; }
    float nmu = 0.f, rs = 0.f, nmu4[4] = {0.f, 0.f, 0.f, 0.f}, rs4[4] = {0.f, 0.f, 0.f, 0.f};
    auto stats = [&]() {
        if constexpr (LAYER == 0) asm volatile("" : "+v"(st[0]), "+v"(st[1]), "+v"(cs4[0]), "+v"(cs4[1]), "+v"(cs1[0]), "+v"(cs1[1]), "+v"(cs1[2]), "+v"(cs1[3]), "+v"(cs1[4]));
        else asm volatile("" : "+v"(st[0]), "+v"(st[1]), "+v"(cs4[0]), "+v"(cs4[1]), "+v"(cs4[2]), "+v"(cs4[3]), "+v"(cs1[0]), "+v"(cs1[1]), "+v"(cs1[2]));
        float s = rowsum4(st[0][0] + st[0][2] + st[1][0] + st[1][2]);
        const float mean = s * (1.f / 576.f);
        const float d0 = st[0][0] * (1.f / 40.f) - mean, d1 = st[0][2] * (1.f / 32.f) - mean;
        const float d2 = st[1][0] * (1.f / 40.f) - mean, d3 = st[1][2] * (1.f / 32.f) - mean;
        const float q = rowsum4(st[0][1] + st[0][3] + st[1][1] + st[1][3] + 40.f * (d0 * d0 + d2 * d2) + 32.f * (d1 * d1 + d3 * d3));
        nmu = -mean; rs = __builtin_amdgcn_rsqf(q * (1.f / 576.f) + 1e-5f);
#pragma unroll
        for (int r = 0; r < 4; ++r) { nmu4[r] = __shfl(nmu, 4 * g + r); rs4[r] = __shfl(rs, 4 * g + r); }
#pragma unroll
        for (int c = 0; c < NTR; ++c) cs4[c] *= nmu;
        SBAR();
    };
#define MM(j) do { _Pragma("unroll") for (int c = 0; c < 7; ++c) { \
            if ((j) < 8 && !(LAYER == 0 && c < 2 && (j) >= 3)) bb[((j) + 1) & 1][c] = *(lds_f8)(bl + (((j) + 1) * 14 + c) * 1024); \
            SBAR(); \
            if (LAYER == 0 && c < 2 && (j) >= 4) {} else \
            if (c < NTR) { acc[1][c] = MFMA16(bb[(j) & 1][c], a[1][j], acc[1][c]); acc[0][c] = MFMA16(bb[(j) & 1][c], a[0][j], acc[0][c]); } \
            else         { acc[1][c] = MFMA16(a[1][j], bb[(j) & 1][c], acc[1][c]); acc[0][c] = MFMA16(a[0][j], bb[(j) & 1][c], acc[0][c]); } \
            SBAR(); \
        } } while (0)
#define WAITA(j, N) asm volatile("s_waitcnt vmcnt(" #N ")" : "+v"(a[0][j]), "+v"(a[1][j]) :: "memory")
    SBAR();
    WAITA(0, 4); RETIRE(pl); LDA(3); LDA(4); MM(0);
    WAITA(1, 6); LDA(5); LDA(6); MM(1);
    if (kh == 0) stats();
    WAITA(2, 8); LDA(7); LDA(8); MM(2);
    WAITA(3, 10); MM(3);
    WAITA(4, 8); MM(4);
    asm volatile("s_barrier" ::: "memory");
    WAITA(5, 6); MM(5);
    if (kh == 1) stats();
    WAITA(6, 4); MM(6);
    WAITA(7, 2); MM(7);
    WAITA(8, 0); MM(8);
#undef MM
#undef WAITA
#undef LDA
    asm volatile("" : "+v"(lane)); m = lane & 15; g = lane >> 4;
    constexpr int C0 = LAYER == 0 ? 2 : 0, SLOT = (7 - C0) * 1024;
    {
        lds_ptr ex = (lds_ptr)smem + w * SLOT + lane * 16;
#pragma unroll
        for (int c = C0; c < 7; ++c) *(lds_f4)(ex + (c - C0) * 1024) = acc[1][c];
        asm volatile("s_waitcnt lgkmcnt(0)\n\ts_barrier" ::: "memory");
        lds_ptr ey = (lds_ptr)smem + (w ^ 4) * SLOT + lane * 16;
#pragma unroll
        for (int c = C0; c < 7; ++c) {
            acc[0][c] += *(lds_f4)(ey + (c - C0) * 1024);
            if (c < NTR) acc[0][c] += cs4[c];
            else { const float cv = cs1[c - NTR]; acc[0][c] += (f32x4){nmu4[0] * cv, nmu4[1] * cv, nmu4[2] * cv, nmu4[3] * cv}; }
        }
    }
    const int h0 = 2 * ct;
    if constexpr (LAYER == 0) {
        unsigned qt[2];
        lin_kv_part(ws, smem, L, panel, ct, w, lane, acc[0], rs4, qt);
        const lds_cptr bl2 = (lds_cptr)smem + LDS_W + kh * 7 * 1024 + lane * 16;
#pragma unroll
        for (int j = 4; j < 9; ++j) {
            const bf16x8 b0 = *(lds_f8)(bl2 + (j * 14) * 1024), b1 = *(lds_f8)(bl2 + (j * 14 + 1) * 1024);
            acc[1][0] = MFMA16(b0, a[1][j], acc[1][0]); acc[0][0] = MFMA16(b0, a[0][j], acc[0][0]);
            acc[1][1] = MFMA16(b1, a[1][j], acc[1][1]); acc[0][1] = MFMA16(b1, a[0][j], acc[0][1]);
            if (j == 5) signal_cnt<0>(ws, L * C_LAY + C_QA + ct);
        }
        lds_ptr e2 = (lds_ptr)smem + 40960 + w * 2048 + lane * 16;
        *(lds_f4)(e2) = acc[1][0]; *(lds_f4)(e2 + 1024) = acc[1][1];
        asm volatile("s_waitcnt lgkmcnt(0)\n\ts_barrier" ::: "memory");
        const Pf pf = pf_wo(ws, smem, 0, bid, w, lane);
        if (FUSED) { PF(pf, 0); PF(pf, 1); PF(pf, 2); }
        u32x4 bq[2];
        lds_ptr e3 = (lds_ptr)smem + 40960 + (w ^ 4) * 2048 + lane * 16;
#pragma unroll
        for (int c = 0; c < 2; ++c) {
            const f32x4 t = acc[0][c] + *(lds_f4)(e3 + c * 1024) + cs4[c];
            bq[c] = (u32x4){pack2(phi_f(rs * t[0]), phi_f(rs * t[1])), pack2(phi_f(rs * t[2]), phi_f(rs * t[3])), qt[c], 0u};
        }
        const Polls pl2 = wait_cnt(ws, L * C_LAY + C_QA + ct, 16);
        lin_o_part(ws, smem, L, rb, ct, w, lane, bq, pl2);
        if (FUSED) { PF(pf, 3); PF(pf, 4); signal_cnt<4>(ws, L * C_LAY + C_OA + panel); PFO_LAST(pf); }
        else signal_cnt<0>(ws, L * C_LAY + C_OA + panel);
    } else {
        const int tok = rb * 16 + m;
        char* img = lay + LO_IMG + (size_t)h0 * IMG_HEAD + (size_t)(rb >> 4) * IMG_CHUNK;
        const float rq = rs * QSCALE;
#pragma unroll
        for (int p = 0; p < 2; ++p) {
            const float sc = p == 0 ? rq : rs;
            const f32x4 A = acc[0][2 * p], B = acc[0][2 * p + 1];
            auto s0 = __builtin_amdgcn_permlane16_swap(pack2(sc * A[0], sc * A[1]), pack2(sc * B[0], sc * B[1]), false, false);
            auto s1 = __builtin_amdgcn_permlane16_swap(pack2(sc * A[2], sc * A[3]), pack2(sc * B[2], sc * B[3]), false, false);
            const u32x4 d = {s0[0], s1[0], s0[1], s1[1]};
            if (p == 0) gst16c(lay + LO_Q1 + ((size_t)(h0 + (g & 1)) * SEQ + tok) * 32 + (g >> 1) * 16, d);
            else gst16c(img + (g & 1) * IMG_HEAD + ((tok & 255) >> 5) * 1024 + (g >> 1) * 512 + (tok & 31) * 16, d);
        }
        {
            const f32x4 A = acc[0][4], B = acc[0][5];
            auto s0 = __builtin_amdgcn_permlane32_swap(pack2(rs4[0] * A[0], rs4[1] * A[1]), pack2(rs4[0] * B[0], rs4[1] * B[1]), false, false);
            auto s1 = __builtin_amdgcn_permlane32_swap(pack2(rs4[2] * A[2], rs4[3] * A[3]), pack2(rs4[2] * B[2], rs4[3] * B[3]), false, false);
            gst16c(img + (g >> 1) * IMG_HEAD + 9216 + ((rb & 15) * 2 + (g & 1)) * 320 + m * 16, (u32x4){s0[0], s1[0], s0[1], s1[1]});
        }
        {
            const float qs = m < 4 ? QSCALE : 1.f;
            const f32x4 A = acc[0][6];
            const unsigned o0 = pack2(qs * rs4[0] * A[0], qs * rs4[1] * A[1]), o1 = pack2(qs * rs4[2] * A[2], qs * rs4[3] * A[3]);
            const unsigned n0 = __builtin_amdgcn_mov_dpp(o0, 0xB1, 0xf, 0xf, true), n1 = __builtin_amdgcn_mov_dpp(o1, 0xB1, 0xf, 0xf, true);
            auto u0 = __builtin_amdgcn_permlane32_swap(o0, o0, false, false); auto u1 = __builtin_amdgcn_permlane32_swap(o1, o1, false, false);
            const int hsel = (m >> 1) & 1, tok0 = rb * 16 + 4 * g;
            u32x4 d; char* p;
            if (m < 8) {
                d = (u32x4){(o0 & 0xffffu) | (n0 << 16), (o0 >> 16) | (n0 & 0xffff0000u), (o1 & 0xffffu) | (n1 << 16), (o1 >> 16) | (n1 & 0xffff0000u)};
                p = m < 4 ? lay + LO_Q2 + ((size_t)(h0 + hsel) * SEQ + tok0) * 4 : img + hsel * IMG_HEAD + 8192 + (tok0 & 255) * 4;
            } else {
                const unsigned cst = (m & 1) ? 0u : 0x3f803f80u;
                d = m < 12 ? (u32x4){o0, o1, u0[1], u1[1]} : (u32x4){cst, cst, cst, cst};
                p = img + hsel * IMG_HEAD + 9216 + ((rb & 15) * 2 + (g & 1)) * 320 + (16 + (m & 1) + ((m >> 1) & 2)) * 16;
            }
            if (m < 8 ? !(m & 1) : g < 2) gst16c(p, d);
        }
        signal_cnt<0>(ws, L * C_LAY + C_QB + ct);
    }
}

template <bool FUSED>
__device__ __forceinline__ void wo_phase(char* ws, char* smem, const unsigned short* x0, unsigned short* out, int L, int mode, int w, int lane) {
    LAUNDER();
    const int m = lane & 15, g = lane >> 4, rb4 = w & 3, kh = w >> 2;
    const int xcd = bid & 7, idx = bid >> 3;
    const int panel = 4 * xcd + (idx >> 3), jt = idx & 7;
    const int rb = panel * 4 + rb4;
    const int xv = 2 * L + mode;
    const char* ob = ws + OFF_LAY + L * LAY_BYTES + LO_O + mode * O_BYTES;
    const bool last = xv == 3;
    const bool dopf = FUSED && !last;
    const Pf pf = pf_qkv(ws, smem, mode ? 0 : 1, bid, w, lane);
    if (!FUSED) { const Pf po = pf_wo(ws, smem, mode, bid, w, lane); PF(po, 0); PF(po, 1); PF(po, 2); PF(po, 3); PF(po, 4); PFO_LAST(po); }
    u32x2 xr[3];
#pragma unroll
    for (int c = 0; c < 3; ++c) {
        const int cb = c == 2 ? 4 : 2 * kh + c, gg = c == 2 ? (g & 1) : g;
        const int n0 = 72 * jt + 16 * cb + 4 * gg;
        const char* xp = xv == 0 ? (const char*)(x0 + (size_t)(rb * 16 + m) * 576 + n0)
                                 : ws + OFF_XF + xv * XF_BYTES + ((size_t)(rb * 18 + (n0 >> 5)) * 64 + ((n0 >> 3) & 3) * 16 + m) * 16 + ((n0 >> 2) & 1) * 8;
        gld8c<0>(xr[c], xp);
    }
    if (dopf) { PF(pf, 0); PF(pf, 1); }
    const Polls pl = wait_cnt(ws, L * C_LAY + (mode ? C_AT + (panel >> 2) : C_OA + (panel >> 1)), mode ? 32u : 16u);
    asm volatile("" : "+v"(xr[0]), "+v"(xr[1]), "+v"(xr[2]));
    bf16x8 af[9]; unsigned pt[4];
    {
        const char* op = ob + ((size_t)(rb * 16 + 8 * kh) * 64 + lane) * 16;
        gld16c<0>(af[0], op); gld16c<1024>(af[1], op); gld16c<2048>(af[2], op); gld16c<3072>(af[3], op);
        gld16c<0>(af[4], op + 4096); gld16c<1024>(af[5], op + 4096); gld16c<2048>(af[6], op + 4096); gld16c<3072>(af[7], op + 4096);
        const char* tp = ob + O_TAIL + ((size_t)(rb * 32 + 16 * kh + 4 * g) * 16 + m) * 4;
        gld4c<0>(pt[0], tp); gld4c<64>(pt[1], tp); gld4c<128>(pt[2], tp); gld4c<192>(pt[3], tp);
    }
    f32x4 acc[5];
#pragma unroll
    for (int c = 0; c < 5; ++c) acc[c] = (f32x4){0.f, 0.f, 0.f, 0.f};
    const lds_cptr bl = (lds_cptr)smem + LDS_WO + kh * 5 * 1024 + lane * 16;
    bf16x8 bb[3][5];
#pragma unroll
    for (int c = 0; c < 5; ++c) { bb[0][c] = *(lds_f8)(bl + c * 1024); bb[1][c] = *(lds_f8)(bl + (10 + c) * 1024); }
#define KSTEP(i_) do { _Pragma("unroll") for (int c = 0; c < 5; ++c) { \
        if ((i_) < 7) bb[((i_) + 2) % 3][c] = *(lds_f8)(bl + (((i_) + 2) * 10 + c) * 1024); \
        SBAR(); acc[c] = MFMA16(bb[(i_) % 3][c], af[i_], acc[c]); SBAR(); } } while (0)
#define WAITF(i_, N) asm volatile("s_waitcnt vmcnt(" #N ")" : "+v"(af[i_]) :: "memory")
    SBAR();
    WAITF(0, 11); RETIRE(pl); KSTEP(0); WAITF(1, 10); KSTEP(1); WAITF(2, 9); KSTEP(2); WAITF(3, 8); KSTEP(3);
    WAITF(4, 7); KSTEP(4); WAITF(5, 6); KSTEP(5); WAITF(6, 5); KSTEP(6); WAITF(7, 4); KSTEP(7);
    asm volatile("s_waitcnt vmcnt(0)" : "+v"(pt[0]), "+v"(pt[1]), "+v"(pt[2]), "+v"(pt[3]) :: "memory");
    af[8] = __builtin_bit_cast(bf16x8, (u32x4){pt[0], pt[1], pt[2], pt[3]});
    KSTEP(8);
#undef KSTEP
#undef WAITF
    lds_ptr ex = (lds_ptr)smem + rb4 * 5 * 1024 + lane * 16;
    if (kh == 0) { *(lds_f4)(ex + 2 * 1024) = acc[2]; *(lds_f4)(ex + 3 * 1024) = acc[3]; }
    else { *(lds_f4)(ex) = acc[0]; *(lds_f4)(ex + 1024) = acc[1]; *(lds_f4)(ex + 4 * 1024) = acc[4]; }
    asm volatile("s_waitcnt lgkmcnt(0)\n\ts_barrier" ::: "memory");
    unsigned pk[3][2];
    {
        const f32x4 o0 = (kh ? acc[2] : acc[0]) + *(lds_f4)(ex + (2 * kh) * 1024), o1 = (kh ? acc[3] : acc[1]) + *(lds_f4)(ex + (2 * kh + 1) * 1024);
        const f32x4 o2 = acc[4] + *(lds_f4)(ex + 4 * 1024);
        pk[0][0] = pack2(o0[0] + bf_lo(xr[0][0]), o0[1] + bf_hi(xr[0][0])); pk[0][1] = pack2(o0[2] + bf_lo(xr[0][1]), o0[3] + bf_hi(xr[0][1]));
        pk[1][0] = pack2(o1[0] + bf_lo(xr[1][0]), o1[1] + bf_hi(xr[1][0])); pk[1][1] = pack2(o1[2] + bf_lo(xr[1][1]), o1[3] + bf_hi(xr[1][1]));
        pk[2][0] = pack2(o2[0] + bf_lo(xr[2][0]), o2[1] + bf_hi(xr[2][0])); pk[2][1] = pack2(o2[2] + bf_lo(xr[2][1]), o2[3] + bf_hi(xr[2][1]));
    }
    {
        auto s0 = __builtin_amdgcn_permlane16_swap(pk[0][0], pk[1][0], false, false), s1 = __builtin_amdgcn_permlane16_swap(pk[0][1], pk[1][1], false, false);
        auto t0 = __builtin_amdgcn_permlane16_swap(pk[2][0], pk[2][0], false, false), t1 = __builtin_amdgcn_permlane16_swap(pk[2][1], pk[2][1], false, false);
        const u32x4 d = {s0[0], s1[0], s0[1], s1[1]}, e = {t0[0], t1[0], t0[1], t1[1]};
        const int n0 = 72 * jt + 32 * kh + 16 * (g & 1) + 8 * (g >> 1), n1 = 72 * jt + 64;
        if (last) {
            typedef __attribute__((address_space(1))) u32x4* g_u4;
            unsigned short* orow = out + (size_t)(rb * 16 + m) * 576;
            *(g_u4)(orow + n0) = d;
            if (kh == 0 && g == 0) *(g_u4)(orow + n1) = e;
        } else {
            char* xo = ws + OFF_XF + (xv + 1) * XF_BYTES + (size_t)rb * 18 * 1024 + m * 16;
            gst16c(xo + (n0 >> 5) * 1024 + ((n0 >> 3) & 3) * 256, d);
            if (kh == 0 && g == 0) gst16c(xo + (n1 >> 5) * 1024 + ((n1 >> 3) & 3) * 256, e);
        }
    }
    if (!last) {
        const bool v2 = kh == 0 && g < 2;
        float v[12];
#pragma unroll
        for (int c = 0; c < 3; ++c) { v[4 * c] = bf_lo(pk[c][0]); v[4 * c + 1] = bf_hi(pk[c][0]); v[4 * c + 2] = bf_lo(pk[c][1]); v[4 * c + 3] = bf_hi(pk[c][1]); }
        float s = ((v[0] + v[1]) + (v[2] + v[3])) + ((v[4] + v[5]) + (v[6] + v[7])) + (v2 ? (v[8] + v[9]) + (v[10] + v[11]) : 0.f);
        s = rowsum4(s);
        const float mean = s * (kh ? (1.f / 32.f) : (1.f / 40.f));
        float q = 0.f, q2 = 0.f;
#pragma unroll
        for (int r = 0; r < 8; ++r) { const float dd = v[r] - mean; q += dd * dd; }
#pragma unroll
        for (int r = 8; r < 12; ++r) { const float dd = v[r] - mean; q2 += dd * dd; }
        q += v2 ? q2 : 0.f;
        q = rowsum4(q);
        if (g == 0) gst8c(ws + OFF_ST + (xv + 1) * ST_BYTES + (size_t)(rb * 16 + m) * 128 + jt * 16 + kh * 8, (f32x2){s, q});
        const int line = L * C_LAY + (mode ? C_WB : C_WA) + (panel >> 1);
        if (FUSED) { PF(pf, 2); PF(pf, 3); signal_cnt<4>(ws, line); PF(pf, 4); PF(pf, 5); PF(pf, 6); PFQ_LAST(pf); }
        else signal_cnt<0>(ws, line);
    }
}

__device__ __forceinline__ float half_max(float v) {
    auto r = __builtin_amdgcn_permlane32_swap(__float_as_uint(v), __float_as_uint(v), false, false);
    return fmaxf(__uint_as_float(r[0]), __uint_as_float(r[1]));
}
#define STR_(x) #x
#define STR(x) STR_(x)
#define AEXP(r) "v_exp_f32 v" STR(r) ", v" STR(r) "\n\t"
#define ACVT(d, r0, r1) "v_cvt_pk_bf16_f32 v" STR(d) ", v" STR(r0) ", v" STR(r1) "\n\t"
#define AE4(r0, r1, r2, r3, d0, d1) AEXP(r0) AEXP(r1) AEXP(r2) AEXP(r3) ACVT(d0, r0, r1) ACVT(d1, r2, r3)
#define ATILE(KO, K2O, V1O, V0O, XK, X0, X1, XV) \
    "v_mfma_f32_32x32x16_bf16 v[48:63], v[80:83], v[72:75], 0\n\t" XK \
    "ds_read_b128 v[80:83], v104 offset:" STR(KO) "\n\t" \
    AE4(32, 33, 34, 35, 96, 97) \
    "v_mfma_f32_32x32x16_bf16 v[16:31], v[92:95], v[100:103], v[16:31]\n\t" \
    "ds_read_b128 v[92:95], v106 offset:" STR(V1O) "\n\t" X0 \
    AE4(36, 37, 38, 39, 98, 99) \
    "v_mfma_f32_32x32x16_bf16 v[48:63], v[84:87], v[76:79], v[48:63]\n\t" \
    "ds_read_b32 v84, v105 offset:" STR(K2O) "\n\t" \
    AE4(40, 41, 42, 43, 100, 101) \
    "s_waitcnt lgkmcnt(3)\n\t" \
    "v_mfma_f32_32x32x16_bf16 v[0:15], v[88:91], v[96:99], v[0:15]\n\t" \
    AE4(44, 45, 46, 47, 102, 103) \
    "s_waitcnt lgkmcnt(1)\n\t" \
    "v_mfma_f32_32x32x16_bf16 v[32:47], v[80:83], v[64:67], 0\n\t" \
    AE4(48, 49, 50, 51, 96, 97) \
    "v_mfma_f32_32x32x16_bf16 v[0:15], v[92:95], v[100:103], v[0:15]\n\t" X1 \
    AE4(52, 53, 54, 55, 98, 99) \
    "s_waitcnt lgkmcnt(0)\n\t" \
    "v_mfma_f32_32x32x16_bf16 v[32:47], v[84:87], v[68:71], v[32:47]\n\t" \
    AE4(56, 57, 58, 59, 100, 101) \
    "v_mfma_f32_32x32x16_bf16 v[16:31], v[88:91], v[96:99], v[16:31]\n\t" XV \
    "ds_read_b128 v[88:91], v106 offset:" STR(V0O) "\n\t" \
    AE4(60, 61, 62, 63, 102, 103)
#define ACHUNK \
    "s_mov_b32 m0, s22\n\ts_add_u32 s28, s20, 0x2000\n\ts_addc_u32 s29, s21, 0\n\tglobal_load_lds_dwordx4 v107, s[20:21] sc1\n\t" \
    "s_add_u32 s27, s22, 0x2000\n\ts_mov_b32 m0, s27\n\ts_cmp_lt_u32 %[w], 3\n\tglobal_load_lds_dwordx4 v107, s[28:29] sc1\n\t" \
    "s_cbranch_scc0 7f\n\t" \
    "s_add_u32 s27, s22, 0x4000\n\ts_mov_b32 m0, s27\n\ts_add_u32 s28, s20, 0x4000\n\ts_addc_u32 s29, s21, 0\n\tglobal_load_lds_dwordx4 v107, s[28:29] sc1\n\t" \
    "7:\n\t" \
    "s_add_u32 s20, s20, 0x4c00\n\ts_addc_u32 s21, s21, 0\n\t"
#define AWPF \
    "s_mov_b32 m0, s25\n\ts_add_u32 s25, s25, 0x4000\n\tglobal_load_lds_dwordx4 v107, s[30:31]\n\tglobal_load_lds_dwordx4 v107, s[30:31] offset:1024\n\t" \
    "s_add_u32 s30, s30, 0x4000\n\ts_addc_u32 s31, s31, 0\n\t"
#define AKADV "v_add_u32 v104, s23, v104\n\tv_add_u32 v105, s23, v105\n\t"
#define AVADV "v_add_u32 v106, s23, v106\n\t"

template <bool FUSED>
__device__ __forceinline__ void attn_phase(char* ws, char* smem, int L, int w, int lane) {
    LAUNDER();
    const int r32 = lane & 31, hh = lane >> 5, kh = w >> 2;
    const int xcd = bid & 7, idx = bid >> 3;
    const int head = 4 * xcd + (idx >> 3), qb = idx & 7;
    const int qA = qb * 256 + (w & 3) * 64 + r32;
    char* lay = ws + OFF_LAY + L * LAY_BYTES;
    const unsigned lds0 = lds_off(smem) + w * 1024;
    const Polls pl = wait_cnt(ws, L * C_LAY + C_QB + (head >> 1), 16);
    bf16x8 q1a, q1b; unsigned q2wa, q2wb;
    {
        const char* qp = lay + LO_Q1 + ((size_t)head * SEQ + qA) * 32 + hh * 16;
        const char* q2p = lay + LO_Q2 + ((size_t)head * SEQ + qA) * 4;
        gld16c<0>(q1a, qp); gld16c<1024>(q1b, qp); gld4c<0>(q2wa, q2p); gld4c<128>(q2wb, q2p);
    }
    const char* srcw = lay + LO_IMG + (size_t)head * IMG_HEAD + w * 1024;
    {
        const char* src = srcw + lane * 16;
#pragma unroll
        for (int k = 0; k < 4; ++k) glds1_sc1(src + k * 8192, lds0 + k * 8192);
        glds1_sc1(src + (w < 6 ? 4 : 3) * 8192, lds0 + (w < 6 ? 4 : 3) * 8192);
    }
    lds_cptr kp1 = (lds_cptr)smem + lane * 16;
    lds_cptr kp2 = (lds_cptr)smem + 8192 + r32 * 4;
    const int dmap = r32 < 19 ? r32 : 19;
    lds_cptr vp = (lds_cptr)smem + 9216 + hh * 320 + dmap * 16;
    asm volatile("s_waitcnt vmcnt(2)\n\ts_barrier" : "+v"(q1a), "+v"(q1b), "+v"(q2wa), "+v"(q2wb) :: "memory");
    RETIRE(pl);
#define LDK1(t) (*(lds_f8)(kp1 + (t) * 1024))
#define LDK2(t) (__builtin_bit_cast(bf16x8, (u32x4){*(lds_vu32)(kp2 + (t) * 128), 0x3f80u, 0u, 0u}))
#define LDV(u) (*(lds_f8)(vp + (u) * 640))
    const f32x16 z16 = {};
    f32x16 s0, s1, oa = {}, ob = {};
    bf16x8 k1 = LDK1(0), k2 = LDK2(0), v0, v1, q2a, q2b;
    u32x4 p0, p1 = {0u, 0u, 0u, 0u};
    {
        q2a = __builtin_bit_cast(bf16x8, (u32x4){hh ? 0u : q2wa, 0u, 0u, 0u});
        q2b = __builtin_bit_cast(bf16x8, (u32x4){hh ? 0u : q2wb, 0u, 0u, 0u});
        f32x16 sa = MFMA32(k1, q1a, z16), sb = MFMA32(k1, q1b, z16); sa = MFMA32(k2, q2a, sa); sb = MFMA32(k2, q2b, sb);
        kp1 += kh * 4096; kp2 += kh * 512; vp += kh * 5120;
        k1 = LDK1(0); k2 = LDK2(0); v0 = LDV(0); v1 = v0;
        float ma = fmaxf(sa[0], sa[1]), mb = fmaxf(sb[0], sb[1]);
#pragma unroll
        for (int i = 2; i < 16; ++i) { ma = fmaxf(ma, sa[i]); mb = fmaxf(mb, sb[i]); }
        ma = half_max(ma); mb = half_max(mb);
        q2a = __builtin_bit_cast(bf16x8, (u32x4){hh ? 0u : q2wa, hh ? 0u : (pack2(-ma, 0.f) & 0xffffu), 0u, 0u});
        q2b = __builtin_bit_cast(bf16x8, (u32x4){hh ? 0u : q2wb, hh ? 0u : (pack2(-mb, 0.f) & 0xffffu), 0u, 0u});
    }
    s0 = MFMA32(k1, q1a, z16); s0 = MFMA32(k2, q2a, s0);
    {
        unsigned a1 = (unsigned)(size_t)kp1, a2 = (unsigned)(size_t)kp2, a3 = (unsigned)(size_t)vp;
        const unsigned loff = lane * 16;
        const char* dsrc = srcw + 2 * IMG_CHUNK; const unsigned dbase = lds0;
        const int jt = (bid >> 3) & 7;
        const char* wsrc = ws + OFF_WOB + (size_t)jt * WO_TILE + w * 2048; const unsigned wdst = lds_off(smem) + LDS_WO + w * 2048;
        asm volatile(
            "s_mov_b32 s26, m0\n\t"
            "s_mov_b64 s[20:21], %[dsrc]\n\t"
            "s_add_u32 s22, %[dbase], 0x9800\n\t"
            "s_mov_b64 s[30:31], %[wsrc]\n\t"
            "s_mov_b32 s25, %[wdst]\n\t"
            "s_mov_b32 s24, 0\n\t"
            "s_mov_b32 s33, 0\n\t"
            "s_mov_b32 s34, 0x4c00\n\t"
            "s_waitcnt lgkmcnt(0)\n\t"
            "s_nop 7\n\t"
            "s_nop 3\n\t"
            "2:\n\t"
            "s_setprio 2\n\t"
            ATILE(1024, 128, 640, 1280, "", "", "", "")
            "s_setprio 1\n\t"
            "s_cmp_eq_u32 s33, 2\n\t"
            "s_cselect_b32 s23, 0xffff6800, s34\n\t"
            "s_sub_u32 s35, s24, 1\n\t"
            "s_cmp_lt_u32 s35, %[npf]\n\t"
            "s_cbranch_scc0 3f\n\t"
            AWPF
            "3:\n\t"
            ATILE(2048, 256, 1920, 2560, "", "", "", "")
            "s_setprio 0\n\t"
            ATILE(3072, 384, 3200, 3840, "", "", "", "")
            "s_waitcnt vmcnt(2)\n\t"
            "s_cmp_lt_u32 s35, %[npf]\n\t"
            "s_cbranch_scc1 4f\n\t"
            "s_waitcnt vmcnt(0)\n\t"
            "4:\n\t"
            "s_barrier\n\t"
            "s_cmp_lt_u32 s24, 6\n\t"
            "s_cbranch_scc0 5f\n\t"
            ACHUNK
            "s_cmp_eq_u32 s33, 0\n\t"
            "s_cselect_b32 s27, 0xffff6800, s34\n\t"
            "s_add_u32 s22, s22, s27\n\t"
            "5:\n\t"
            "s_setprio 3\n\t"
            ATILE(0, 0, 4480, 0, AKADV, "", "", AVADV)
            "s_add_u32 s24, s24, 1\n\t"
            "s_add_u32 s33, s33, 1\n\t"
            "s_cmp_eq_u32 s33, 3\n\t"
            "s_cselect_b32 s33, 0, s33\n\t"
            "s_cmp_lt_u32 s24, 8\n\t"
            "s_cbranch_scc1 2b\n\t"
            "s_waitcnt lgkmcnt(0)\n\t"
            "v_mfma_f32_32x32x16_bf16 v[16:31], v[92:95], v[100:103], v[16:31]\n\t"
            "s_mov_b32 m0, s26\n\t"
            "s_setprio 0\n\t"
            "s_nop 7\n\t"
            "s_nop 3\n\t"
            : "+{v[0:15]}"(oa), "+{v[16:31]}"(ob), "+{v[32:47]}"(s0), "={v[48:63]}"(s1), "+{v[80:83]}"(k1), "+{v[84:87]}"(k2), "+{v[88:91]}"(v0), "+{v[92:95]}"(v1),
              "={v[96:99]}"(p0), "+{v[100:103]}"(p1), "+{v104}"(a1), "+{v105}"(a2), "+{v106}"(a3)
            : "{v[64:67]}"(q1a), "{v[68:71]}"(q2a), "{v[72:75]}"(q1b), "{v[76:79]}"(q2b), "{v107}"(loff), [dsrc] "s"(dsrc), [dbase] "s"(dbase),
              [wsrc] "s"(wsrc), [wdst] "s"(wdst), [w] "s"(w), [npf] "s"(FUSED ? (w < 5 ? 6 : 5) : 0)
            : "s20", "s21", "s22", "s23", "s24", "s25", "s26", "s27", "s28", "s29", "s30", "s31", "s33", "s34", "s35", "scc", "memory");
    }
    lds_ptr ex = (lds_ptr)smem + (w < 6 ? w * 3072 : 2 * (int)IMG_CHUNK + 12288 + (w - 6) * 3072) + lane * 16;
    const int wo_ = w ^ 4;
    lds_ptr ey = (lds_ptr)smem + (wo_ < 6 ? wo_ * 3072 : 2 * (int)IMG_CHUNK + 12288 + (wo_ - 6) * 3072) + lane * 16;
    f32x16 o;
    {
        const f32x16 snd = kh ? oa : ob; o = kh ? ob : oa;
        *(lds_f4)(ex) = (f32x4){snd[0], snd[1], snd[2], snd[3]}; *(lds_f4)(ex + 1024) = (f32x4){snd[4], snd[5], snd[6], snd[7]};
        *(lds_f4)(ex + 2048) = (f32x4){snd[8], snd[9], snd[10], snd[11]};
    }
    asm volatile("s_waitcnt lgkmcnt(0)\n\ts_barrier" ::: "memory");
    {
        const f32x4 a = *(lds_f4)(ey), b = *(lds_f4)(ey + 1024), c = *(lds_f4)(ey + 2048);
#pragma unroll
        for (int i = 0; i < 4; ++i) { o[i] += a[i]; o[4 + i] += b[i]; o[8 + i] += c[i]; }
    }
    const int q = qA + 32 * kh;
    bool bad = !(fabsf(o[10]) < 1.0e30f);
#pragma unroll
    for (int i = 0; i < 10; ++i) bad = bad || !(fabsf(o[i]) < 1.0e38f);
    if (__any(bad)) {
        o = (f32x16){};
        float mrun = -INFINITY;
        const bf16x8 q1 = kh ? q1b : q1a;
        const bf16x8 q2 = __builtin_bit_cast(bf16x8, (u32x4){hh ? 0u : (kh ? q2wb : q2wa), 0u, 0u, 0u});
        const char* img = lay + LO_IMG + (size_t)head * IMG_HEAD;
#pragma unroll 1
        for (int t = 0; t < 64; ++t) {
            const char* cb = img + (t >> 3) * IMG_CHUNK; const int tt = t & 7;
            const char* vb = cb + 9216 + tt * 1280 + hh * 320 + dmap * 16;
            bf16x8 fk, fv0, fv1; unsigned fk2;
            gld16c<0>(fk, cb + tt * 1024 + lane * 16); gld4c<0>(fk2, cb + 8192 + tt * 128 + r32 * 4); gld16c<0>(fv0, vb); gld16c<640>(fv1, vb);
            asm volatile("s_waitcnt vmcnt(0)" : "+v"(fk), "+v"(fk2), "+v"(fv0), "+v"(fv1) :: "memory");
            f32x16 s = MFMA32(fk, q1, z16);
            s = MFMA32(__builtin_bit_cast(bf16x8, (u32x4){fk2, 0u, 0u, 0u}), q2, s);
            float mx = fmaxf(s[0], s[1]);
#pragma unroll
            for (int i = 2; i < 16; ++i) mx = fmaxf(mx, s[i]);
            mx = fmaxf(half_max(mx), mrun);
            const float al = EXP2(mrun - mx);
            mrun = mx;
#pragma unroll
            for (int i = 0; i < 16; ++i) o[i] *= al;
#pragma unroll
            for (int i = 0; i < 4; ++i) {
                p0[i] = pack2(EXP2(s[2 * i] - mx), EXP2(s[2 * i + 1] - mx));
                p1[i] = pack2(EXP2(s[8 + 2 * i] - mx), EXP2(s[8 + 2 * i + 1] - mx));
            }
            o = MFMA32(fv0, __builtin_bit_cast(bf16x8, p0), o);
            o = MFMA32(fv1, __builtin_bit_cast(bf16x8, p1), o);
        }
    }
    {
        auto r = __builtin_amdgcn_permlane32_swap(__float_as_uint(o[10]), __float_as_uint(o[10]), false, false);
        const float rl = __builtin_amdgcn_rcpf(__uint_as_float(r[0]));
        const int rb = q >> 4, m = q & 15;
        gst16c(lay + LO_O + O_BYTES + ((size_t)(rb * 16 + (head >> 1)) * 64 + (2 * (head & 1) + hh) * 16 + m) * 16,
               (u32x4){pack2(o[0] * rl, o[1] * rl), pack2(o[2] * rl, o[3] * rl), pack2(o[4] * rl, o[5] * rl), pack2(o[6] * rl, o[7] * rl)});
        const unsigned t0 = pack2(o[8] * rl, o[9] * rl);
        const unsigned t1 = __builtin_amdgcn_mov_dpp(t0, 0x55, 0xf, 0xf, true), t2 = __builtin_amdgcn_mov_dpp(t0, 0xAA, 0xf, 0xf, true),
                       t3 = __builtin_amdgcn_mov_dpp(t0, 0xFF, 0xf, 0xf, true);
        if (hh == 0 && (r32 & 3) == 0) gst16c(lay + LO_O + O_BYTES + O_TAIL + ((size_t)(rb * 32 + head) * 16 + m) * 4, (u32x4){t0, t1, t2, t3});
    }
    signal_cnt<0>(ws, L * C_LAY + C_AT + qb);
#undef LDK1
#undef LDK2
#undef LDV
}

__global__ __launch_bounds__(512) void prep_kernel(const unsigned short* __restrict__ x0, const unsigned short* __restrict__ wA,
                                                   const unsigned short* __restrict__ woA, const unsigned short* __restrict__ wB,
                                                   const unsigned short* __restrict__ woB, char* ws) {
    __shared__ __attribute__((aligned(16))) char tbuf[7 * 4096];
    __shared__ __attribute__((aligned(16))) float red[72 * 16];
    const int tid = threadIdx.x, bid = blockIdx.x, w = __builtin_amdgcn_readfirstlane(tid >> 6), l = tid & 63;
    const int lin = (bid & 7) * 27 + (bid >> 3);
    const bool jobW = (bid >> 3) < 27;
    const int cmat = lin >= 108, cblk = lin - 108 * cmat;
    u32x2 sv[8]; u32x4 xv[9];
    if (w < 5) {
        if (jobW) {
            const int T = min(tid, 287);
            const char* p = (const char*)((cmat ? wB : wA) + (size_t)(8 * (T >> 2)) * 1728 + 16 * cblk + 4 * (T & 3));
            gld8<0>(sv[0], p); gld8<3456>(sv[1], p); p += 6912; gld8<0>(sv[2], p); gld8<3456>(sv[3], p); p += 6912;
            gld8<0>(sv[4], p); gld8<3456>(sv[5], p); p += 6912; gld8<0>(sv[6], p); gld8<3456>(sv[7], p);
        }
    } else if (w < 7) {
        const int T = bid * 90 + min(tid - 320, 89), jmat = T / 11520, r = T - jmat * 11520, jsg = r / 160, jq = r - jsg * 160;
        const int jt = jq / 20, lc = min(4 * (jq - jt * 20), 68), ss = jsg >> 2, gg = jsg & 3;
        int r0, rb_, rc;
        if (ss >= 16) { r0 = (16 * (ss - 16) + 4 * gg) * 18 + 16; rb_ = 18; rc = 36; }
        else if (jmat == 0) { r0 = 36 * ss + 4 * gg; rb_ = 2; rc = 18; }
        else { r0 = (2 * ss + (gg >> 1)) * 18 + 4 * (gg & 1); rb_ = 2; rc = 8; }
        const char* p = (const char*)((jmat ? woB : woA) + (size_t)r0 * 576 + 72 * jt + lc);
        const char* p2 = p + rb_ * 1152; const char* p4 = p + rc * 1152; const char* p6 = p4 + rb_ * 1152;
        gld8<0>(sv[0], p); gld8<1152>(sv[1], p); gld8<0>(sv[2], p2); gld8<1152>(sv[3], p2);
        gld8<0>(sv[4], p4); gld8<1152>(sv[5], p4); gld8<0>(sv[6], p6); gld8<1152>(sv[7], p6);
    } else {
        const int T = bid * 64 + l, m = T & 15, j = (T >> 4) & 7, rb = T >> 7;
        const char* p = (const char*)(x0 + (size_t)(rb * 16 + m) * 576 + 72 * j);
        gld16<0>(xv[0], p); gld16<16>(xv[1], p); gld16<32>(xv[2], p); gld16<48>(xv[3], p); gld16<64>(xv[4], p);
        gld16<80>(xv[5], p); gld16<96>(xv[6], p); gld16<112>(xv[7], p); gld16<128>(xv[8], p);
    }
    int tct, tcb, tc; qkv_col_map(16 * cblk + (l & 15), tct, tcb, tc);
    asm volatile("s_waitcnt vmcnt(0)" : "+v"(sv[0]), "+v"(sv[1]), "+v"(sv[2]), "+v"(sv[3]), "+v"(sv[4]), "+v"(sv[5]), "+v"(sv[6]), "+v"(sv[7]) :: "memory");
    asm volatile("" : "+v"(xv[0]), "+v"(xv[1]), "+v"(xv[2]), "+v"(xv[3]), "+v"(xv[4]), "+v"(xv[5]), "+v"(xv[6]), "+v"(xv[7]), "+v"(xv[8]));
    u32x4 cc[4];
    if (w < 7) {
        transpose8x4(sv, cc);
        lds_ptr tb = (lds_ptr)tbuf + w * 4096;
#pragma unroll
        for (int i = 0; i < 4; ++i) *(lds_u4)(tb + (l * 4 + i) * 16) = cc[i];
#pragma unroll
        for (int i = 0; i < 4; ++i) cc[i] = *(lds_u4)(tb + (i * 64 + l) * 16);
        if (w < 5) {
#pragma unroll
            for (int i = 0; i < 4; ++i) {
                const int sg = 16 * w + 4 * i + (l >> 4);
                if (sg < 72) red[sg * 16 + (l & 15)] = dot_ones(cc[i][3], dot_ones(cc[i][2], dot_ones(cc[i][1], dot_ones(cc[i][0], 0.f))));
            }
        }
    }
    if (w < 5) {
        if (jobW) {
            char* dst = ws + (cmat ? OFF_WTB : OFF_WTA) + ((size_t)(tct * 18 * 7 + tcb) * 64 + (l >> 4) * 16 + tc) * 16;
#pragma unroll
            for (int i = 0; i < 4; ++i)
                if (4 * w + i < 18) gst16c(dst + (4 * w + i) * 7168, cc[i]);
        }
    } else if (w < 7) {
#pragma unroll
        for (int i = 0; i < 4; ++i) {
            const int loc = (w - 5) * 256 + i * 64 + l;
            const int X = bid * 360 + loc, mat = X / 46080, r = X - mat * 46080, sg = r / 640, n = r - sg * 640, jt = n / 80, lc = n - jt * 80;
            const int ss = sg >> 2, okh = ss < 16 ? (ss >> 3) : (ss - 16), oi = ss < 16 ? (ss & 7) : 8;
            if (loc < 360) gst16c(ws + (mat ? OFF_WOB : OFF_WOA) + (size_t)jt * WO_TILE + (size_t)((oi * 2 + okh) * 5 + (lc >> 4)) * 1024 + ((sg & 3) * 16 + (lc & 15)) * 16,
                                  lc < 72 ? cc[i] : (u32x4){0u, 0u, 0u, 0u});
        }
    } else {
        const int T = bid * 64 + l, m = T & 15, j = (T >> 4) & 7, rb = T >> 7;
        float sa = 0.f, sb = 0.f;
#pragma unroll
        for (int i = 0; i < 9; ++i) {
            const int q = 9 * j + i;
            gst16c(ws + OFF_XF + ((size_t)(rb * 18 + (q >> 2)) * 64 + (q & 3) * 16 + m) * 16, xv[i]);
            const float a = dot_ones(xv[i][3], dot_ones(xv[i][2], dot_ones(xv[i][1], dot_ones(xv[i][0], 0.f))));
            if (i < 4 || i == 8) sa += a; else sb += a;
        }
        const float ma = sa * (1.f / 40.f), mb = sb * (1.f / 32.f);
        float qa = 0.f, qb = 0.f;
#pragma unroll
        for (int i = 0; i < 9; ++i) {
            const bool pa = i < 4 || i == 8; const float mu = pa ? ma : mb; float a = 0.f;
#pragma unroll
            for (int k = 0; k < 4; ++k) { const float d0 = bf_lo(xv[i][k]) - mu, d1 = bf_hi(xv[i][k]) - mu; a += d0 * d0 + d1 * d1; }
            if (pa) qa += a; else qb += a;
        }
        gst16c(ws + OFF_ST + (size_t)(rb * 16 + m) * 128 + j * 16, __builtin_bit_cast(u32x4, (f32x4){sa, qa, sb, qb}));
    }
    if (w == 6) {
        if (l < 36) {
            const int Z = bid * 36 + l, zm = Z / 4608, z2 = Z - zm * 4608, zct = z2 / 288, r = z2 - zct * 288;
            gst16c(ws + (zm ? OFF_WTB : OFF_WTA) + ((size_t)((zct * 18 + (r >> 4)) * 7 + 6) * 64 + ((r >> 2) & 3) * 16 + 12 + (r & 3)) * 16, (u32x4){0u, 0u, 0u, 0u});
        } else if (l < 49) {
            const int Z = bid * 13 + (l - 36);
            if ((size_t)Z * 16 < ZERO_BYTES) gst16c(ws + (size_t)Z * 16, (u32x4){0u, 0u, 0u, 0u});
        } else if (l == 49 && bid < 32) gst16c(ws + ((bid & 16) ? OFF_CSB : OFF_CSA) + ((bid & 15) * 112 + 108) * 4, (u32x4){0u, 0u, 0u, 0u});
    }
    asm volatile("s_waitcnt lgkmcnt(0)\n\ts_barrier" ::: "memory");
    if (jobW && w == 0) {
        float a = 0.f;
#pragma unroll
        for (int i = 0; i < 18; ++i) a += red[((l >> 4) * 18 + i) * 16 + (l & 15)];
        a = rowsum4(a);
        if (l < 16) __hip_atomic_store((gu32*)(ws + (cmat ? OFF_CSB : OFF_CSA)) + tct * 112 + tcb * 16 + tc, __float_as_uint(a), RLX_AGENT);
    }
}

template <int MASK>
__global__ __launch_bounds__(512, 2) void mega_kernel(char* ws, const unsigned short* x0, unsigned short* out, int h_lo, int h_hi) {
    __shared__ __attribute__((aligned(16))) char smem[160 * 1024];
    const int lane = threadIdx.x & 63, w = __builtin_amdgcn_readfirstlane(threadIdx.x >> 6);
    constexpr bool FUSED = MASK == 7;
    if (FUSED && w == 7 && (blockIdx.x >> 3) < 4) {
        unsigned long long pc; asm volatile("s_getpc_b64 %0" : "=s"(pc));
        const char* cp = (const char*)(pc & ~127ull) + (blockIdx.x >> 3) * 5120 + min(lane, 39) * 128;
        unsigned t; asm volatile("global_load_dword %0, %1, off\n\ts_waitcnt vmcnt(0)" : "=v"(t) : "v"(cp) : "memory");
    }
#pragma unroll 1
    for (int hl = h_lo; hl < h_hi; ++hl) {
        const int L = hl >> 1;
        if (hl & 1) {
            if (MASK & 1) qkv_phase<1, FUSED>(ws, smem, L, w, lane);
            if (MASK & 2) attn_phase<FUSED>(ws, smem, L, w, lane);
        } else {
            if (MASK & 1) qkv_phase<0, FUSED>(ws, smem, L, w, lane);
        }
        if (MASK & 4) wo_phase<FUSED>(ws, smem, x0, out, L, hl & 1, w, lane);
    }
}

extern "C" void kernel_launch(void* const* d_in, const int* in_sizes, int n_in, void* d_out, int out_size, void* d_ws, size_t ws_size,
                              hipStream_t stream) {
    const unsigned short* x0 = (const unsigned short*)d_in[0];
    const unsigned short* wA = (const unsigned short*)d_in[1];
    const unsigned short* woA = (const unsigned short*)d_in[2];
    const unsigned short* wB = (const unsigned short*)d_in[4];
    const unsigned short* woB = (const unsigned short*)d_in[5];
    char* ws = (char*)d_ws;
    unsigned short* out = (unsigned short*)d_out;
    prep_kernel<<<256, 512, 0, stream>>>(x0, wA, woA, wB, woB, ws);
    mega_kernel<7><<<256, 512, 0, stream>>>(ws, x0, out, 0, 4);
}
```

```cpp
#include <hip/hip_runtime.h>
#include <hip/hip_bf16.h>
#include <stdint.h>

typedef __attribute__((ext_vector_type(8))) short bf16x8;
typedef __attribute__((ext_vector_type(4))) short bf16x4;
typedef __attribute__((ext_vector_type(4))) float f32x4;
typedef __attribute__((ext_vector_type(2))) float f32x2;
typedef __attribute__((ext_vector_type(16))) float f32x16;
typedef __attribute__((ext_vector_type(4))) unsigned u32x4;
typedef __attribute__((ext_vector_type(2))) unsigned u32x2;
typedef __attribute__((ext_vector_type(2))) __bf16 bfx2;
#define LDSAS __attribute__((address_space(3)))
typedef LDSAS char* lds_ptr;
typedef const LDSAS char* lds_cptr;
typedef LDSAS f32x4* lds_f4;
typedef LDSAS f32x2* lds_f2;
typedef LDSAS float* lds_f1;
typedef LDSAS u32x4* lds_u4;
typedef const volatile LDSAS unsigned* lds_vu32;
typedef const LDSAS bf16x8* lds_f8;

constexpr int SEQ = 2048, DM = 576, NH = 32;

constexpr size_t WT_TILE = 126 * 1024;
constexpr size_t WO_TILE = 90 * 1024;
constexpr size_t IMG_CHUNK = 19 * 1024;
constexpr size_t IMG_HEAD = 8 * IMG_CHUNK;
constexpr int KV_HEAD = 19 * 20;
constexpr size_t XF_BYTES = (size_t)SEQ * DM * 2, ST_BYTES = (size_t)SEQ * 128;
constexpr size_t OFF_CNT = 0;
constexpr size_t CNT_BYTES = 2 * 96 * 256 + 256;
constexpr size_t ZERO_BYTES = 49664;
constexpr size_t OFF_KV  = 65536;
constexpr size_t OFF_WTA = OFF_KV + 2 * 16 * 16 * 3072;
constexpr size_t OFF_WTB = OFF_WTA + 16 * WT_TILE + 4096;
constexpr size_t OFF_CSA = OFF_WTB + 16 * WT_TILE + 4096;
constexpr size_t OFF_CSB = OFF_CSA + 8192;
constexpr size_t OFF_WOA = OFF_CSB + 8192;
constexpr size_t OFF_WOB = OFF_WOA + 8 * WO_TILE + 8192;
constexpr size_t OFF_XF  = OFF_WOB + 8 * WO_TILE + 8192;
constexpr size_t OFF_ST  = OFF_XF + 4 * XF_BYTES;
constexpr size_t OFF_LAY = OFF_ST + 4 * ST_BYTES;
constexpr size_t O_BYTES = (size_t)128 * 16 * 1024 + (size_t)128 * 32 * 64;
constexpr size_t O_TAIL = (size_t)128 * 16 * 1024;
constexpr size_t LO_O   = 0;
constexpr size_t LO_Q1  = LO_O + 2 * O_BYTES;
constexpr size_t LO_Q2  = LO_Q1 + (size_t)NH * SEQ * 32;
constexpr size_t LO_IMG = LO_Q2 + (size_t)NH * SEQ * 4;
constexpr size_t LAY_BYTES = LO_IMG + (size_t)NH * IMG_HEAD;
static_assert(ZERO_BYTES <= OFF_KV && CNT_BYTES <= ZERO_BYTES && ZERO_BYTES % 16 == 0, "layout");
constexpr int C_QA = 0, C_OA = 16, C_WA = 32, C_QB = 48, C_AT = 64, C_WB = 80, C_LAY = 96;

constexpr float QSCALE = 0.23570226039551584f * 1.4426950408889634f;

__device__ __forceinline__ unsigned pack2(float lo, float hi) {
    f32x2 v = {lo, hi};
    bfx2 r = __builtin_convertvector(v, bfx2);
    return __builtin_bit_cast(unsigned, r);
}
__device__ __forceinline__ float bf_lo(unsigned u) { return __uint_as_float(u << 16); }
__device__ __forceinline__ float bf_hi(unsigned u) { return __uint_as_float(u & 0xffff0000u); }
__device__ __forceinline__ float phi_f(float t) { return (t > 0.f ? t + 1.f : __expf(t)) + 1e-6f; }
__device__ __forceinline__ unsigned lds_off(const void* p) { return (unsigned)(size_t)(lds_cptr)p; }

#define MFMA16(a, b, c) __builtin_amdgcn_mfma_f32_16x16x32_bf16(a, b, c, 0, 0, 0)
#define MFMA32(a, b, c) __builtin_amdgcn_mfma_f32_32x32x16_bf16(a, b, c, 0, 0, 0)
#define MFMA16K16(a, b, c) __builtin_amdgcn_mfma_f32_16x16x16bf16_1k(a, b, c, 0, 0, 0)
__device__ __forceinline__ float rowsum4(float v) {
    auto a = __builtin_amdgcn_permlane16_swap(__float_as_uint(v), __float_as_uint(v), false, false);
    v = __uint_as_float(a[0]) + __uint_as_float(a[1]);
    auto b = __builtin_amdgcn_permlane32_swap(__float_as_uint(v), __float_as_uint(v), false, false);
    return __uint_as_float(b[0]) + __uint_as_float(b[1]);
}
#define PIN(x) asm volatile("" : "+v"(x))
#define LAUNDER() int bid = blockIdx.x; asm volatile("" : "+v"(lane)); asm volatile("" : "+s"(w), "+s"(ws), "+s"(bid), "+s"(L))
#define SBAR() __builtin_amdgcn_sched_barrier(0)
#define EXP2(x) __builtin_amdgcn_exp2f(x)

template <int N>
__device__ __forceinline__ void glds(const void* g, unsigned lds) {
    unsigned keep;
    if constexpr (N == 4)
        asm volatile("s_mov_b32 %0, m0\n\ts_mov_b32 m0, %2\n\ts_nop 0\n\t"
                     "global_load_lds_dwordx4 %1, off\n\tglobal_load_lds_dwordx4 %1, off offset:1024\n\t"
                     "global_load_lds_dwordx4 %1, off offset:2048\n\tglobal_load_lds_dwordx4 %1, off offset:3072\n\t"
                     "s_mov_b32 m0, %0" : "=&s"(keep) : "v"(g), "s"(lds) : "memory");
    else
        asm volatile("s_mov_b32 %0, m0\n\ts_mov_b32 m0, %2\n\ts_nop 0\n\t"
                     "global_load_lds_dwordx4 %1, off\n\tglobal_load_lds_dwordx4 %1, off offset:1024\n\t"
                     "s_mov_b32 m0, %0" : "=&s"(keep) : "v"(g), "s"(lds) : "memory");
}
__device__ __forceinline__ void glds1_sc1(const void* g, unsigned lds) {
    unsigned keep;
    asm volatile("s_mov_b32 %0, m0\n\ts_mov_b32 m0, %2\n\ts_nop 0\n\tglobal_load_lds_dwordx4 %1, off sc1\n\ts_mov_b32 m0, %0"
                 : "=&s"(keep) : "v"(g), "s"(lds) : "memory");
}
template <int OFF, class T> __device__ __forceinline__ void gld16(T& d, const void* p) {
    asm volatile("global_load_dwordx4 %0, %1, off offset:%2" : "=v"(d) : "v"(p), "i"(OFF) : "memory"); }
template <int OFF, class T> __device__ __forceinline__ void gld4(T& d, const void* p) {
    asm volatile("global_load_dword %0, %1, off offset:%2" : "=v"(d) : "v"(p), "i"(OFF) : "memory"); }
template <int OFF, class T> __device__ __forceinline__ void gld16c(T& d, const void* p) {
    asm volatile("global_load_dwordx4 %0, %1, off offset:%2 sc1" : "=v"(d) : "v"(p), "i"(OFF) : "memory"); }
template <int OFF, class T> __device__ __forceinline__ void gld8c(T& d, const void* p) {
    asm volatile("global_load_dwordx2 %0, %1, off offset:%2 sc1" : "=v"(d) : "v"(p), "i"(OFF) : "memory"); }
template <int OFF, class T> __device__ __forceinline__ void gld4c(T& d, const void* p) {
    asm volatile("global_load_dword %0, %1, off offset:%2 sc1" : "=v"(d) : "v"(p), "i"(OFF) : "memory"); }
__device__ __forceinline__ void gst16c(void* p, u32x4 v) { asm volatile("global_store_dwordx4 %0, %1, off sc1\n\ts_nop 1" :: "v"(p), "v"(v) : "memory"); }
__device__ __forceinline__ void gst8c(void* p, f32x2 v) { asm volatile("global_store_dwordx2 %0, %1, off sc1" :: "v"(p), "v"(v) : "memory"); }

typedef __attribute__((address_space(1))) unsigned gu32;
#define RLX_AGENT __ATOMIC_RELAXED, __HIP_MEMORY_SCOPE_AGENT
template <int N> __device__ __forceinline__ void signal_cnt(char* ws, int line) {
    asm volatile("s_waitcnt vmcnt(%0) lgkmcnt(0)\n\ts_barrier" :: "i"(N) : "memory");
    if (threadIdx.x == 0) __hip_atomic_fetch_add((gu32*)(ws + OFF_CNT + (size_t)line * 256), 1u, RLX_AGENT);
}
struct Polls { unsigned v0, v1, v2, v3; };
#define RETIRE(pl) asm volatile("" :: "v"((pl).v0), "v"((pl).v1), "v"((pl).v2), "v"((pl).v3))
__device__ __forceinline__ Polls wait_cnt(char* ws, int line, unsigned want) {
    unsigned v0, v1, v2, v3, r, n;
    const char* p = ws + OFF_CNT + (size_t)line * 256;
    const unsigned poller = __builtin_amdgcn_readfirstlane(threadIdx.x < 64 ? 1u : 0u);
    asm volatile(
        "s_cmp_eq_u32 %[poller], 0\n\t"
        "s_cbranch_scc1 3f\n\t"
        "global_load_dword %[v0], %[p], off sc1\n\ts_sleep 1\n\tglobal_load_dword %[v1], %[p], off sc1\n\ts_sleep 1\n\t"
        "global_load_dword %[v2], %[p], off sc1\n\ts_sleep 1\n\tglobal_load_dword %[v3], %[p], off sc1\n\t"
        "s_mov_b32 %[n], 0x20000\n\t"
        "1:\n\t"
        "s_waitcnt vmcnt(3)\n\tv_readfirstlane_b32 %[r], %[v0]\n\ts_nop 0\n\tglobal_load_dword %[v0], %[p], off sc1\n\ts_cmp_ge_u32 %[r], %[want]\n\ts_cbranch_scc1 4f\n\t"
        "s_waitcnt vmcnt(3)\n\tv_readfirstlane_b32 %[r], %[v1]\n\ts_nop 0\n\tglobal_load_dword %[v1], %[p], off sc1\n\ts_cmp_ge_u32 %[r], %[want]\n\ts_cbranch_scc1 4f\n\t"
        "s_waitcnt vmcnt(3)\n\tv_readfirstlane_b32 %[r], %[v2]\n\ts_nop 0\n\tglobal_load_dword %[v2], %[p], off sc1\n\ts_cmp_ge_u32 %[r], %[want]\n\ts_cbranch_scc1 4f\n\t"
        "s_waitcnt vmcnt(3)\n\tv_readfirstlane_b32 %[r], %[v3]\n\ts_nop 0\n\tglobal_load_dword %[v3], %[p], off sc1\n\ts_cmp_ge_u32 %[r], %[want]\n\ts_cbranch_scc1 4f\n\t"
        "s_sub_u32 %[n], %[n], 1\n\t"
        "s_cmp_lg_u32 %[n], 0\n\t"
        "s_cbranch_scc1 1b\n\t"
        "3:\n\t"
        "s_waitcnt vmcnt(0)\n\t"
        "4:\n\t"
        "s_waitcnt lgkmcnt(0)\n\t"
        "s_barrier"
        : [v0] "=&v"(v0), [v1] "=&v"(v1), [v2] "=&v"(v2), [v3] "=&v"(v3), [r] "=&s"(r), [n] "=&s"(n)
        : [p] "v"(p), [want] "s"(want), [poller] "s"(poller) : "scc", "memory");
    return Polls{v0, v1, v2, v3};
}

__device__ __forceinline__ void qkv_col_map(int n, int& ct, int& cb, int& c) {
    const int part = n / 576, rem = n - part * 576, head = rem / 18, d = rem - head * 18;
    ct = head >> 1; const int hb = head & 1;
    if (d < 16) { cb = part * 2 + hb; c = d; } else { cb = 6; c = part * 4 + hb * 2 + (d - 16); }
}
__device__ __forceinline__ float dot_ones(unsigned u, float acc) {
    return __builtin_amdgcn_fdot2_f32_bf16(__builtin_bit_cast(bfx2, u), __builtin_bit_cast(bfx2, 0x3f803f80u), acc, false); }
__device__ __forceinline__ float dot_self(unsigned u, float acc) {
    return __builtin_amdgcn_fdot2_f32_bf16(__builtin_bit_cast(bfx2, u), __builtin_bit_cast(bfx2, u), acc, false); }
__device__ __forceinline__ void transpose8x4(const u32x2 (&v)[8], u32x4 (&c)[4]) {
#pragma unroll
    for (int k = 0; k < 4; ++k) {
        c[0][k] = __builtin_amdgcn_perm(v[2 * k + 1][0], v[2 * k][0], 0x05040100u); c[1][k] = __builtin_amdgcn_perm(v[2 * k + 1][0], v[2 * k][0], 0x07060302u);
        c[2][k] = __builtin_amdgcn_perm(v[2 * k + 1][1], v[2 * k][1], 0x05040100u); c[3][k] = __builtin_amdgcn_perm(v[2 * k + 1][1], v[2 * k][1], 0x07060302u);
    }
}
template <int OFF, class T> __device__ __forceinline__ void gld8(T& d, const void* p) {
    asm volatile("global_load_dwordx2 %0, %1, off offset:%2" : "=v"(d) : "v"(p), "i"(OFF) : "memory"); }

constexpr int LDS_W = 34 * 1024, LDS_WO = 70 * 1024;
struct Pf { const char* src; unsigned dst; };
__device__ __forceinline__ Pf pf_qkv(char* ws, char* smem, int layer, int bid, int w, int lane) {
    const int ct = 8 * (bid & 1) + ((bid >> 3) & 7);
    return Pf{ws + (layer ? OFF_WTB : OFF_WTA) + (size_t)ct * WT_TILE + w * 2048 + lane * 16, lds_off(smem) + LDS_W + w * 2048};
}
__device__ __forceinline__ Pf pf_wo(char* ws, char* smem, int mode, int bid, int w, int lane) {
    const int jt = (bid >> 3) & 7;
    return Pf{ws + (mode ? OFF_WOB : OFF_WOA) + (size_t)jt * WO_TILE + w * 2048 + lane * 16, lds_off(smem) + LDS_WO + w * 2048};
}
#define PF(p, j) glds<2>((p).src + (j) * 16384, (p).dst + (j) * 16384)
#define PF_LAST(p, j, n) do { const int jj_ = w < (n) ? (j) : (j) - 1; PF(p, jj_); } while (0)
#define PFQ_LAST(p) PF_LAST(p, 7, 7)
#define PFO_LAST(p) PF_LAST(p, 5, 5)

__device__ __forceinline__ void lin_kv_part(char* ws, char* smem, int L, int panel, int ct, int w, int lane, const f32x4 (&acc)[7], const float (&rs4)[4], unsigned (&qt)[2]) {
    const int m = lane & 15, g = lane >> 4, tid = w * 64 + lane;
    {
        u32x2 fr[5];
#pragma unroll
        for (int c = 2; c < 7; ++c) {
            float v[4];
#pragma unroll
            for (int r = 0; r < 4; ++r) {
                float x = rs4[r] * acc[c][r];
                if (c < 4) x = phi_f(x);
                else if (c == 6) { const float p = phi_f(x); x = m < 8 ? p : x; x = m == 12 ? 1.f : x; }
                v[r] = x;
            }
            fr[c - 2] = (u32x2){pack2(v[0], v[1]), pack2(v[2], v[3])};
        }
        const bf16x4 k0 = __builtin_bit_cast(bf16x4, fr[0]), k1 = __builtin_bit_cast(bf16x4, fr[1]);
        const bf16x4 v0 = __builtin_bit_cast(bf16x4, fr[2]), v1 = __builtin_bit_cast(bf16x4, fr[3]), T = __builtin_bit_cast(bf16x4, fr[4]);
        const f32x4 z4 = {0.f, 0.f, 0.f, 0.f};
        const f32x4 d1 = MFMA16K16(k0, v0, z4), d2 = MFMA16K16(k1, v1, z4), d3 = MFMA16K16(T, v0, z4), d4 = MFMA16K16(T, v1, z4);
        const f32x4 d5 = MFMA16K16(k0, T, z4), d6 = MFMA16K16(k1, T, z4), d7 = MFMA16K16(T, T, z4);
        lds_ptr red = (lds_ptr)smem + (w ^ 4) * 5120;
        *(lds_f4)(red + (m * 20 + 4 * g) * 4) = d1;
        *(lds_f4)(red + (KV_HEAD + m * 20 + 4 * g) * 4) = d2;
        if (g == 1) {
            *(lds_f2)(red + (m * 20 + 16) * 4) = (f32x2){d3[0], d3[1]};
            *(lds_f2)(red + (KV_HEAD + m * 20 + 16) * 4) = (f32x2){d4[2], d4[3]};
        }
        if (m == 8 || m == 9 || m == 12) {
            const int e = m == 12 ? 18 : 8 + m;
            *(lds_f4)(red + (e * 20 + 4 * g) * 4) = d5;
            if (g == 1) *(lds_f2)(red + (e * 20 + 16) * 4) = (f32x2){d7[0], d7[1]};
        }
        if (m == 10 || m == 11 || m == 12) {
            const int e = m == 12 ? 18 : 6 + m;
            *(lds_f4)(red + (KV_HEAD + e * 20 + 4 * g) * 4) = d6;
            if (g == 1) *(lds_f2)(red + (KV_HEAD + e * 20 + 16) * 4) = (f32x2){d7[2], d7[3]};
        }
        {
            const unsigned o0 = fr[4][0], o1 = fr[4][1];
            const unsigned n0 = __builtin_amdgcn_mov_dpp(o0, 0xB1, 0xf, 0xf, true), n1 = __builtin_amdgcn_mov_dpp(o1, 0xB1, 0xf, 0xf, true);
            if (m == 0 || m == 2)
                *(lds_u4)(red + 4096 + (m >> 1) * 64 + g * 16) =
                    (u32x4){(o0 & 0xffffu) | (n0 << 16), (o0 >> 16) | (n0 & 0xffff0000u), (o1 & 0xffffu) | (n1 << 16), (o1 >> 16) | (n1 & 0xffff0000u)};
        }
        asm volatile("s_waitcnt lgkmcnt(0)\n\ts_barrier" ::: "memory");
        char* kvp = ws + OFF_KV + (size_t)(L * 16 + ct) * 16 * 3072;
        if (tid < 190) {
            f32x4 s4 = *(lds_f4)((lds_ptr)smem + tid * 16);
#pragma unroll
            for (int i = 1; i < 8; ++i) s4 += *(lds_f4)((lds_ptr)smem + i * 5120 + tid * 16);
            gst16c(kvp + panel * 3072 + tid * 16, __builtin_bit_cast(u32x4, s4));
        }
        qt[0] = *(lds_vu32)(red + 4096 + m * 4); qt[1] = *(lds_vu32)(red + 4096 + 64 + m * 4);
    }
}
__device__ __forceinline__ void lin_o_part(char* ws, char* smem, int L, int rb, int ct, int w, int lane, u32x4 (&bq)[2], const Polls& pl) {
    const int m = lane & 15, g = lane >> 4, tid = w * 64 + lane, h0 = 2 * ct;
    char* lay = ws + OFF_LAY + 0 * LAY_BYTES;
    char* kvp = ws + OFF_KV + (size_t)(L * 16 + ct) * 16 * 3072;
    const f32x4 z4 = {0.f, 0.f, 0.f, 0.f};
    {
        {
            f32x4 pa[3], pb[3];
            const char* pp = kvp + w * 6144 + lane * 16;
            gld16c<0>(pa[0], pp); gld16c<1024>(pa[1], pp); gld16c<2048>(pa[2], pp);
            gld16c<0>(pb[0], pp + 3072); gld16c<1024>(pb[1], pp + 3072); gld16c<2048>(pb[2], pp + 3072);
            asm volatile("s_waitcnt vmcnt(0)" : "+v"(pa[0]), "+v"(pa[1]), "+v"(pa[2]), "+v"(pb[0]), "+v"(pb[1]), "+v"(pb[2]), "+v"(bq[0]), "+v"(bq[1]) :: "memory");
            RETIRE(pl);
#pragma unroll
            for (int i = 0; i < 3; ++i) *(lds_f4)((lds_ptr)smem + w * 3072 + i * 1024 + lane * 16) = pa[i] + pb[i];
            asm volatile("s_waitcnt lgkmcnt(0)\n\ts_barrier" ::: "memory");
            if (tid < 190) {
                f32x4 s4 = *(lds_f4)((lds_ptr)smem + tid * 16);
#pragma unroll
                for (int i = 1; i < 8; ++i) s4 += *(lds_f4)((lds_ptr)smem + i * 3072 + tid * 16);
                *(lds_f4)((lds_ptr)smem + 24576 + tid * 16) = s4;
            }
            asm volatile("s_waitcnt lgkmcnt(0)\n\ts_barrier" ::: "memory");
        }
        f32x4 ka[2], kb[2]; f32x2 ka2[2], kb2[2];
        const int r3 = m & 3; const bool isz = r3 == 2;
        {
            lds_ptr p1 = (lds_ptr)smem + 24576 + m * 80, p2 = (lds_ptr)smem + 24576 + (isz ? 18 : 16 + (r3 & 1)) * 80;
#pragma unroll
            for (int h = 0; h < 2; ++h) {
                ka[h] = *(lds_f4)(p1 + h * KV_HEAD * 4 + 16 * g); ka2[h] = *(lds_f2)(p1 + h * KV_HEAD * 4 + 64);
                kb[h] = *(lds_f4)(p2 + h * KV_HEAD * 4 + 16 * g); kb2[h] = *(lds_f2)(p2 + h * KV_HEAD * 4 + 64);
            }
        }
        const bool use2 = isz || m < 2;
        unsigned om[2][2], ot[2];
#pragma unroll
        for (int h = 0; h < 2; ++h) {
            const u32x4 f1 = {pack2(ka[h][0], ka[h][1]), pack2(ka[h][2], ka[h][3]), g == 0 ? pack2(ka2[h][0], ka2[h][1]) : 0u, 0u};
            u32x4 f2 = {pack2(kb[h][0], kb[h][1]), pack2(kb[h][2], kb[h][3]), g == 0 ? pack2(kb2[h][0], kb2[h][1]) : 0u, 0u};
            if (!use2) f2 = (u32x4){0u, 0u, 0u, 0u};
            const bf16x8 b = __builtin_bit_cast(bf16x8, bq[h]);
            const f32x4 e1 = MFMA16(__builtin_bit_cast(bf16x8, f1), b, z4), e2 = MFMA16(__builtin_bit_cast(bf16x8, f2), b, z4);
            const float r = __builtin_amdgcn_rcpf(e2[2] + 1e-6f);
            om[h][0] = pack2(e1[0] * r, e1[1] * r); om[h][1] = pack2(e1[2] * r, e1[3] * r);
            ot[h] = pack2(e2[0] * r, e2[1] * r);
        }
        gst16c(lay + LO_O + ((size_t)(rb * 16 + ct) * 64 + lane) * 16, (u32x4){om[0][0], om[0][1], om[1][0], om[1][1]});
        {
            const unsigned a0 = __builtin_amdgcn_mov_dpp(ot[0], 0x00, 0xf, 0xf, true), a1 = __builtin_amdgcn_mov_dpp(ot[0], 0x55, 0xf, 0xf, true),
                           a2 = __builtin_amdgcn_mov_dpp(ot[0], 0xAA, 0xf, 0xf, true), a3 = __builtin_amdgcn_mov_dpp(ot[0], 0xFF, 0xf, 0xf, true);
            const unsigned b0 = __builtin_amdgcn_mov_dpp(ot[1], 0x00, 0xf, 0xf, true), b1 = __builtin_amdgcn_mov_dpp(ot[1], 0x55, 0xf, 0xf, true),
                           b2 = __builtin_amdgcn_mov_dpp(ot[1], 0xAA, 0xf, 0xf, true), b3 = __builtin_amdgcn_mov_dpp(ot[1], 0xFF, 0xf, 0xf, true);
            const bool h1 = r3 == 1;
            if (g == 0 && r3 < 2)
                gst16c(lay + LO_O + O_TAIL + ((size_t)(rb * 32 + h0 + r3) * 16 + (m & 12)) * 4, (u32x4){h1 ? b0 : a0, h1 ? b1 : a1, h1 ? b2 : a2, h1 ? b3 : a3});
        }
    }
}

template <int LAYER, bool FUSED>
__device__ __forceinline__ void qkv_phase(char* ws, char* smem, int L, int w, int lane) {
    LAUNDER();
    int m = lane & 15, g = lane >> 4; const int rp = w & 3, kh = w >> 2;
    const int xcd = bid & 7, idx = bid >> 3;
    const int panel = 4 * (xcd >> 1) + (idx >> 3), ct = 8 * (xcd & 1) + (idx & 7);
    const int rb = panel * 8 + 2 * rp + kh, rbo = panel * 8 + 2 * rp + 1 - kh;
    const int xv = 2 * L + LAYER;
    char* lay = ws + OFF_LAY + 0 * LAY_BYTES;
    f32x4 st[2], cs4[4]; float cs1[5];
    {
        const float* cs = (const float*)(ws + (LAYER ? OFF_CSB : OFF_CSA)) + ct * 112;
        const float* c4 = cs + 4 * g; const float* c1 = cs + m;
        if constexpr (LAYER == 0) {
            gld16<0>(cs4[0], c4); gld16<64>(cs4[1], c4);
            gld4<128>(cs1[0], c1); gld4<192>(cs1[1], c1); gld4<256>(cs1[2], c1); gld4<320>(cs1[3], c1); gld4<384>(cs1[4], c1);
        } else {
            gld16<0>(cs4[0], c4); gld16<64>(cs4[1], c4); gld16<128>(cs4[2], c4); gld16<192>(cs4[3], c4);
            gld4<256>(cs1[0], c1); gld4<320>(cs1[1], c1); gld4<384>(cs1[2], c1);
        }
    }
    if (!FUSED || xv == 0) {
        const Pf pf = pf_qkv(ws, smem, LAYER, bid, w, lane);
        PF(pf, 0); PF(pf, 1); PF(pf, 2); PF(pf, 3); PF(pf, 4); PF(pf, 5); PF(pf, 6); PFQ_LAST(pf);
    }
    const Polls pl = wait_cnt(ws, LAYER ? L * C_LAY + C_WA + panel : C_WB + panel, xv ? 16u : 0u);
    bf16x8 a[2][9];
    const char* xa0 = ws + OFF_XF + (xv & 1) * XF_BYTES + (size_t)(rb * 18 + kh) * 1024 + lane * 16;
    const char* xa1 = ws + OFF_XF + (xv & 1) * XF_BYTES + (size_t)(rbo * 18 + kh) * 1024 + lane * 16;
#define LDA(j) do { gld16c<((j) & 1) * 2048>(a[0][j], xa0 + ((j) >> 1) * 4096); gld16c<((j) & 1) * 2048>(a[1][j], xa1 + ((j) >> 1) * 4096); } while (0)
    {
        const char* sp = ws + OFF_ST + (xv & 1) * ST_BYTES + (size_t)(rb * 16 + m) * 128 + g * 32;
        gld16c<0>(st[0], sp); gld16c<16>(st[1], sp);
    }
    LDA(0); LDA(1); LDA(2);
    const lds_cptr bl = (lds_cptr)smem + LDS_W + kh * 7 * 1024 + lane * 16;
    bf16x8 bb[2][7];
#pragma unroll
    for (int c = 0; c < 7; ++c) bb[0][c] = *(lds_f8)(bl + c * 1024);
    constexpr int NTR = LAYER == 0 ? 2 : 4;
    f32x4 acc[2][7];
#pragma unroll
    for (int c = 0; c < 7; ++c) { acc[0][c] = (f32x4){0.f, 0.f, 0.f, 0.f}; acc[1][c] = (f32x4){0.f, 0.f, 0.f, 0.f}; }
    float nmu = 0.f, rs = 0.f, nmu4[4] = {0.f, 0.f, 0.f, 0.f}, rs4[4] = {0.f, 0.f, 0.f, 0.f};
    auto stats = [&]() {
        if constexpr (LAYER == 0) asm volatile("" : "+v"(st[0]), "+v"(st[1]), "+v"(cs4[0]), "+v"(cs4[1]), "+v"(cs1[0]), "+v"(cs1[1]), "+v"(cs1[2]), "+v"(cs1[3]), "+v"(cs1[4]));
        else asm volatile("" : "+v"(st[0]), "+v"(st[1]), "+v"(cs4[0]), "+v"(cs4[1]), "+v"(cs4[2]), "+v"(cs4[3]), "+v"(cs1[0]), "+v"(cs1[1]), "+v"(cs1[2]));
        float s = rowsum4(st[0][0] + st[0][2] + st[1][0] + st[1][2]);
        const float mean = s * (1.f / 576.f);
        const float d0 = st[0][0] * (1.f / 40.f) - mean, d1 = st[0][2] * (1.f / 32.f) - mean;
        const float d2 = st[1][0] * (1.f / 40.f) - mean, d3 = st[1][2] * (1.f / 32.f) - mean;
        const float q = rowsum4(st[0][1] + st[0][3] + st[1][1] + st[1][3] + 40.f * (d0 * d0 + d2 * d2) + 32.f * (d1 * d1 + d3 * d3));
        nmu = -mean; rs = __builtin_amdgcn_rsqf(q * (1.f / 576.f) + 1e-5f);
#pragma unroll
        for (int r = 0; r < 4; ++r) { nmu4[r] = __shfl(nmu, 4 * g + r); rs4[r] = __shfl(rs, 4 * g + r); }
#pragma unroll
        for (int c = 0; c < NTR; ++c) cs4[c] *= nmu;
        SBAR();
    };
#define MM(j) do { _Pragma("unroll") for (int c = 0; c < 7; ++c) { \
            if ((j) < 8 && !(LAYER == 0 && c < 2 && (j) >= 3)) bb[((j) + 1) & 1][c] = *(lds_f8)(bl + (((j) + 1) * 14 + c) * 1024); \
            SBAR(); \
            if (LAYER == 0 && c < 2 && (j) >= 4) {} else \
            if (c < NTR) { acc[1][c] = MFMA16(bb[(j) & 1][c], a[1][j], acc[1][c]); acc[0][c] = MFMA16(bb[(j) & 1][c], a[0][j], acc[0][c]); } \
            else         { acc[1][c] = MFMA16(a[1][j], bb[(j) & 1][c], acc[1][c]); acc[0][c] = MFMA16(a[0][j], bb[(j) & 1][c], acc[0][c]); } \
            SBAR(); \
        } } while (0)
#define WAITA(j, N) asm volatile("s_waitcnt vmcnt(" #N ")" : "+v"(a[0][j]), "+v"(a[1][j]) :: "memory")
    SBAR();
    WAITA(0, 4); RETIRE(pl); LDA(3); LDA(4); MM(0);
    WAITA(1, 6); LDA(5); LDA(6); MM(1);
    if (kh == 0) stats();
    WAITA(2, 8); LDA(7); LDA(8); MM(2);
    WAITA(3, 10); MM(3);
    WAITA(4, 8); MM(4);
    asm volatile("s_barrier" ::: "memory");
    WAITA(5, 6); MM(5);
    if (kh == 1) stats();
    WAITA(6, 4); MM(6);
    WAITA(7, 2); MM(7);
    WAITA(8, 0); MM(8);
#undef MM
#undef WAITA
#undef LDA
    asm volatile("" : "+v"(lane)); m = lane & 15; g = lane >> 4;
    constexpr int C0 = LAYER == 0 ? 2 : 0, SLOT = (7 - C0) * 1024;
    {
        lds_ptr ex = (lds_ptr)smem + w * SLOT + lane * 16;
#pragma unroll
        for (int c = C0; c < 7; ++c) *(lds_f4)(ex + (c - C0) * 1024) = acc[1][c];
        asm volatile("s_waitcnt lgkmcnt(0)\n\ts_barrier" ::: "memory");
        lds_ptr ey = (lds_ptr)smem + (w ^ 4) * SLOT + lane * 16;
#pragma unroll
        for (int c = C0; c < 7; ++c) {
            acc[0][c] += *(lds_f4)(ey + (c - C0) * 1024);
            if (c < NTR) acc[0][c] += cs4[c];
            else { const float cv = cs1[c - NTR]; acc[0][c] += (f32x4){nmu4[0] * cv, nmu4[1] * cv, nmu4[2] * cv, nmu4[3] * cv}; }
        }
    }
    const int h0 = 2 * ct;
    if constexpr (LAYER == 0) {
        unsigned qt[2];
        lin_kv_part(ws, smem, L, panel, ct, w, lane, acc[0], rs4, qt);
        const lds_cptr bl2 = (lds_cptr)smem + LDS_W + kh * 7 * 1024 + lane * 16;
#pragma unroll
        for (int j = 4; j < 9; ++j) {
            const bf16x8 b0 = *(lds_f8)(bl2 + (j * 14) * 1024), b1 = *(lds_f8)(bl2 + (j * 14 + 1) * 1024);
            acc[1][0] = MFMA16(b0, a[1][j], acc[1][0]); acc[0][0] = MFMA16(b0, a[0][j], acc[0][0]);
            acc[1][1] = MFMA16(b1, a[1][j], acc[1][1]); acc[0][1] = MFMA16(b1, a[0][j], acc[0][1]);
            if (j == 5) signal_cnt<0>(ws, L * C_LAY + C_QA + ct);
        }
        lds_ptr e2 = (lds_ptr)smem + 40960 + w * 2048 + lane * 16;
        *(lds_f4)(e2) = acc[1][0]; *(lds_f4)(e2 + 1024) = acc[1][1];
        asm volatile("s_waitcnt lgkmcnt(0)\n\ts_barrier" ::: "memory");
        const Pf pf = pf_wo(ws, smem, 0, bid, w, lane);
        if (FUSED) { PF(pf, 0); PF(pf, 1); PF(pf, 2); }
        u32x4 bq[2];
        lds_ptr e3 = (lds_ptr)smem + 40960 + (w ^ 4) * 2048 + lane * 16;
#pragma unroll
        for (int c = 0; c < 2; ++c) {
            const f32x4 t = acc[0][c] + *(lds_f4)(e3 + c * 1024) + cs4[c];
            bq[c] = (u32x4){pack2(phi_f(rs * t[0]), phi_f(rs * t[1])), pack2(phi_f(rs * t[2]), phi_f(rs * t[3])), qt[c], 0u};
        }
        const Polls pl2 = wait_cnt(ws, L * C_LAY + C_QA + ct, 16);
        lin_o_part(ws, smem, L, rb, ct, w, lane, bq, pl2);
        signal_cnt<0>(ws, L * C_LAY + C_OA + panel);
        if (FUSED) { PF(pf, 3); PF(pf, 4); PFO_LAST(pf); }
    } else {
        const int tok = rb * 16 + m;
        char* img = lay + LO_IMG + (size_t)h0 * IMG_HEAD + (size_t)(rb >> 4) * IMG_CHUNK;
        const float rq = rs * QSCALE;
#pragma unroll
        for (int p = 0; p < 2; ++p) {
            const float sc = p == 0 ? rq : rs;
            const f32x4 A = acc[0][2 * p], B = acc[0][2 * p + 1];
            auto s0 = __builtin_amdgcn_permlane16_swap(pack2(sc * A[0], sc * A[1]), pack2(sc * B[0], sc * B[1]), false, false);
            auto s1 = __builtin_amdgcn_permlane16_swap(pack2(sc * A[2], sc * A[3]), pack2(sc * B[2], sc * B[3]), false, false);
            const u32x4 d = {s0[0], s1[0], s0[1], s1[1]};
            if (p == 0) gst16c(lay + LO_Q1 + ((size_t)(h0 + (g & 1)) * SEQ + tok) * 32 + (g >> 1) * 16, d);
            else gst16c(img + (g & 1) * IMG_HEAD + ((tok & 255) >> 5) * 1024 + (g >> 1) * 512 + (tok & 31) * 16, d);
        }
        {
            const f32x4 A = acc[0][4], B = acc[0][5];
            auto s0 = __builtin_amdgcn_permlane32_swap(pack2(rs4[0] * A[0], rs4[1] * A[1]), pack2(rs4[0] * B[0], rs4[1] * B[1]), false, false);
            auto s1 = __builtin_amdgcn_permlane32_swap(pack2(rs4[2] * A[2], rs4[3] * A[3]), pack2(rs4[2] * B[2], rs4[3] * B[3]), false, false);
            gst16c(img + (g >> 1) * IMG_HEAD + 9216 + ((rb & 15) * 2 + (g & 1)) * 320 + m * 16, (u32x4){s0[0], s1[0], s0[1], s1[1]});
        }
        {
            const float qs = m < 4 ? QSCALE : 1.f;
            const f32x4 A = acc[0][6];
            const unsigned o0 = pack2(qs * rs4[0] * A[0], qs * rs4[1] * A[1]), o1 = pack2(qs * rs4[2] * A[2], qs * rs4[3] * A[3]);
            const unsigned n0 = __builtin_amdgcn_mov_dpp(o0, 0xB1, 0xf, 0xf, true), n1 = __builtin_amdgcn_mov_dpp(o1, 0xB1, 0xf, 0xf, true);
            auto u0 = __builtin_amdgcn_permlane32_swap(o0, o0, false, false); auto u1 = __builtin_amdgcn_permlane32_swap(o1, o1, false, false);
            const int hsel = (m >> 1) & 1, tok0 = rb * 16 + 4 * g;
            u32x4 d; char* p;
            if (m < 8) {
                d = (u32x4){(o0 & 0xffffu) | (n0 << 16), (o0 >> 16) | (n0 & 0xffff0000u), (o1 & 0xffffu) | (n1 << 16), (o1 >> 16) | (n1 & 0xffff0000u)};
                p = m < 4 ? lay + LO_Q2 + ((size_t)(h0 + hsel) * SEQ + tok0) * 4 : img + hsel * IMG_HEAD + 8192 + (tok0 & 255) * 4;
            } else {
                const unsigned cst = (m & 1) ? 0u : 0x3f803f80u;
                d = m < 12 ? (u32x4){o0, o1, u0[1], u1[1]} : (u32x4){cst, cst, cst, cst};
                p = img + hsel * IMG_HEAD + 9216 + ((rb & 15) * 2 + (g & 1)) * 320 + (16 + (m & 1) + ((m >> 1) & 2)) * 16;
            }
            if (m < 8 ? !(m & 1) : g < 2) gst16c(p, d);
        }
        signal_cnt<0>(ws, L * C_LAY + C_QB + ct);
    }
}

template <bool FUSED>
__device__ __forceinline__ void wo_phase(char* ws, char* smem, const unsigned short* x0, unsigned short* out, int L, int mode, int w, int lane) {
    LAUNDER();
    const int m = lane & 15, g = lane >> 4, rb4 = w & 3, kh = w >> 2;
    const int xcd = bid & 7, idx = bid >> 3;
    const int panel = 4 * xcd + (idx >> 3), jt = idx & 7;
    const int rb = panel * 4 + rb4;
    const int xv = 2 * L + mode;
    const char* ob = ws + OFF_LAY + 0 * LAY_BYTES + LO_O + mode * O_BYTES;
    const bool last = xv == 3;
    const bool dopf = FUSED && !last;
    const Pf pf = pf_qkv(ws, smem, mode ? 0 : 1, bid, w, lane);
    if (!FUSED) { const Pf po = pf_wo(ws, smem, mode, bid, w, lane); PF(po, 0); PF(po, 1); PF(po, 2); PF(po, 3); PF(po, 4); PFO_LAST(po); }
    u32x2 xr[3];
#pragma unroll
    for (int c = 0; c < 3; ++c) {
        const int cb = c == 2 ? 4 : 2 * kh + c, gg = c == 2 ? (g & 1) : g;
        const int n0 = 72 * jt + 16 * cb + 4 * gg;
        const char* xp = xv == 0 ? (const char*)(x0 + (size_t)(rb * 16 + m) * 576 + n0)
                                 : ws + OFF_XF + (xv & 1) * XF_BYTES + ((size_t)(rb * 18 + (n0 >> 5)) * 64 + ((n0 >> 3) & 3) * 16 + m) * 16 + ((n0 >> 2) & 1) * 8;
        gld8c<0>(xr[c], xp);
    }
    if (dopf) { PF(pf, 0); PF(pf, 1); }
    const Polls pl = wait_cnt(ws, L * C_LAY + (mode ? C_AT + (panel >> 2) : C_OA + (panel >> 1)), mode ? 32u : 16u);
    asm volatile("" : "+v"(xr[0]), "+v"(xr[1]), "+v"(xr[2]));
    bf16x8 af[9]; unsigned pt[4];
    {
        const char* op = ob + ((size_t)(rb * 16 + 8 * kh) * 64 + lane) * 16;
        gld16c<0>(af[0], op); gld16c<1024>(af[1], op); gld16c<2048>(af[2], op); gld16c<3072>(af[3], op);
        gld16c<0>(af[4], op + 4096); gld16c<1024>(af[5], op + 4096); gld16c<2048>(af[6], op + 4096); gld16c<3072>(af[7], op + 4096);
        const char* tp = ob + O_TAIL + ((size_t)(rb * 32 + 16 * kh + 4 * g) * 16 + m) * 4;
        gld4c<0>(pt[0], tp); gld4c<64>(pt[1], tp); gld4c<128>(pt[2], tp); gld4c<192>(pt[3], tp);
    }
    f32x4 acc[5];
#pragma unroll
    for (int c = 0; c < 5; ++c) acc[c] = (f32x4){0.f, 0.f, 0.f, 0.f};
    const lds_cptr bl = (lds_cptr)smem + LDS_WO + kh * 5 * 1024 + lane * 16;
    bf16x8 bb[3][5];
#pragma unroll
    for (int c = 0; c < 5; ++c) { bb[0][c] = *(lds_f8)(bl + c * 1024); bb[1][c] = *(lds_f8)(bl + (10 + c) * 1024); }
#define KSTEP(i_) do { _Pragma("unroll") for (int c = 0; c < 5; ++c) { \
        if ((i_) < 7) bb[((i_) + 2) % 3][c] = *(lds_f8)(bl + (((i_) + 2) * 10 + c) * 1024); \
        SBAR(); acc[c] = MFMA16(bb[(i_) % 3][c], af[i_], acc[c]); SBAR(); } } while (0)
#define WAITF(i_, N) asm volatile("s_waitcnt vmcnt(" #N ")" : "+v"(af[i_]) :: "memory")
    SBAR();
    WAITF(0, 11); RETIRE(pl); KSTEP(0); WAITF(1, 10); KSTEP(1); WAITF(2, 9); KSTEP(2); WAITF(3, 8); KSTEP(3);
    WAITF(4, 7); KSTEP(4); WAITF(5, 6); KSTEP(5); WAITF(6, 5); KSTEP(6); WAITF(7, 4); KSTEP(7);
    asm volatile("s_waitcnt vmcnt(0)" : "+v"(pt[0]), "+v"(pt[1]), "+v"(pt[2]), "+v"(pt[3]) :: "memory");
    af[8] = __builtin_bit_cast(bf16x8, (u32x4){pt[0], pt[1], pt[2], pt[3]});
    KSTEP(8);
#undef KSTEP
#undef WAITF
    lds_ptr ex = (lds_ptr)smem + rb4 * 5 * 1024 + lane * 16;
    if (kh == 0) { *(lds_f4)(ex + 2 * 1024) = acc[2]; *(lds_f4)(ex + 3 * 1024) = acc[3]; }
    else { *(lds_f4)(ex) = acc[0]; *(lds_f4)(ex + 1024) = acc[1]; *(lds_f4)(ex + 4 * 1024) = acc[4]; }
    asm volatile("s_waitcnt lgkmcnt(0)\n\ts_barrier" ::: "memory");
    unsigned pk[3][2];
    {
        const f32x4 o0 = (kh ? acc[2] : acc[0]) + *(lds_f4)(ex + (2 * kh) * 1024), o1 = (kh ? acc[3] : acc[1]) + *(lds_f4)(ex + (2 * kh + 1) * 1024);
        const f32x4 o2 = acc[4] + *(lds_f4)(ex + 4 * 1024);
        pk[0][0] = pack2(o0[0] + bf_lo(xr[0][0]), o0[1] + bf_hi(xr[0][0])); pk[0][1] = pack2(o0[2] + bf_lo(xr[0][1]), o0[3] + bf_hi(xr[0][1]));
        pk[1][0] = pack2(o1[0] + bf_lo(xr[1][0]), o1[1] + bf_hi(xr[1][0])); pk[1][1] = pack2(o1[2] + bf_lo(xr[1][1]), o1[3] + bf_hi(xr[1][1]));
        pk[2][0] = pack2(o2[0] + bf_lo(xr[2][0]), o2[1] + bf_hi(xr[2][0])); pk[2][1] = pack2(o2[2] + bf_lo(xr[2][1]), o2[3] + bf_hi(xr[2][1]));
    }
    {
        auto s0 = __builtin_amdgcn_permlane16_swap(pk[0][0], pk[1][0], false, false), s1 = __builtin_amdgcn_permlane16_swap(pk[0][1], pk[1][1], false, false);
        auto t0 = __builtin_amdgcn_permlane16_swap(pk[2][0], pk[2][0], false, false), t1 = __builtin_amdgcn_permlane16_swap(pk[2][1], pk[2][1], false, false);
        const u32x4 d = {s0[0], s1[0], s0[1], s1[1]}, e = {t0[0], t1[0], t0[1], t1[1]};
        const int n0 = 72 * jt + 32 * kh + 16 * (g & 1) + 8 * (g >> 1), n1 = 72 * jt + 64;
        if (last) {
            typedef __attribute__((address_space(1))) u32x4* g_u4;
            unsigned short* orow = out + (size_t)(rb * 16 + m) * 576;
            *(g_u4)(orow + n0) = d;
            if (kh == 0 && g == 0) *(g_u4)(orow + n1) = e;
        } else {
            char* xo = ws + OFF_XF + ((xv + 1) & 1) * XF_BYTES + (size_t)rb * 18 * 1024 + m * 16;
            gst16c(xo + (n0 >> 5) * 1024 + ((n0 >> 3) & 3) * 256, d);
            if (kh == 0 && g == 0) gst16c(xo + (n1 >> 5) * 1024 + ((n1 >> 3) & 3) * 256, e);
        }
    }
    if (!last) {
        const bool v2 = kh == 0 && g < 2;
        float v[12];
#pragma unroll
        for (int c = 0; c < 3; ++c) { v[4 * c] = bf_lo(pk[c][0]); v[4 * c + 1] = bf_hi(pk[c][0]); v[4 * c + 2] = bf_lo(pk[c][1]); v[4 * c + 3] = bf_hi(pk[c][1]); }
        float s = ((v[0] + v[1]) + (v[2] + v[3])) + ((v[4] + v[5]) + (v[6] + v[7])) + (v2 ? (v[8] + v[9]) + (v[10] + v[11]) : 0.f);
        s = rowsum4(s);
        const float mean = s * (kh ? (1.f / 32.f) : (1.f / 40.f));
        float q = 0.f, q2 = 0.f;
#pragma unroll
        for (int r = 0; r < 8; ++r) { const float dd = v[r] - mean; q += dd * dd; }
#pragma unroll
        for (int r = 8; r < 12; ++r) { const float dd = v[r] - mean; q2 += dd * dd; }
        q += v2 ? q2 : 0.f;
        q = rowsum4(q);
        if (g == 0) gst8c(ws + OFF_ST + ((xv + 1) & 1) * ST_BYTES + (size_t)(rb * 16 + m) * 128 + jt * 16 + kh * 8, (f32x2){s, q});
        const int line = L * C_LAY + (mode ? C_WB : C_WA) + (panel >> 1);
        signal_cnt<0>(ws, line);
        if (FUSED) { PF(pf, 2); PF(pf, 3); PF(pf, 4); PF(pf, 5); PF(pf, 6); PFQ_LAST(pf); }
    }
}

__device__ __forceinline__ float half_max(float v) {
    auto r = __builtin_amdgcn_permlane32_swap(__float_as_uint(v), __float_as_uint(v), false, false);
    return fmaxf(__uint_as_float(r[0]), __uint_as_float(r[1]));
}
#define STR_(x) #x
#define STR(x) STR_(x)
#define AEXP(r) "v_exp_f32 v" STR(r) ", v" STR(r) "\n\t"
#define ACVT(d, r0, r1) "v_cvt_pk_bf16_f32 v" STR(d) ", v" STR(r0) ", v" STR(r1) "\n\t"
#define AE4(r0, r1, r2, r3, d0, d1) AEXP(r0) AEXP(r1) AEXP(r2) AEXP(r3) ACVT(d0, r0, r1) ACVT(d1, r2, r3)
#define ATILE(KO, K2O, V1O, V0O, XK, X0, X1, XV) \
    "v_mfma_f32_32x32x16_bf16 v[48:63], v[80:83], v[72:75], 0\n\t" XK \
    "ds_read_b128 v[80:83], v104 offset:" STR(KO) "\n\t" \
    AE4(32, 33, 34, 35, 96, 97) \
    "v_mfma_f32_32x32x16_bf16 v[16:31], v[92:95], v[100:103], v[16:31]\n\t" \
    "ds_read_b128 v[92:95], v106 offset:" STR(V1O) "\n\t" X0 \
    AE4(36, 37, 38, 39, 98, 99) \
    "v_mfma_f32_32x32x16_bf16 v[48:63], v[84:87], v[76:79], v[48:63]\n\t" \
    "ds_read_b32 v84, v105 offset:" STR(K2O) "\n\t" \
    AE4(40, 41, 42, 43, 100, 101) \
    "s_waitcnt lgkmcnt(3)\n\t" \
    "v_mfma_f32_32x32x16_bf16 v[0:15], v[88:91], v[96:99], v[0:15]\n\t" \
    AE4(44, 45, 46, 47, 102, 103) \
    "s_waitcnt lgkmcnt(1)\n\t" \
    "v_mfma_f32_32x32x16_bf16 v[32:47], v[80:83], v[64:67], 0\n\t" \
    AE4(48, 49, 50, 51, 96, 97) \
    "v_mfma_f32_32x32x16_bf16 v[0:15], v[92:95], v[100:103], v[0:15]\n\t" X1 \
    AE4(52, 53, 54, 55, 98, 99) \
    "s_waitcnt lgkmcnt(0)\n\t" \
    "v_mfma_f32_32x32x16_bf16 v[32:47], v[84:87], v[68:71], v[32:47]\n\t" \
    AE4(56, 57, 58, 59, 100, 101) \
    "v_mfma_f32_32x32x16_bf16 v[16:31], v[88:91], v[96:99], v[16:31]\n\t" XV \
    "ds_read_b128 v[88:91], v106 offset:" STR(V0O) "\n\t" \
    AE4(60, 61, 62, 63, 102, 103)
#define ACHUNK \
    "s_mov_b32 m0, s22\n\ts_add_u32 s28, s20, 0x2000\n\ts_addc_u32 s29, s21, 0\n\tglobal_load_lds_dwordx4 v107, s[20:21] sc1\n\t" \
    "s_add_u32 s27, s22, 0x2000\n\ts_mov_b32 m0, s27\n\ts_cmp_lt_u32 %[w], 3\n\tglobal_load_lds_dwordx4 v107, s[28:29] sc1\n\t" \
    "s_cbranch_scc0 7f\n\t" \
    "s_add_u32 s27, s22, 0x4000\n\ts_mov_b32 m0, s27\n\ts_add_u32 s28, s20, 0x4000\n\ts_addc_u32 s29, s21, 0\n\tglobal_load_lds_dwordx4 v107, s[28:29] sc1\n\t" \
    "7:\n\t" \
    "s_add_u32 s20, s20, 0x4c00\n\ts_addc_u32 s21, s21, 0\n\t"
#define AWPF \
    "s_mov_b32 m0, s25\n\ts_add_u32 s25, s25, 0x4000\n\tglobal_load_lds_dwordx4 v107, s[30:31]\n\tglobal_load_lds_dwordx4 v107, s[30:31] offset:1024\n\t" \
    "s_add_u32 s30, s30, 0x4000\n\ts_addc_u32 s31, s31, 0\n\t"
#define AKADV "v_add_u32 v104, s23, v104\n\tv_add_u32 v105, s23, v105\n\t"
#define AVADV "v_add_u32 v106, s23, v106\n\t"

template <bool FUSED>
__device__ __forceinline__ void attn_phase(char* ws, char* smem, int L, int w, int lane) {
    LAUNDER();
    const int r32 = lane & 31, hh = lane >> 5, kh = w >> 2;
    const int xcd = bid & 7, idx = bid >> 3;
    const int head = 4 * xcd + (idx >> 3), qb = idx & 7;
    const int qA = qb * 256 + (w & 3) * 64 + r32;
    char* lay = ws + OFF_LAY + 0 * LAY_BYTES;
    const unsigned lds0 = lds_off(smem) + w * 1024;
    const Polls pl = wait_cnt(ws, L * C_LAY + C_QB + (head >> 1), 16);
    bf16x8 q1a, q1b; unsigned q2wa, q2wb;
    {
        const char* qp = lay + LO_Q1 + ((size_t)head * SEQ + qA) * 32 + hh * 16;
        const char* q2p = lay + LO_Q2 + ((size_t)head * SEQ + qA) * 4;
        gld16c<0>(q1a, qp); gld16c<1024>(q1b, qp); gld4c<0>(q2wa, q2p); gld4c<128>(q2wb, q2p);
    }
    const char* srcw = lay + LO_IMG + (size_t)head * IMG_HEAD + w * 1024;
    {
        const char* src = srcw + lane * 16;
#pragma unroll
        for (int k = 0; k < 4; ++k) glds1_sc1(src + k * 8192, lds0 + k * 8192);
        glds1_sc1(src + (w < 6 ? 4 : 3) * 8192, lds0 + (w < 6 ? 4 : 3) * 8192);
    }
    lds_cptr kp1 = (lds_cptr)smem + lane * 16;
    lds_cptr kp2 = (lds_cptr)smem + 8192 + r32 * 4;
    const int dmap = r32 < 19 ? r32 : 19;
    lds_cptr vp = (lds_cptr)smem + 9216 + hh * 320 + dmap * 16;
    asm volatile("s_waitcnt vmcnt(2)\n\ts_barrier" : "+v"(q1a), "+v"(q1b), "+v"(q2wa), "+v"(q2wb) :: "memory");
    RETIRE(pl);
#define LDK1(t) (*(lds_f8)(kp1 + (t) * 1024))
#define LDK2(t) (__builtin_bit_cast(bf16x8, (u32x4){*(lds_vu32)(kp2 + (t) * 128), 0x3f80u, 0u, 0u}))
#define LDV(u) (*(lds_f8)(vp + (u) * 640))
    const f32x16 z16 = {};
    f32x16 s0, s1, oa = {}, ob = {};
    bf16x8 k1 = LDK1(0), k2 = LDK2(0), v0, v1, q2a, q2b;
    u32x4 p0, p1 = {0u, 0u, 0u, 0u};
    {
        q2a = __builtin_bit_cast(bf16x8, (u32x4){hh ? 0u : q2wa, 0u, 0u, 0u});
        q2b = __builtin_bit_cast(bf16x8, (u32x4){hh ? 0u : q2wb, 0u, 0u, 0u});
        f32x16 sa = MFMA32(k1, q1a, z16), sb = MFMA32(k1, q1b, z16); sa = MFMA32(k2, q2a, sa); sb = MFMA32(k2, q2b, sb);
        kp1 += kh * 4096; kp2 += kh * 512; vp += kh * 5120;
        k1 = LDK1(0); k2 = LDK2(0); v0 = LDV(0); v1 = v0;
        float ma = fmaxf(sa[0], sa[1]), mb = fmaxf(sb[0], sb[1]);
#pragma unroll
        for (int i = 2; i < 16; ++i) { ma = fmaxf(ma, sa[i]); mb = fmaxf(mb, sb[i]); }
        ma = half_max(ma); mb = half_max(mb);
        q2a = __builtin_bit_cast(bf16x8, (u32x4){hh ? 0u : q2wa, hh ? 0u : (pack2(-ma, 0.f) & 0xffffu), 0u, 0u});
        q2b = __builtin_bit_cast(bf16x8, (u32x4){hh ? 0u : q2wb, hh ? 0u : (pack2(-mb, 0.f) & 0xffffu), 0u, 0u});
    }
    s0 = MFMA32(k1, q1a, z16); s0 = MFMA32(k2, q2a, s0);
    {
        unsigned a1 = (unsigned)(size_t)kp1, a2 = (unsigned)(size_t)kp2, a3 = (unsigned)(size_t)vp;
        const unsigned loff = lane * 16;
        const char* dsrc = srcw + 2 * IMG_CHUNK; const unsigned dbase = lds0;
        const int jt = (bid >> 3) & 7;
        const char* wsrc = ws + OFF_WOB + (size_t)jt * WO_TILE + w * 2048; const unsigned wdst = lds_off(smem) + LDS_WO + w * 2048;
        asm volatile(
            "s_mov_b32 s26, m0\n\t"
            "s_mov_b64 s[20:21], %[dsrc]\n\t"
            "s_add_u32 s22, %[dbase], 0x9800\n\t"
            "s_mov_b64 s[30:31], %[wsrc]\n\t"
            "s_mov_b32 s25, %[wdst]\n\t"
            "s_mov_b32 s24, 0\n\t"
            "s_mov_b32 s33, 0\n\t"
            "s_mov_b32 s34, 0x4c00\n\t"
            "s_waitcnt lgkmcnt(0)\n\t"
            "s_nop 7\n\t"
            "s_nop 3\n\t"
            "2:\n\t"
            "s_setprio 2\n\t"
            ATILE(1024, 128, 640, 1280, "", "", "", "")
            "s_setprio 1\n\t"
            "s_cmp_eq_u32 s33, 2\n\t"
            "s_cselect_b32 s23, 0xffff6800, s34\n\t"
            "s_sub_u32 s35, s24, 1\n\t"
            "s_cmp_lt_u32 s35, %[npf]\n\t"
            "s_cbranch_scc0 3f\n\t"
            AWPF
            "3:\n\t"
            ATILE(2048, 256, 1920, 2560, "", "", "", "")
            "s_setprio 0\n\t"
            ATILE(3072, 384, 3200, 3840, "", "", "", "")
            "s_waitcnt vmcnt(2)\n\t"
            "s_cmp_lt_u32 s35, %[npf]\n\t"
            "s_cbranch_scc1 4f\n\t"
            "s_waitcnt vmcnt(0)\n\t"
            "4:\n\t"
            "s_barrier\n\t"
            "s_cmp_lt_u32 s24, 6\n\t"
            "s_cbranch_scc0 5f\n\t"
            ACHUNK
            "s_cmp_eq_u32 s33, 0\n\t"
            "s_cselect_b32 s27, 0xffff6800, s34\n\t"
            "s_add_u32 s22, s22, s27\n\t"
            "5:\n\t"
            "s_setprio 3\n\t"
            ATILE(0, 0, 4480, 0, AKADV, "", "", AVADV)
            "s_add_u32 s24, s24, 1\n\t"
            "s_add_u32 s33, s33, 1\n\t"
            "s_cmp_eq_u32 s33, 3\n\t"
            "s_cselect_b32 s33, 0, s33\n\t"
            "s_cmp_lt_u32 s24, 8\n\t"
            "s_cbranch_scc1 2b\n\t"
            "s_waitcnt lgkmcnt(0)\n\t"
            "v_mfma_f32_32x32x16_bf16 v[16:31], v[92:95], v[100:103], v[16:31]\n\t"
            "s_mov_b32 m0, s26\n\t"
            "s_setprio 0\n\t"
            "s_nop 7\n\t"
            "s_nop 3\n\t"
            : "+{v[0:15]}"(oa), "+{v[16:31]}"(ob), "+{v[32:47]}"(s0), "={v[48:63]}"(s1), "+{v[80:83]}"(k1), "+{v[84:87]}"(k2), "+{v[88:91]}"(v0), "+{v[92:95]}"(v1),
              "={v[96:99]}"(p0), "+{v[100:103]}"(p1), "+{v104}"(a1), "+{v105}"(a2), "+{v106}"(a3)
            : "{v[64:67]}"(q1a), "{v[68:71]}"(q2a), "{v[72:75]}"(q1b), "{v[76:79]}"(q2b), "{v107}"(loff), [dsrc] "s"(dsrc), [dbase] "s"(dbase),
              [wsrc] "s"(wsrc), [wdst] "s"(wdst), [w] "s"(w), [npf] "s"(FUSED ? (w < 5 ? 6 : 5) : 0)
            : "s20", "s21", "s22", "s23", "s24", "s25", "s26", "s27", "s28", "s29", "s30", "s31", "s33", "s34", "s35", "scc", "memory");
    }
    lds_ptr ex = (lds_ptr)smem + (w < 6 ? w * 3072 : 2 * (int)IMG_CHUNK + 12288 + (w - 6) * 3072) + lane * 16;
    const int wo_ = w ^ 4;
    lds_ptr ey = (lds_ptr)smem + (wo_ < 6 ? wo_ * 3072 : 2 * (int)IMG_CHUNK + 12288 + (wo_ - 6) * 3072) + lane * 16;
    f32x16 o;
    {
        const f32x16 snd = kh ? oa : ob; o = kh ? ob : oa;
        *(lds_f4)(ex) = (f32x4){snd[0], snd[1], snd[2], snd[3]}; *(lds_f4)(ex + 1024) = (f32x4){snd[4], snd[5], snd[6], snd[7]};
        *(lds_f4)(ex + 2048) = (f32x4){snd[8], snd[9], snd[10], snd[11]};
    }
    asm volatile("s_waitcnt lgkmcnt(0)\n\ts_barrier" ::: "memory");
    {
        const f32x4 a = *(lds_f4)(ey), b = *(lds_f4)(ey + 1024), c = *(lds_f4)(ey + 2048);
#pragma unroll
        for (int i = 0; i < 4; ++i) { o[i] += a[i]; o[4 + i] += b[i]; o[8 + i] += c[i]; }
    }
    const int q = qA + 32 * kh;
    bool bad = !(fabsf(o[10]) < 1.0e30f);
#pragma unroll
    for (int i = 0; i < 10; ++i) bad = bad || !(fabsf(o[i]) < 1.0e38f);
    if (__any(bad)) {
        o = (f32x16){};
        float mrun = -INFINITY;
        const bf16x8 q1 = kh ? q1b : q1a;
        const bf16x8 q2 = __builtin_bit_cast(bf16x8, (u32x4){hh ? 0u : (kh ? q2wb : q2wa), 0u, 0u, 0u});
        const char* img = lay + LO_IMG + (size_t)head * IMG_HEAD;
#pragma unroll 1
        for (int t = 0; t < 64; ++t) {
            const char* cb = img + (t >> 3) * IMG_CHUNK; const int tt = t & 7;
            const char* vb = cb + 9216 + tt * 1280 + hh * 320 + dmap * 16;
            bf16x8 fk, fv0, fv1; unsigned fk2;
            gld16c<0>(fk, cb + tt * 1024 + lane * 16); gld4c<0>(fk2, cb + 8192 + tt * 128 + r32 * 4); gld16c<0>(fv0, vb); gld16c<640>(fv1, vb);
            asm volatile("s_waitcnt vmcnt(0)" : "+v"(fk), "+v"(fk2), "+v"(fv0), "+v"(fv1) :: "memory");
            f32x16 s = MFMA32(fk, q1, z16);
            s = MFMA32(__builtin_bit_cast(bf16x8, (u32x4){fk2, 0u, 0u, 0u}), q2, s);
            float mx = fmaxf(s[0], s[1]);
#pragma unroll
            for (int i = 2; i < 16; ++i) mx = fmaxf(mx, s[i]);
            mx = fmaxf(half_max(mx), mrun);
            const float al = EXP2(mrun - mx);
            mrun = mx;
#pragma unroll
            for (int i = 0; i < 16; ++i) o[i] *= al;
#pragma unroll
            for (int i = 0; i < 4; ++i) {
                p0[i] = pack2(EXP2(s[2 * i] - mx), EXP2(s[2 * i + 1] - mx));
                p1[i] = pack2(EXP2(s[8 + 2 * i] - mx), EXP2(s[8 + 2 * i + 1] - mx));
            }
            o = MFMA32(fv0, __builtin_bit_cast(bf16x8, p0), o);
            o = MFMA32(fv1, __builtin_bit_cast(bf16x8, p1), o);
        }
    }
    {
        auto r = __builtin_amdgcn_permlane32_swap(__float_as_uint(o[10]), __float_as_uint(o[10]), false, false);
        const float rl = __builtin_amdgcn_rcpf(__uint_as_float(r[0]));
        const int rb = q >> 4, m = q & 15;
        gst16c(lay + LO_O + O_BYTES + ((size_t)(rb * 16 + (head >> 1)) * 64 + (2 * (head & 1) + hh) * 16 + m) * 16,
               (u32x4){pack2(o[0] * rl, o[1] * rl), pack2(o[2] * rl, o[3] * rl), pack2(o[4] * rl, o[5] * rl), pack2(o[6] * rl, o[7] * rl)});
        const unsigned t0 = pack2(o[8] * rl, o[9] * rl);
        const unsigned t1 = __builtin_amdgcn_mov_dpp(t0, 0x55, 0xf, 0xf, true), t2 = __builtin_amdgcn_mov_dpp(t0, 0xAA, 0xf, 0xf, true),
                       t3 = __builtin_amdgcn_mov_dpp(t0, 0xFF, 0xf, 0xf, true);
        if (hh == 0 && (r32 & 3) == 0) gst16c(lay + LO_O + O_BYTES + O_TAIL + ((size_t)(rb * 32 + head) * 16 + m) * 4, (u32x4){t0, t1, t2, t3});
    }
    signal_cnt<0>(ws, L * C_LAY + C_AT + qb);
#undef LDK1
#undef LDK2
#undef LDV
}

__global__ __launch_bounds__(512) void prep_kernel(const unsigned short* __restrict__ x0, const unsigned short* __restrict__ wA,
                                                   const unsigned short* __restrict__ woA, const unsigned short* __restrict__ wB,
                                                   const unsigned short* __restrict__ woB, char* ws) {
    __shared__ __attribute__((aligned(16))) char tbuf[7 * 4096];
    __shared__ __attribute__((aligned(16))) float red[72 * 16];
    const int tid = threadIdx.x, bid = blockIdx.x, w = __builtin_amdgcn_readfirstlane(tid >> 6), l = tid & 63;
    const int lin = (bid & 7) * 27 + (bid >> 3);
    const bool jobW = (bid >> 3) < 27;
    const int cmat = lin >= 108, cblk = lin - 108 * cmat;
    u32x2 sv[8]; u32x4 xv[9];
    if (w < 5) {
        if (jobW) {
            const int T = min(tid, 287);
            const char* p = (const char*)((cmat ? wB : wA) + (size_t)(8 * (T >> 2)) * 1728 + 16 * cblk + 4 * (T & 3));
            gld8<0>(sv[0], p); gld8<3456>(sv[1], p); p += 6912; gld8<0>(sv[2], p); gld8<3456>(sv[3], p); p += 6912;
            gld8<0>(sv[4], p); gld8<3456>(sv[5], p); p += 6912; gld8<0>(sv[6], p); gld8<3456>(sv[7], p);
        }
    } else if (w < 7) {
        const int T = bid * 90 + min(tid - 320, 89), jmat = T / 11520, r = T - jmat * 11520, jsg = r / 160, jq = r - jsg * 160;
        const int jt = jq / 20, lc = min(4 * (jq - jt * 20), 68), ss = jsg >> 2, gg = jsg & 3;
        int r0, rb_, rc;
        if (ss >= 16) { r0 = (16 * (ss - 16) + 4 * gg) * 18 + 16; rb_ = 18; rc = 36; }
        else if (jmat == 0) { r0 = 36 * ss + 4 * gg; rb_ = 2; rc = 18; }
        else { r0 = (2 * ss + (gg >> 1)) * 18 + 4 * (gg & 1); rb_ = 2; rc = 8; }
        const char* p = (const char*)((jmat ? woB : woA) + (size_t)r0 * 576 + 72 * jt + lc);
        const char* p2 = p + rb_ * 1152; const char* p4 = p + rc * 1152; const char* p6 = p4 + rb_ * 1152;
        gld8<0>(sv[0], p); gld8<1152>(sv[1], p); gld8<0>(sv[2], p2); gld8<1152>(sv[3], p2);
        gld8<0>(sv[4], p4); gld8<1152>(sv[5], p4); gld8<0>(sv[6], p6); gld8<1152>(sv[7], p6);
    } else {
        const int T = bid * 64 + l, m = T & 15, j = (T >> 4) & 7, rb = T >> 7;
        const char* p = (const char*)(x0 + (size_t)(rb * 16 + m) * 576 + 72 * j);
        gld16<0>(xv[0], p); gld16<16>(xv[1], p); gld16<32>(xv[2], p); gld16<48>(xv[3], p); gld16<64>(xv[4], p);
        gld16<80>(xv[5], p); gld16<96>(xv[6], p); gld16<112>(xv[7], p); gld16<128>(xv[8], p);
    }
    int tct, tcb, tc; qkv_col_map(16 * cblk + (l & 15), tct, tcb, tc);
    asm volatile("s_waitcnt vmcnt(0)" : "+v"(sv[0]), "+v"(sv[1]), "+v"(sv[2]), "+v"(sv[3]), "+v"(sv[4]), "+v"(sv[5]), "+v"(sv[6]), "+v"(sv[7]) :: "memory");
    asm volatile("" : "+v"(xv[0]), "+v"(xv[1]), "+v"(xv[2]), "+v"(xv[3]), "+v"(xv[4]), "+v"(xv[5]), "+v"(xv[6]), "+v"(xv[7]), "+v"(xv[8]));
    u32x4 cc[4];
    if (w < 7) {
        transpose8x4(sv, cc);
        lds_ptr tb = (lds_ptr)tbuf + w * 4096;
#pragma unroll
        for (int i = 0; i < 4; ++i) *(lds_u4)(tb + (l * 4 + i) * 16) = cc[i];
#pragma unroll
        for (int i = 0; i < 4; ++i) cc[i] = *(lds_u4)(tb + (i * 64 + l) * 16);
        if (w < 5) {
#pragma unroll
            for (int i = 0; i < 4; ++i) {
                const int sg = 16 * w + 4 * i + (l >> 4);
                if (sg < 72) red[sg * 16 + (l & 15)] = dot_ones(cc[i][3], dot_ones(cc[i][2], dot_ones(cc[i][1], dot_ones(cc[i][0], 0.f))));
            }
        }
    }
    if (w < 5) {
        if (jobW) {
            char* dst = ws + (cmat ? OFF_WTB : OFF_WTA) + ((size_t)(tct * 18 * 7 + tcb) * 64 + (l >> 4) * 16 + tc) * 16;
#pragma unroll
            for (int i = 0; i < 4; ++i)
                if (4 * w + i < 18) gst16c(dst + (4 * w + i) * 7168, cc[i]);
        }
    } else if (w < 7) {
#pragma unroll
        for (int i = 0; i < 4; ++i) {
            const int loc = (w - 5) * 256 + i * 64 + l;
            const int X = bid * 360 + loc, mat = X / 46080, r = X - mat * 46080, sg = r / 640, n = r - sg * 640, jt = n / 80, lc = n - jt * 80;
            const int ss = sg >> 2, okh = ss < 16 ? (ss >> 3) : (ss - 16), oi = ss < 16 ? (ss & 7) : 8;
            if (loc < 360) gst16c(ws + (mat ? OFF_WOB : OFF_WOA) + (size_t)jt * WO_TILE + (size_t)((oi * 2 + okh) * 5 + (lc >> 4)) * 1024 + ((sg & 3) * 16 + (lc & 15)) * 16,
                                  lc < 72 ? cc[i] : (u32x4){0u, 0u, 0u, 0u});
        }
    } else {
        const int T = bid * 64 + l, m = T & 15, j = (T >> 4) & 7, rb = T >> 7;
        float sa = 0.f, sb = 0.f;
#pragma unroll
        for (int i = 0; i < 9; ++i) {
            const int q = 9 * j + i;
            gst16c(ws + OFF_XF + ((size_t)(rb * 18 + (q >> 2)) * 64 + (q & 3) * 16 + m) * 16, xv[i]);
            const float a = dot_ones(xv[i][3], dot_ones(xv[i][2], dot_ones(xv[i][1], dot_ones(xv[i][0], 0.f))));
            if (i < 4 || i == 8) sa += a; else sb += a;
        }
        const float ma = sa * (1.f / 40.f), mb = sb * (1.f / 32.f);
        float qa = 0.f, qb = 0.f;
#pragma unroll
        for (int i = 0; i < 9; ++i) {
            const bool pa = i < 4 || i == 8; const float mu = pa ? ma : mb; float a = 0.f;
#pragma unroll
            for (int k = 0; k < 4; ++k) { const float d0 = bf_lo(xv[i][k]) - mu, d1 = bf_hi(xv[i][k]) - mu; a += d0 * d0 + d1 * d1; }
            if (pa) qa += a; else qb += a;
        }
        gst16c(ws + OFF_ST + (size_t)(rb * 16 + m) * 128 + j * 16, __builtin_bit_cast(u32x4, (f32x4){sa, qa, sb, qb}));
    }
    if (w == 6) {
        if (l < 36) {
            const int Z = bid * 36 + l, zm = Z / 4608, z2 = Z - zm * 4608, zct = z2 / 288, r = z2 - zct * 288;
            gst16c(ws + (zm ? OFF_WTB : OFF_WTA) + ((size_t)((zct * 18 + (r >> 4)) * 7 + 6) * 64 + ((r >> 2) & 3) * 16 + 12 + (r & 3)) * 16, (u32x4){0u, 0u, 0u, 0u});
        } else if (l < 49) {
            const int Z = bid * 13 + (l - 36);
            if ((size_t)Z * 16 < ZERO_BYTES) gst16c(ws + (size_t)Z * 16, (u32x4){0u, 0u, 0u, 0u});
        } else if (l == 49 && bid < 32) gst16c(ws + ((bid & 16) ? OFF_CSB : OFF_CSA) + ((bid & 15) * 112 + 108) * 4, (u32x4){0u, 0u, 0u, 0u});
    }
    asm volatile("s_waitcnt lgkmcnt(0)\n\ts_barrier" ::: "memory");
    if (jobW && w == 0) {
        float a = 0.f;
#pragma unroll
        for (int i = 0; i < 18; ++i) a += red[((l >> 4) * 18 + i) * 16 + (l & 15)];
        a = rowsum4(a);
        if (l < 16) __hip_atomic_store((gu32*)(ws + (cmat ? OFF_CSB : OFF_CSA)) + tct * 112 + tcb * 16 + tc, __float_as_uint(a), RLX_AGENT);
    }
}

template <int MASK>
__global__ __launch_bounds__(512, 2) void mega_kernel(char* ws, const unsigned short* x0, unsigned short* out, int h_lo, int h_hi) {
    __shared__ __attribute__((aligned(16))) char smem[160 * 1024];
    const int lane = threadIdx.x & 63, w = __builtin_amdgcn_readfirstlane(threadIdx.x >> 6);
    constexpr bool FUSED = MASK == 7;
    if (FUSED && w == 7 && (blockIdx.x >> 3) < 4) {
        unsigned long long pc; asm volatile("s_getpc_b64 %0" : "=s"(pc));
        const char* cp = (const char*)(pc & ~127ull) + (blockIdx.x >> 3) * 5120 + min(lane, 39) * 128;
        unsigned t; asm volatile("global_load_dword %0, %1, off\n\ts_waitcnt vmcnt(0)" : "=v"(t) : "v"(cp) : "memory");
    }
#pragma unroll 1
    for (int hl = h_lo; hl < h_hi; ++hl) {
        const int L = hl >> 1;
        if (hl & 1) {
            if (MASK & 1) qkv_phase<1, FUSED>(ws, smem, L, w, lane);
            if (MASK & 2) attn_phase<FUSED>(ws, smem, L, w, lane);
        } else {
            if (MASK & 1) qkv_phase<0, FUSED>(ws, smem, L, w, lane);
        }
        if (MASK & 4) wo_phase<FUSED>(ws, smem, x0, out, L, hl & 1, w, lane);
    }
}

extern "C" void kernel_launch(void* const* d_in, const int* in_sizes, int n_in, void* d_out, int out_size, void* d_ws, size_t ws_size,
                              hipStream_t stream) {
    const unsigned short* x0 = (const unsigned short*)d_in[0];
    const unsigned short* wA = (const unsigned short*)d_in[1];
    const unsigned short* woA = (const unsigned short*)d_in[2];
    const unsigned short* wB = (const unsigned short*)d_in[4];
    const unsigned short* woB = (const unsigned short*)d_in[5];
    char* ws = (char*)d_ws;
    unsigned short* out = (unsigned short*)d_out;
    prep_kernel<<<256, 512, 0, stream>>>(x0, wA, woA, wB, woB, ws);
    mega_kernel<7><<<256, 512, 0, stream>>>(ws, x0, out, 0, 4);
}
```

```cpp
#include <hip/hip_runtime.h>
#include <hip/hip_bf16.h>
#include <stdint.h>

typedef __attribute__((ext_vector_type(8))) short bf16x8;
typedef __attribute__((ext_vector_type(4))) short bf16x4;
typedef __attribute__((ext_vector_type(4))) float f32x4;
typedef __attribute__((ext_vector_type(2))) float f32x2;
typedef __attribute__((ext_vector_type(16))) float f32x16;
typedef __attribute__((ext_vector_type(4))) unsigned u32x4;
typedef __attribute__((ext_vector_type(2))) unsigned u32x2;
typedef __attribute__((ext_vector_type(2))) __bf16 bfx2;
#define LDSAS __attribute__((address_space(3)))
typedef LDSAS char* lds_ptr;
typedef const LDSAS char* lds_cptr;
typedef LDSAS f32x4* lds_f4;
typedef LDSAS f32x2* lds_f2;
typedef LDSAS float* lds_f1;
typedef LDSAS u32x4* lds_u4;
typedef const volatile LDSAS unsigned* lds_vu32;
typedef const LDSAS bf16x8* lds_f8;

constexpr int SEQ = 2048, DM = 576, NH = 32;

constexpr size_t WT_TILE = 126 * 1024;
constexpr size_t WO_TILE = 90 * 1024;
constexpr size_t IMG_CHUNK = 19 * 1024;
constexpr size_t IMG_HEAD = 8 * IMG_CHUNK;
constexpr int KV_HEAD = 19 * 20;
constexpr size_t XF_BYTES = (size_t)SEQ * DM * 2, ST_BYTES = (size_t)SEQ * 128;
constexpr size_t OFF_CNT = 0;
constexpr size_t CNT_BYTES = 2 * 96 * 256 + 256;
constexpr size_t ZERO_BYTES = 49664;
constexpr size_t OFF_KV  = 65536;
constexpr size_t OFF_WTA = OFF_KV + 2 * 16 * 16 * 3072;
constexpr size_t OFF_WTB = OFF_WTA + 16 * WT_TILE + 4096;
constexpr size_t OFF_CSA = OFF_WTB + 16 * WT_TILE + 4096;
constexpr size_t OFF_CSB = OFF_CSA + 8192;
constexpr size_t OFF_WOA = OFF_CSB + 8192;
constexpr size_t OFF_WOB = OFF_WOA + 8 * WO_TILE + 8192;
constexpr size_t OFF_XF  = OFF_WOB + 8 * WO_TILE + 8192;
constexpr size_t OFF_ST  = OFF_XF + 4 * XF_BYTES;
constexpr size_t OFF_LAY = OFF_ST + 4 * ST_BYTES;
constexpr size_t O_BYTES = (size_t)128 * 16 * 1024 + (size_t)128 * 32 * 64;
constexpr size_t O_TAIL = (size_t)128 * 16 * 1024;
constexpr size_t LO_O   = 0;
constexpr size_t LO_Q1  = LO_O + 2 * O_BYTES;
constexpr size_t LO_Q2  = LO_Q1 + (size_t)NH * SEQ * 32;
constexpr size_t LO_IMG = LO_Q2 + (size_t)NH * SEQ * 4;
constexpr size_t LAY_BYTES = LO_IMG + (size_t)NH * IMG_HEAD;
static_assert(ZERO_BYTES <= OFF_KV && CNT_BYTES <= ZERO_BYTES && ZERO_BYTES % 16 == 0, "layout");
constexpr int C_QA = 0, C_OA = 16, C_WA = 32, C_QB = 48, C_AT = 64, C_WB = 80, C_LAY = 96;

constexpr float QSCALE = 0.23570226039551584f * 1.4426950408889634f;

__device__ __forceinline__ unsigned pack2(float lo, float hi) {
    f32x2 v = {lo, hi};
    bfx2 r = __builtin_convertvector(v, bfx2);
    return __builtin_bit_cast(unsigned, r);
}
__device__ __forceinline__ float bf_lo(unsigned u) { return __uint_as_float(u << 16); }
__device__ __forceinline__ float bf_hi(unsigned u) { return __uint_as_float(u & 0xffff0000u); }
__device__ __forceinline__ float phi_f(float t) { return (t > 0.f ? t + 1.f : __expf(t)) + 1e-6f; }
__device__ __forceinline__ unsigned lds_off(const void* p) { return (unsigned)(size_t)(lds_cptr)p; }

#define MFMA16(a, b, c) __builtin_amdgcn_mfma_f32_16x16x32_bf16(a, b, c, 0, 0, 0)
#define MFMA32(a, b, c) __builtin_amdgcn_mfma_f32_32x32x16_bf16(a, b, c, 0, 0, 0)
#define MFMA16K16(a, b, c) __builtin_amdgcn_mfma_f32_16x16x16bf16_1k(a, b, c, 0, 0, 0)
__device__ __forceinline__ float rowsum4(float v) {
    auto a = __builtin_amdgcn_permlane16_swap(__float_as_uint(v), __float_as_uint(v), false, false);
    v = __uint_as_float(a[0]) + __uint_as_float(a[1]);
    auto b = __builtin_amdgcn_permlane32_swap(__float_as_uint(v), __float_as_uint(v), false, false);
    return __uint_as_float(b[0]) + __uint_as_float(b[1]);
}
#define PIN(x) asm volatile("" : "+v"(x))
#define LAUNDER() int bid = blockIdx.x; asm volatile("" : "+v"(lane)); asm volatile("" : "+s"(w), "+s"(ws), "+s"(bid), "+s"(L))
#define SBAR() __builtin_amdgcn_sched_barrier(0)
#define EXP2(x) __builtin_amdgcn_exp2f(x)

template <int N>
__device__ __forceinline__ void glds(const void* g, unsigned lds) {
    unsigned keep;
    if constexpr (N == 4)
        asm volatile("s_mov_b32 %0, m0\n\ts_mov_b32 m0, %2\n\ts_nop 0\n\t"
                     "global_load_lds_dwordx4 %1, off\n\tglobal_load_lds_dwordx4 %1, off offset:1024\n\t"
                     "global_load_lds_dwordx4 %1, off offset:2048\n\tglobal_load_lds_dwordx4 %1, off offset:3072\n\t"
                     "s_mov_b32 m0, %0" : "=&s"(keep) : "v"(g), "s"(lds) : "memory");
    else
        asm volatile("s_mov_b32 %0, m0\n\ts_mov_b32 m0, %2\n\ts_nop 0\n\t"
                     "global_load_lds_dwordx4 %1, off\n\tglobal_load_lds_dwordx4 %1, off offset:1024\n\t"
                     "s_mov_b32 m0, %0" : "=&s"(keep) : "v"(g), "s"(lds) : "memory");
}
__device__ __forceinline__ void glds1_sc1(const void* g, unsigned lds) {
    unsigned keep;
    asm volatile("s_mov_b32 %0, m0\n\ts_mov_b32 m0, %2\n\ts_nop 0\n\tglobal_load_lds_dwordx4 %1, off sc1\n\ts_mov_b32 m0, %0"
                 : "=&s"(keep) : "v"(g), "s"(lds) : "memory");
}
template <int OFF, class T> __device__ __forceinline__ void gld16(T& d, const void* p) {
    asm volatile("global_load_dwordx4 %0, %1, off offset:%2" : "=v"(d) : "v"(p), "i"(OFF) : "memory"); }
template <int OFF, class T> __device__ __forceinline__ void gld4(T& d, const void* p) {
    asm volatile("global_load_dword %0, %1, off offset:%2" : "=v"(d) : "v"(p), "i"(OFF) : "memory"); }
template <int OFF, class T> __device__ __forceinline__ void gld16c(T& d, const void* p) {
    asm volatile("global_load_dwordx4 %0, %1, off offset:%2 sc1" : "=v"(d) : "v"(p), "i"(OFF) : "memory"); }
template <int OFF, class T> __device__ __forceinline__ void gld8c(T& d, const void* p) {
    asm volatile("global_load_dwordx2 %0, %1, off offset:%2 sc1" : "=v"(d) : "v"(p), "i"(OFF) : "memory"); }
template <int OFF, class T> __device__ __forceinline__ void gld4c(T& d, const void* p) {
    asm volatile("global_load_dword %0, %1, off offset:%2 sc1" : "=v"(d) : "v"(p), "i"(OFF) : "memory"); }
__device__ __forceinline__ void gst16c(void* p, u32x4 v) { asm volatile("global_store_dwordx4 %0, %1, off sc1\n\ts_nop 1" :: "v"(p), "v"(v) : "memory"); }
__device__ __forceinline__ void gst8c(void* p, f32x2 v) { asm volatile("global_store_dwordx2 %0, %1, off sc1" :: "v"(p), "v"(v) : "memory"); }

typedef __attribute__((address_space(1))) unsigned gu32;
#define RLX_AGENT __ATOMIC_RELAXED, __HIP_MEMORY_SCOPE_AGENT
template <int N> __device__ __forceinline__ void signal_cnt(char* ws, int line) {
    asm volatile("s_waitcnt vmcnt(%0) lgkmcnt(0)\n\ts_barrier" :: "i"(N) : "memory");
    if (threadIdx.x == 0) __hip_atomic_fetch_add((gu32*)(ws + OFF_CNT + (size_t)line * 256), 1u, RLX_AGENT);
}
struct Polls { unsigned v0, v1, v2, v3; };
#define RETIRE(pl) asm volatile("" :: "v"((pl).v0), "v"((pl).v1), "v"((pl).v2), "v"((pl).v3))
__device__ __forceinline__ Polls wait_cnt(char* ws, int line, unsigned want) {
    unsigned v0, v1, v2, v3, r, n;
    const char* p = ws + OFF_CNT + (size_t)line * 256;
    const unsigned poller = __builtin_amdgcn_readfirstlane(threadIdx.x < 64 ? 1u : 0u);
    asm volatile(
        "s_cmp_eq_u32 %[poller], 0\n\t"
        "s_cbranch_scc1 3f\n\t"
        "global_load_dword %[v0], %[p], off sc1\n\ts_sleep 1\n\tglobal_load_dword %[v1], %[p], off sc1\n\ts_sleep 1\n\t"
        "global_load_dword %[v2], %[p], off sc1\n\ts_sleep 1\n\tglobal_load_dword %[v3], %[p], off sc1\n\t"
        "s_mov_b32 %[n], 0x20000\n\t"
        "1:\n\t"
        "s_waitcnt vmcnt(3)\n\tv_readfirstlane_b32 %[r], %[v0]\n\ts_nop 0\n\tglobal_load_dword %[v0], %[p], off sc1\n\ts_cmp_ge_u32 %[r], %[want]\n\ts_cbranch_scc1 4f\n\t"
        "s_waitcnt vmcnt(3)\n\tv_readfirstlane_b32 %[r], %[v1]\n\ts_nop 0\n\tglobal_load_dword %[v1], %[p], off sc1\n\ts_cmp_ge_u32 %[r], %[want]\n\ts_cbranch_scc1 4f\n\t"
        "s_waitcnt vmcnt(3)\n\tv_readfirstlane_b32 %[r], %[v2]\n\ts_nop 0\n\tglobal_load_dword %[v2], %[p], off sc1\n\ts_cmp_ge_u32 %[r], %[want]\n\ts_cbranch_scc1 4f\n\t"
        "s_waitcnt vmcnt(3)\n\tv_readfirstlane_b32 %[r], %[v3]\n\ts_nop 0\n\tglobal_load_dword %[v3], %[p], off sc1\n\ts_cmp_ge_u32 %[r], %[want]\n\ts_cbranch_scc1 4f\n\t"
        "s_sub_u32 %[n], %[n], 1\n\t"
        "s_cmp_lg_u32 %[n], 0\n\t"
        "s_cbranch_scc1 1b\n\t"
        "3:\n\t"
        "s_waitcnt vmcnt(0)\n\t"
        "4:\n\t"
        "s_waitcnt lgkmcnt(0)\n\t"
        "s_barrier"
        : [v0] "=&v"(v0), [v1] "=&v"(v1), [v2] "=&v"(v2), [v3] "=&v"(v3), [r] "=&s"(r), [n] "=&s"(n)
        : [p] "v"(p), [want] "s"(want), [poller] "s"(poller) : "scc", "memory");
    return Polls{v0, v1, v2, v3};
}

__device__ __forceinline__ void qkv_col_map(int n, int& ct, int& cb, int& c) {
    const int part = n / 576, rem = n - part * 576, head = rem / 18, d = rem - head * 18;
    ct = head >> 1; const int hb = head & 1;
    if (d < 16) { cb = part * 2 + hb; c = d; } else { cb = 6; c = part * 4 + hb * 2 + (d - 16); }
}
__device__ __forceinline__ float dot_ones(unsigned u, float acc) {
    return __builtin_amdgcn_fdot2_f32_bf16(__builtin_bit_cast(bfx2, u), __builtin_bit_cast(bfx2, 0x3f803f80u), acc, false); }
__device__ __forceinline__ float dot_self(unsigned u, float acc) {
    return __builtin_amdgcn_fdot2_f32_bf16(__builtin_bit_cast(bfx2, u), __builtin_bit_cast(bfx2, u), acc, false); }
__device__ __forceinline__ void transpose8x4(const u32x2 (&v)[8], u32x4 (&c)[4]) {
#pragma unroll
    for (int k = 0; k < 4; ++k) {
        c[0][k] = __builtin_amdgcn_perm(v[2 * k + 1][0], v[2 * k][0], 0x05040100u); c[1][k] = __builtin_amdgcn_perm(v[2 * k + 1][0], v[2 * k][0], 0x07060302u);
        c[2][k] = __builtin_amdgcn_perm(v[2 * k + 1][1], v[2 * k][1], 0x05040100u); c[3][k] = __builtin_amdgcn_perm(v[2 * k + 1][1], v[2 * k][1], 0x07060302u);
    }
}
template <int OFF, class T> __device__ __forceinline__ void gld8(T& d, const void* p) {
    asm volatile("global_load_dwordx2 %0, %1, off offset:%2" : "=v"(d) : "v"(p), "i"(OFF) : "memory"); }

constexpr int LDS_W = 34 * 1024, LDS_WO = 70 * 1024;
struct Pf { const char* src; unsigned dst; };
__device__ __forceinline__ Pf pf_qkv(char* ws, char* smem, int layer, int bid, int w, int lane) {
    const int ct = 8 * (bid & 1) + ((bid >> 3) & 7);
    return Pf{ws + (layer ? OFF_WTB : OFF_WTA) + (size_t)ct * WT_TILE + w * 2048 + lane * 16, lds_off(smem) + LDS_W + w * 2048};
}
__device__ __forceinline__ Pf pf_wo(char* ws, char* smem, int mode, int bid, int w, int lane) {
    const int jt = (bid >> 3) & 7;
    return Pf{ws + (mode ? OFF_WOB : OFF_WOA) + (size_t)jt * WO_TILE + w * 2048 + lane * 16, lds_off(smem) + LDS_WO + w * 2048};
}
#define PF(p, j) glds<2>((p).src + (j) * 16384, (p).dst + (j) * 16384)
#define PF_LAST(p, j, n) do { const int jj_ = w < (n) ? (j) : (j) - 1; PF(p, jj_); } while (0)
#define PFQ_LAST(p) PF_LAST(p, 7, 7)
#define PFO_LAST(p) PF_LAST(p, 5, 5)

__device__ __forceinline__ void lin_kv_part(char* ws, char* smem, int L, int panel, int ct, int w, int lane, const f32x4 (&acc)[7], const float (&rs4)[4], unsigned (&qt)[2]) {
    const int m = lane & 15, g = lane >> 4, tid = w * 64 + lane;
    {
        u32x2 fr[5];
#pragma unroll
        for (int c = 2; c < 7; ++c) {
            float v[4];
#pragma unroll
            for (int r = 0; r < 4; ++r) {
                float x = rs4[r] * acc[c][r];
                if (c < 4) x = phi_f(x);
                else if (c == 6) { const float p = phi_f(x); x = m < 8 ? p : x; x = m == 12 ? 1.f : x; }
                v[r] = x;
            }
            fr[c - 2] = (u32x2){pack2(v[0], v[1]), pack2(v[2], v[3])};
        }
        const bf16x4 k0 = __builtin_bit_cast(bf16x4, fr[0]), k1 = __builtin_bit_cast(bf16x4, fr[1]);
        const bf16x4 v0 = __builtin_bit_cast(bf16x4, fr[2]), v1 = __builtin_bit_cast(bf16x4, fr[3]), T = __builtin_bit_cast(bf16x4, fr[4]);
        const f32x4 z4 = {0.f, 0.f, 0.f, 0.f};
        const f32x4 d1 = MFMA16K16(k0, v0, z4), d2 = MFMA16K16(k1, v1, z4), d3 = MFMA16K16(T, v0, z4), d4 = MFMA16K16(T, v1, z4);
        const f32x4 d5 = MFMA16K16(k0, T, z4), d6 = MFMA16K16(k1, T, z4), d7 = MFMA16K16(T, T, z4);
        lds_ptr red = (lds_ptr)smem + (w ^ 4) * 5120;
        *(lds_f4)(red + (m * 20 + 4 * g) * 4) = d1;
        *(lds_f4)(red + (KV_HEAD + m * 20 + 4 * g) * 4) = d2;
        if (g == 1) {
            *(lds_f2)(red + (m * 20 + 16) * 4) = (f32x2){d3[0], d3[1]};
            *(lds_f2)(red + (KV_HEAD + m * 20 + 16) * 4) = (f32x2){d4[2], d4[3]};
        }
        if (m == 8 || m == 9 || m == 12) {
            const int e = m == 12 ? 18 : 8 + m;
            *(lds_f4)(red + (e * 20 + 4 * g) * 4) = d5;
            if (g == 1) *(lds_f2)(red + (e * 20 + 16) * 4) = (f32x2){d7[0], d7[1]};
        }
        if (m == 10 || m == 11 || m == 12) {
            const int e = m == 12 ? 18 : 6 + m;
            *(lds_f4)(red + (KV_HEAD + e * 20 + 4 * g) * 4) = d6;
            if (g == 1) *(lds_f2)(red + (KV_HEAD + e * 20 + 16) * 4) = (f32x2){d7[2], d7[3]};
        }
        {
            const unsigned o0 = fr[4][0], o1 = fr[4][1];
            const unsigned n0 = __builtin_amdgcn_mov_dpp(o0, 0xB1, 0xf, 0xf, true), n1 = __builtin_amdgcn_mov_dpp(o1, 0xB1, 0xf, 0xf, true);
            if (m == 0 || m == 2)
                *(lds_u4)(red + 4096 + (m >> 1) * 64 + g * 16) =
                    (u32x4){(o0 & 0xffffu) | (n0 << 16), (o0 >> 16) | (n0 & 0xffff0000u), (o1 & 0xffffu) | (n1 << 16), (o1 >> 16) | (n1 & 0xffff0000u)};
        }
        asm volatile("s_waitcnt lgkmcnt(0)\n\ts_barrier" ::: "memory");
        char* kvp = ws + OFF_KV + (size_t)(L * 16 + ct) * 16 * 3072;
        if (tid < 190) {
            f32x4 s4 = *(lds_f4)((lds_ptr)smem + tid * 16);
#pragma unroll
            for (int i = 1; i < 8; ++i) s4 += *(lds_f4)((lds_ptr)smem + i * 5120 + tid * 16);
            gst16c(kvp + panel * 3072 + tid * 16, __builtin_bit_cast(u32x4, s4));
        }
        qt[0] = *(lds_vu32)(red + 4096 + m * 4); qt[1] = *(lds_vu32)(red + 4096 + 64 + m * 4);
    }
}
__device__ __forceinline__ void lin_o_part(char* ws, char* smem, int L, int rb, int ct, int w, int lane, u32x4 (&bq)[2], const Polls& pl) {
    const int m = lane & 15, g = lane >> 4, tid = w * 64 + lane, h0 = 2 * ct;
    char* lay = ws + OFF_LAY + L * LAY_BYTES;
    char* kvp = ws + OFF_KV + (size_t)(L * 16 + ct) * 16 * 3072;
    const f32x4 z4 = {0.f, 0.f, 0.f, 0.f};
    {
        {
            f32x4 pa[3], pb[3];
            const char* pp = kvp + w * 6144 + lane * 16;
            gld16c<0>(pa[0], pp); gld16c<1024>(pa[1], pp); gld16c<2048>(pa[2], pp);
            gld16c<0>(pb[0], pp + 3072); gld16c<1024>(pb[1], pp + 3072); gld16c<2048>(pb[2], pp + 3072);
            asm volatile("s_waitcnt vmcnt(0)" : "+v"(pa[0]), "+v"(pa[1]), "+v"(pa[2]), "+v"(pb[0]), "+v"(pb[1]), "+v"(pb[2]), "+v"(bq[0]), "+v"(bq[1]) :: "memory");
            RETIRE(pl);
#pragma unroll
            for (int i = 0; i < 3; ++i) *(lds_f4)((lds_ptr)smem + w * 3072 + i * 1024 + lane * 16) = pa[i] + pb[i];
            asm volatile("s_waitcnt lgkmcnt(0)\n\ts_barrier" ::: "memory");
            if (tid < 190) {
                f32x4 s4 = *(lds_f4)((lds_ptr)smem + tid * 16);
#pragma unroll
                for (int i = 1; i < 8; ++i) s4 += *(lds_f4)((lds_ptr)smem + i * 3072 + tid * 16);
                *(lds_f4)((lds_ptr)smem + 24576 + tid * 16) = s4;
            }
            asm volatile("s_waitcnt lgkmcnt(0)\n\ts_barrier" ::: "memory");
        }
        f32x4 ka[2], kb[2]; f32x2 ka2[2], kb2[2];
        const int r3 = m & 3; const bool isz = r3 == 2;
        {
            lds_ptr p1 = (lds_ptr)smem + 24576 + m * 80, p2 = (lds_ptr)smem + 24576 + (isz ? 18 : 16 + (r3 & 1)) * 80;
#pragma unroll
            for (int h = 0; h < 2; ++h) {
                ka[h] = *(lds_f4)(p1 + h * KV_HEAD * 4 + 16 * g); ka2[h] = *(lds_f2)(p1 + h * KV_HEAD * 4 + 64);
                kb[h] = *(lds_f4)(p2 + h * KV_HEAD * 4 + 16 * g); kb2[h] = *(lds_f2)(p2 + h * KV_HEAD * 4 + 64);
            }
        }
        const bool use2 = isz || m < 2;
        unsigned om[2][2], ot[2];
#pragma unroll
        for (int h = 0; h < 2; ++h) {
            const u32x4 f1 = {pack2(ka[h][0], ka[h][1]), pack2(ka[h][2], ka[h][3]), g == 0 ? pack2(ka2[h][0], ka2[h][1]) : 0u, 0u};
            u32x4 f2 = {pack2(kb[h][0], kb[h][1]), pack2(kb[h][2], kb[h][3]), g == 0 ? pack2(kb2[h][0], kb2[h][1]) : 0u, 0u};
            if (!use2) f2 = (u32x4){0u, 0u, 0u, 0u};
            const bf16x8 b = __builtin_bit_cast(bf16x8, bq[h]);
            const f32x4 e1 = MFMA16(__builtin_bit_cast(bf16x8, f1), b, z4), e2 = MFMA16(__builtin_bit_cast(bf16x8, f2), b, z4);
            const float r = __builtin_amdgcn_rcpf(e2[2] + 1e-6f);
            om[h][0] = pack2(e1[0] * r, e1[1] * r); om[h][1] = pack2(e1[2] * r, e1[3] * r);
            ot[h] = pack2(e2[0] * r, e2[1] * r);
        }
        gst16c(lay + LO_O + ((size_t)(rb * 16 + ct) * 64 + lane) * 16, (u32x4){om[0][0], om[0][1], om[1][0], om[1][1]});
        {
            const unsigned a0 = __builtin_amdgcn_mov_dpp(ot[0], 0x00, 0xf, 0xf, true), a1 = __builtin_amdgcn_mov_dpp(ot[0], 0x55, 0xf, 0xf, true),
                           a2 = __builtin_amdgcn_mov_dpp(ot[0], 0xAA, 0xf, 0xf, true), a3 = __builtin_amdgcn_mov_dpp(ot[0], 0xFF, 0xf, 0xf, true);
            const unsigned b0 = __builtin_amdgcn_mov_dpp(ot[1], 0x00, 0xf, 0xf, true), b1 = __builtin_amdgcn_mov_dpp(ot[1], 0x55, 0xf, 0xf, true),
                           b2 = __builtin_amdgcn_mov_dpp(ot[1], 0xAA, 0xf, 0xf, true), b3 = __builtin_amdgcn_mov_dpp(ot[1], 0xFF, 0xf, 0xf, true);
            const bool h1 = r3 == 1;
            if (g == 0 && r3 < 2)
                gst16c(lay + LO_O + O_TAIL + ((size_t)(rb * 32 + h0 + r3) * 16 + (m & 12)) * 4, (u32x4){h1 ? b0 : a0, h1 ? b1 : a1, h1 ? b2 : a2, h1 ? b3 : a3});
        }
    }
}

template <int LAYER, bool FUSED>
__device__ __forceinline__ void qkv_phase(char* ws, char* smem, int L, int w, int lane) {
    LAUNDER();
    int m = lane & 15, g = lane >> 4; const int rp = w & 3, kh = w >> 2;
    const int xcd = bid & 7, idx = bid >> 3;
    const int panel = 4 * (xcd >> 1) + (idx >> 3), ct = 8 * (xcd & 1) + (idx & 7);
    const int rb = panel * 8 + 2 * rp + kh, rbo = panel * 8 + 2 * rp + 1 - kh;
    const int xv = 2 * L + LAYER;
    char* lay = ws + OFF_LAY + L * LAY_BYTES;
    f32x4 st[2], cs4[4]; float cs1[5];
    {
        const float* cs = (const float*)(ws + (LAYER ? OFF_CSB : OFF_CSA)) + ct * 112;
        const float* c4 = cs + 4 * g; const float* c1 = cs + m;
        if constexpr (LAYER == 0) {
            gld16<0>(cs4[0], c4); gld16<64>(cs4[1], c4);
            gld4<128>(cs1[0], c1); gld4<192>(cs1[1], c1); gld4<256>(cs1[2], c1); gld4<320>(cs1[3], c1); gld4<384>(cs1[4], c1);
        } else {
            gld16<0>(cs4[0], c4); gld16<64>(cs4[1], c4); gld16<128>(cs4[2], c4); gld16<192>(cs4[3], c4);
            gld4<256>(cs1[0], c1); gld4<320>(cs1[1], c1); gld4<384>(cs1[2], c1);
        }
    }
    if (!FUSED || xv == 0) {
        const Pf pf = pf_qkv(ws, smem, LAYER, bid, w, lane);
        PF(pf, 0); PF(pf, 1); PF(pf, 2); PF(pf, 3); PF(pf, 4); PF(pf, 5); PF(pf, 6); PFQ_LAST(pf);
    }
    const Polls pl = wait_cnt(ws, LAYER ? L * C_LAY + C_WA + panel : C_WB + panel, xv ? 16u : 0u);
    bf16x8 a[2][9];
    const char* xa0 = ws + OFF_XF + xv * XF_BYTES + (size_t)(rb * 18 + kh) * 1024 + lane * 16;
    const char* xa1 = ws + OFF_XF + xv * XF_BYTES + (size_t)(rbo * 18 + kh) * 1024 + lane * 16;
#define LDA(j) do { gld16c<((j) & 1) * 2048>(a[0][j], xa0 + ((j) >> 1) * 4096); gld16c<((j) & 1) * 2048>(a[1][j], xa1 + ((j) >> 1) * 4096); } while (0)
    {
        const char* sp = ws + OFF_ST + xv * ST_BYTES + (size_t)(rb * 16 + m) * 128 + g * 32;
        gld16c<0>(st[0], sp); gld16c<16>(st[1], sp);
    }
    LDA(0); LDA(1); LDA(2);
    const lds_cptr bl = (lds_cptr)smem + LDS_W + kh * 7 * 1024 + lane * 16;
    bf16x8 bb[2][7];
#pragma unroll
    for (int c = 0; c < 7; ++c) bb[0][c] = *(lds_f8)(bl + c * 1024);
    constexpr int NTR = LAYER == 0 ? 2 : 4;
    f32x4 acc[2][7];
#pragma unroll
    for (int c = 0; c < 7; ++c) { acc[0][c] = (f32x4){0.f, 0.f, 0.f, 0.f}; acc[1][c] = (f32x4){0.f, 0.f, 0.f, 0.f}; }
    float nmu = 0.f, rs = 0.f, nmu4[4] = {0.f, 0.f, 0.f, 0.f}, rs4[4] = {0.f, 0.f, 0.f, 0.f};
    auto stats = [&]() {
        if constexpr (LAYER == 0) asm volatile("" : "+v"(st[0]), "+v"(st[1]), "+v"(cs4[0]), "+v"(cs4[1]), "+v"(cs1[0]), "+v"(cs1[1]), "+v"(cs1[2]), "+v"(cs1[3]), "+v"(cs1[4]));
        else asm volatile("" : "+v"(st[0]), "+v"(st[1]), "+v"(cs4[0]), "+v"(cs4[1]), "+v"(cs4[2]), "+v"(cs4[3]), "+v"(cs1[0]), "+v"(cs1[1]), "+v"(cs1[2]));
        float s = rowsum4(st[0][0] + st[0][2] + st[1][0] + st[1][2]);
        const float mean = s * (1.f / 576.f);
        const float d0 = st[0][0] * (1.f / 40.f) - mean, d1 = st[0][2] * (1.f / 32.f) - mean;
        const float d2 = st[1][0] * (1.f / 40.f) - mean, d3 = st[1][2] * (1.f / 32.f) - mean;
        const float q = rowsum4(st[0][1] + st[0][3] + st[1][1] + st[1][3] + 40.f * (d0 * d0 + d2 * d2) + 32.f * (d1 * d1 + d3 * d3));
        nmu = -mean; rs = __builtin_amdgcn_rsqf(q * (1.f / 576.f) + 1e-5f);
#pragma unroll
        for (int r = 0; r < 4; ++r) { nmu4[r] = __shfl(nmu, 4 * g + r); rs4[r] = __shfl(rs, 4 * g + r); }
#pragma unroll
        for (int c = 0; c < NTR; ++c) cs4[c] *= nmu;
        SBAR();
    };
#define MM(j) do { _Pragma("unroll") for (int c = 0; c < 7; ++c) { \
            if ((j) < 8 && !(LAYER == 0 && c < 2 && (j) >= 3)) bb[((j) + 1) & 1][c] = *(lds_f8)(bl + (((j) + 1) * 14 + c) * 1024); \
            SBAR(); \
            if (LAYER == 0 && c < 2 && (j) >= 4) {} else \
            if (c < NTR) { acc[1][c] = MFMA16(bb[(j) & 1][c], a[1][j], acc[1][c]); acc[0][c] = MFMA16(bb[(j) & 1][c], a[0][j], acc[0][c]); } \
            else         { acc[1][c] = MFMA16(a[1][j], bb[(j) & 1][c], acc[1][c]); acc[0][c] = MFMA16(a[0][j], bb[(j) & 1][c], acc[0][c]); } \
            SBAR(); \
        } } while (0)
#define WAITA(j, N) asm volatile("s_waitcnt vmcnt(" #N ")" : "+v"(a[0][j]), "+v"(a[1][j]) :: "memory")
    SBAR();
    WAITA(0, 4); RETIRE(pl); LDA(3); LDA(4); MM(0);
    WAITA(1, 6); LDA(5); LDA(6); MM(1);
    if (kh == 0) stats();
    WAITA(2, 8); LDA(7); LDA(8); MM(2);
    WAITA(3, 10); MM(3);
    WAITA(4, 8); MM(4);
    asm volatile("s_barrier" ::: "memory");
    WAITA(5, 6); MM(5);
    if (kh == 1) stats();
    WAITA(6, 4); MM(6);
    WAITA(7, 2); MM(7);
    WAITA(8, 0); MM(8);
#undef MM
#undef WAITA
#undef LDA
    asm volatile("" : "+v"(lane)); m = lane & 15; g = lane >> 4;
    constexpr int C0 = LAYER == 0 ? 2 : 0, SLOT = (7 - C0) * 1024;
    {
        lds_ptr ex = (lds_ptr)smem + w * SLOT + lane * 16;
#pragma unroll
        for (int c = C0; c < 7; ++c) *(lds_f4)(ex + (c - C0) * 1024) = acc[1][c];
        asm volatile("s_waitcnt lgkmcnt(0)\n\ts_barrier" ::: "memory");
        lds_ptr ey = (lds_ptr)smem + (w ^ 4) * SLOT + lane * 16;
#pragma unroll
        for (int c = C0; c < 7; ++c) {
            acc[0][c] += *(lds_f4)(ey + (c - C0) * 1024);
            if (c < NTR) acc[0][c] += cs4[c];
            else { const float cv = cs1[c - NTR]; acc[0][c] += (f32x4){nmu4[0] * cv, nmu4[1] * cv, nmu4[2] * cv, nmu4[3] * cv}; }
        }
    }
    const int h0 = 2 * ct;
    if constexpr (LAYER == 0) {
        unsigned qt[2];
        lin_kv_part(ws, smem, L, panel, ct, w, lane, acc[0], rs4, qt);
        const lds_cptr bl2 = (lds_cptr)smem + LDS_W + kh * 7 * 1024 + lane * 16;
#pragma unroll
        for (int j = 4; j < 9; ++j) {
            const bf16x8 b0 = *(lds_f8)(bl2 + (j * 14) * 1024), b1 = *(lds_f8)(bl2 + (j * 14 + 1) * 1024);
            acc[1][0] = MFMA16(b0, a[1][j], acc[1][0]); acc[0][0] = MFMA16(b0, a[0][j], acc[0][0]);
            acc[1][1] = MFMA16(b1, a[1][j], acc[1][1]); acc[0][1] = MFMA16(b1, a[0][j], acc[0][1]);
            if (j == 5) signal_cnt<0>(ws, L * C_LAY + C_QA + ct);
        }
        lds_ptr e2 = (lds_ptr)smem + 40960 + w * 2048 + lane * 16;
        *(lds_f4)(e2) = acc[1][0]; *(lds_f4)(e2 + 1024) = acc[1][1];
        asm volatile("s_waitcnt lgkmcnt(0)\n\ts_barrier" ::: "memory");
        const Pf pf = pf_wo(ws, smem, 0, bid, w, lane);
        if (FUSED) { PF(pf, 0); PF(pf, 1); PF(pf, 2); }
        u32x4 bq[2];
        lds_ptr e3 = (lds_ptr)smem + 40960 + (w ^ 4) * 2048 + lane * 16;
#pragma unroll
        for (int c = 0; c < 2; ++c) {
            const f32x4 t = acc[0][c] + *(lds_f4)(e3 + c * 1024) + cs4[c];
            bq[c] = (u32x4){pack2(phi_f(rs * t[0]), phi_f(rs * t[1])), pack2(phi_f(rs * t[2]), phi_f(rs * t[3])), qt[c], 0u};
        }
        const Polls pl2 = wait_cnt(ws, L * C_LAY + C_QA + ct, 16);
        lin_o_part(ws, smem, L, rb, ct, w, lane, bq, pl2);
        signal_cnt<0>(ws, L * C_LAY + C_OA + panel);
        if (FUSED) { PF(pf, 3); PF(pf, 4); PFO_LAST(pf); }
    } else {
        const int tok = rb * 16 + m;
        char* img = lay + LO_IMG + (size_t)h0 * IMG_HEAD + (size_t)(rb >> 4) * IMG_CHUNK;
        const float rq = rs * QSCALE;
#pragma unroll
        for (int p = 0; p < 2; ++p) {
            const float sc = p == 0 ? rq : rs;
            const f32x4 A = acc[0][2 * p], B = acc[0][2 * p + 1];
            auto s0 = __builtin_amdgcn_permlane16_swap(pack2(sc * A[0], sc * A[1]), pack2(sc * B[0], sc * B[1]), false, false);
            auto s1 = __builtin_amdgcn_permlane16_swap(pack2(sc * A[2], sc * A[3]), pack2(sc * B[2], sc * B[3]), false, false);
            const u32x4 d = {s0[0], s1[0], s0[1], s1[1]};
            if (p == 0) gst16c(lay + LO_Q1 + ((size_t)(h0 + (g & 1)) * SEQ + tok) * 32 + (g >> 1) * 16, d);
            else gst16c(img + (g & 1) * IMG_HEAD + ((tok & 255) >> 5) * 1024 + (g >> 1) * 512 + (tok & 31) * 16, d);
        }
        {
            const f32x4 A = acc[0][4], B = acc[0][5];
            auto s0 = __builtin_amdgcn_permlane32_swap(pack2(rs4[0] * A[0], rs4[1] * A[1]), pack2(rs4[0] * B[0], rs4[1] * B[1]), false, false);
            auto s1 = __builtin_amdgcn_permlane32_swap(pack2(rs4[2] * A[2], rs4[3] * A[3]), pack2(rs4[2] * B[2], rs4[3] * B[3]), false, false);
            gst16c(img + (g >> 1) * IMG_HEAD + 9216 + ((rb & 15) * 2 + (g & 1)) * 320 + m * 16, (u32x4){s0[0], s1[0], s0[1], s1[1]});
        }
        {
            const float qs = m < 4 ? QSCALE : 1.f;
            const f32x4 A = acc[0][6];
            const unsigned o0 = pack2(qs * rs4[0] * A[0], qs * rs4[1] * A[1]), o1 = pack2(qs * rs4[2] * A[2], qs * rs4[3] * A[3]);
            const unsigned n0 = __builtin_amdgcn_mov_dpp(o0, 0xB1, 0xf, 0xf, true), n1 = __builtin_amdgcn_mov_dpp(o1, 0xB1, 0xf, 0xf, true);
            auto u0 = __builtin_amdgcn_permlane32_swap(o0, o0, false, false); auto u1 = __builtin_amdgcn_permlane32_swap(o1, o1, false, false);
            const int hsel = (m >> 1) & 1, tok0 = rb * 16 + 4 * g;
            u32x4 d; char* p;
            if (m < 8) {
                d = (u32x4){(o0 & 0xffffu) | (n0 << 16), (o0 >> 16) | (n0 & 0xffff0000u), (o1 & 0xffffu) | (n1 << 16), (o1 >> 16) | (n1 & 0xffff0000u)};
                p = m < 4 ? lay + LO_Q2 + ((size_t)(h0 + hsel) * SEQ + tok0) * 4 : img + hsel * IMG_HEAD + 8192 + (tok0 & 255) * 4;
            } else {
                const unsigned cst = (m & 1) ? 0u : 0x3f803f80u;
                d = m < 12 ? (u32x4){o0, o1, u0[1], u1[1]} : (u32x4){cst, cst, cst, cst};
                p = img + hsel * IMG_HEAD + 9216 + ((rb & 15) * 2 + (g & 1)) * 320 + (16 + (m & 1) + ((m >> 1) & 2)) * 16;
            }
            if (m < 8 ? !(m & 1) : g < 2) gst16c(p, d);
        }
        signal_cnt<0>(ws, L * C_LAY + C_QB + ct);
    }
}

template <bool FUSED>
__device__ __forceinline__ void wo_phase(char* ws, char* smem, const unsigned short* x0, unsigned short* out, int L, int mode, int w, int lane) {
    LAUNDER();
    const int m = lane & 15, g = lane >> 4, rb4 = w & 3, kh = w >> 2;
    const int xcd = bid & 7, idx = bid >> 3;
    const int panel = 4 * xcd + (idx >> 3), jt = idx & 7;
    const int rb = panel * 4 + rb4;
    const int xv = 2 * L + mode;
    const char* ob = ws + OFF_LAY + L * LAY_BYTES + LO_O + mode * O_BYTES;
    const bool last = xv == 3;
    const bool dopf = FUSED && !last;
    const Pf pf = pf_qkv(ws, smem, mode ? 0 : 1, bid, w, lane);
    if (!FUSED) { const Pf po = pf_wo(ws, smem, mode, bid, w, lane); PF(po, 0); PF(po, 1); PF(po, 2); PF(po, 3); PF(po, 4); PFO_LAST(po); }
    u32x2 xr[3];
#pragma unroll
    for (int c = 0; c < 3; ++c) {
        const int cb = c == 2 ? 4 : 2 * kh + c, gg = c == 2 ? (g & 1) : g;
        const int n0 = 72 * jt + 16 * cb + 4 * gg;
        const char* xp = xv == 0 ? (const char*)(x0 + (size_t)(rb * 16 + m) * 576 + n0)
                                 : ws + OFF_XF + xv * XF_BYTES + ((size_t)(rb * 18 + (n0 >> 5)) * 64 + ((n0 >> 3) & 3) * 16 + m) * 16 + ((n0 >> 2) & 1) * 8;
        gld8c<0>(xr[c], xp);
    }
    if (dopf) { PF(pf, 0); PF(pf, 1); }
    const Polls pl = wait_cnt(ws, L * C_LAY + (mode ? C_AT + (panel >> 2) : C_OA + (panel >> 1)), mode ? 32u : 16u);
    asm volatile("" : "+v"(xr[0]), "+v"(xr[1]), "+v"(xr[2]));
    bf16x8 af[9]; unsigned pt[4];
    {
        const char* op = ob + ((size_t)(rb * 16 + 8 * kh) * 64 + lane) * 16;
        gld16c<0>(af[0], op); gld16c<1024>(af[1], op); gld16c<2048>(af[2], op); gld16c<3072>(af[3], op);
        gld16c<0>(af[4], op + 4096); gld16c<1024>(af[5], op + 4096); gld16c<2048>(af[6], op + 4096); gld16c<3072>(af[7], op + 4096);
        const char* tp = ob + O_TAIL + ((size_t)(rb * 32 + 16 * kh + 4 * g) * 16 + m) * 4;
        gld4c<0>(pt[0], tp); gld4c<64>(pt[1], tp); gld4c<128>(pt[2], tp); gld4c<192>(pt[3], tp);
    }
    f32x4 acc[5];
#pragma unroll
    for (int c = 0; c < 5; ++c) acc[c] = (f32x4){0.f, 0.f, 0.f, 0.f};
    const lds_cptr bl = (lds_cptr)smem + LDS_WO + kh * 5 * 1024 + lane * 16;
    bf16x8 bb[3][5];
#pragma unroll
    for (int c = 0; c < 5; ++c) { bb[0][c] = *(lds_f8)(bl + c * 1024); bb[1][c] = *(lds_f8)(bl + (10 + c) * 1024); }
#define KSTEP(i_) do { _Pragma("unroll") for (int c = 0; c < 5; ++c) { \
        if ((i_) < 7) bb[((i_) + 2) % 3][c] = *(lds_f8)(bl + (((i_) + 2) * 10 + c) * 1024); \
        SBAR(); acc[c] = MFMA16(bb[(i_) % 3][c], af[i_], acc[c]); SBAR(); } } while (0)
#define WAITF(i_, N) asm volatile("s_waitcnt vmcnt(" #N ")" : "+v"(af[i_]) :: "memory")
    SBAR();
    WAITF(0, 11); RETIRE(pl); KSTEP(0); WAITF(1, 10); KSTEP(1); WAITF(2, 9); KSTEP(2); WAITF(3, 8); KSTEP(3);
    WAITF(4, 7); KSTEP(4); WAITF(5, 6); KSTEP(5); WAITF(6, 5); KSTEP(6); WAITF(7, 4); KSTEP(7);
    asm volatile("s_waitcnt vmcnt(0)" : "+v"(pt[0]), "+v"(pt[1]), "+v"(pt[2]), "+v"(pt[3]) :: "memory");
    af[8] = __builtin_bit_cast(bf16x8, (u32x4){pt[0], pt[1], pt[2], pt[3]});
    KSTEP(8);
#undef KSTEP
#undef WAITF
    lds_ptr ex = (lds_ptr)smem + rb4 * 5 * 1024 + lane * 16;
    if (kh == 0) { *(lds_f4)(ex + 2 * 1024) = acc[2]; *(lds_f4)(ex + 3 * 1024) = acc[3]; }
    else { *(lds_f4)(ex) = acc[0]; *(lds_f4)(ex + 1024) = acc[1]; *(lds_f4)(ex + 4 * 1024) = acc[4]; }
    asm volatile("s_waitcnt lgkmcnt(0)\n\ts_barrier" ::: "memory");
    unsigned pk[3][2];
    {
        const f32x4 o0 = (kh ? acc[2] : acc[0]) + *(lds_f4)(ex + (2 * kh) * 1024), o1 = (kh ? acc[3] : acc[1]) + *(lds_f4)(ex + (2 * kh + 1) * 1024);
        const f32x4 o2 = acc[4] + *(lds_f4)(ex + 4 * 1024);
        pk[0][0] = pack2(o0[0] + bf_lo(xr[0][0]), o0[1] + bf_hi(xr[0][0])); pk[0][1] = pack2(o0[2] + bf_lo(xr[0][1]), o0[3] + bf_hi(xr[0][1]));
        pk[1][0] = pack2(o1[0] + bf_lo(xr[1][0]), o1[1] + bf_hi(xr[1][0])); pk[1][1] = pack2(o1[2] + bf_lo(xr[1][1]), o1[3] + bf_hi(xr[1][1]));
        pk[2][0] = pack2(o2[0] + bf_lo(xr[2][0]), o2[1] + bf_hi(xr[2][0])); pk[2][1] = pack2(o2[2] + bf_lo(xr[2][1]), o2[3] + bf_hi(xr[2][1]));
    }
    {
        auto s0 = __builtin_amdgcn_permlane16_swap(pk[0][0], pk[1][0], false, false), s1 = __builtin_amdgcn_permlane16_swap(pk[0][1], pk[1][1], false, false);
        auto t0 = __builtin_amdgcn_permlane16_swap(pk[2][0], pk[2][0], false, false), t1 = __builtin_amdgcn_permlane16_swap(pk[2][1], pk[2][1], false, false);
        const u32x4 d = {s0[0], s1[0], s0[1], s1[1]}, e = {t0[0], t1[0], t0[1], t1[1]};
        const int n0 = 72 * jt + 32 * kh + 16 * (g & 1) + 8 * (g >> 1), n1 = 72 * jt + 64;
        if (last) {
            typedef __attribute__((address_space(1))) u32x4* g_u4;
            unsigned short* orow = out + (size_t)(rb * 16 + m) * 576;
            *(g_u4)(orow + n0) = d;
            if (kh == 0 && g == 0) *(g_u4)(orow + n1) = e;
        } else {
            char* xo = ws + OFF_XF + (xv + 1) * XF_BYTES + (size_t)rb * 18 * 1024 + m * 16;
            gst16c(xo + (n0 >> 5) * 1024 + ((n0 >> 3) & 3) * 256, d);
            if (kh == 0 && g == 0) gst16c(xo + (n1 >> 5) * 1024 + ((n1 >> 3) & 3) * 256, e);
        }
    }
    if (!last) {
        const bool v2 = kh == 0 && g < 2;
        float v[12];
#pragma unroll
        for (int c = 0; c < 3; ++c) { v[4 * c] = bf_lo(pk[c][0]); v[4 * c + 1] = bf_hi(pk[c][0]); v[4 * c + 2] = bf_lo(pk[c][1]); v[4 * c + 3] = bf_hi(pk[c][1]); }
        float s = ((v[0] + v[1]) + (v[2] + v[3])) + ((v[4] + v[5]) + (v[6] + v[7])) + (v2 ? (v[8] + v[9]) + (v[10] + v[11]) : 0.f);
        s = rowsum4(s);
        const float mean = s * (kh ? (1.f / 32.f) : (1.f / 40.f));
        float q = 0.f, q2 = 0.f;
#pragma unroll
        for (int r = 0; r < 8; ++r) { const float dd = v[r] - mean; q += dd * dd; }
#pragma unroll
        for (int r = 8; r < 12; ++r) { const float dd = v[r] - mean; q2 += dd * dd; }
        q += v2 ? q2 : 0.f;
        q = rowsum4(q);
        if (g == 0) gst8c(ws + OFF_ST + (xv + 1) * ST_BYTES + (size_t)(rb * 16 + m) * 128 + jt * 16 + kh * 8, (f32x2){s, q});
        const int line = L * C_LAY + (mode ? C_WB : C_WA) + (panel >> 1);
        signal_cnt<0>(ws, line);
        if (FUSED) { PF(pf, 2); PF(pf, 3); PF(pf, 4); PF(pf, 5); PF(pf, 6); PFQ_LAST(pf); }
    }
}

__device__ __forceinline__ float half_max(float v) {
    auto r = __builtin_amdgcn_permlane32_swap(__float_as_uint(v), __float_as_uint(v), false, false);
    return fmaxf(__uint_as_float(r[0]), __uint_as_float(r[1]));
}
#define STR_(x) #x
#define STR(x) STR_(x)
#define AEXP(r) "v_exp_f32 v" STR(r) ", v" STR(r) "\n\t"
#define ACVT(d, r0, r1) "v_cvt_pk_bf16_f32 v" STR(d) ", v" STR(r0) ", v" STR(r1) "\n\t"
#define AE4(r0, r1, r2, r3, d0, d1) AEXP(r0) AEXP(r1) AEXP(r2) AEXP(r3) ACVT(d0, r0, r1) ACVT(d1, r2, r3)
#define ATILE(KO, K2O, V1O, V0O, XK, X0, X1, XV) \
    "v_mfma_f32_32x32x16_bf16 v[48:63], v[80:83], v[72:75], 0\n\t" XK \
    "ds_read_b128 v[80:83], v104 offset:" STR(KO) "\n\t" \
    AE4(32, 33, 34, 35, 96, 97) \
    "v_mfma_f32_32x32x16_bf16 v[16:31], v[92:95], v[100:103], v[16:31]\n\t" \
    "ds_read_b128 v[92:95], v106 offset:" STR(V1O) "\n\t" X0 \
    AE4(36, 37, 38, 39, 98, 99) \
    "v_mfma_f32_32x32x16_bf16 v[48:63], v[84:87], v[76:79], v[48:63]\n\t" \
    "ds_read_b32 v84, v105 offset:" STR(K2O) "\n\t" \
    AE4(40, 41, 42, 43, 100, 101) \
    "s_waitcnt lgkmcnt(3)\n\t" \
    "v_mfma_f32_32x32x16_bf16 v[0:15], v[88:91], v[96:99], v[0:15]\n\t" \
    AE4(44, 45, 46, 47, 102, 103) \
    "s_waitcnt lgkmcnt(1)\n\t" \
    "v_mfma_f32_32x32x16_bf16 v[32:47], v[80:83], v[64:67], 0\n\t" \
    AE4(48, 49, 50, 51, 96, 97) \
    "v_mfma_f32_32x32x16_bf16 v[0:15], v[92:95], v[100:103], v[0:15]\n\t" X1 \
    AE4(52, 53, 54, 55, 98, 99) \
    "s_waitcnt lgkmcnt(0)\n\t" \
    "v_mfma_f32_32x32x16_bf16 v[32:47], v[84:87], v[68:71], v[32:47]\n\t" \
    AE4(56, 57, 58, 59, 100, 101) \
    "v_mfma_f32_32x32x16_bf16 v[16:31], v[88:91], v[96:99], v[16:31]\n\t" XV \
    "ds_read_b128 v[88:91], v106 offset:" STR(V0O) "\n\t" \
    AE4(60, 61, 62, 63, 102, 103)
#define ACHUNK \
    "s_mov_b32 m0, s22\n\ts_add_u32 s28, s20, 0x2000\n\ts_addc_u32 s29, s21, 0\n\tglobal_load_lds_dwordx4 v107, s[20:21] sc1\n\t" \
    "s_add_u32 s27, s22, 0x2000\n\ts_mov_b32 m0, s27\n\ts_cmp_lt_u32 %[w], 3\n\tglobal_load_lds_dwordx4 v107, s[28:29] sc1\n\t" \
    "s_cbranch_scc0 7f\n\t" \
    "s_add_u32 s27, s22, 0x4000\n\ts_mov_b32 m0, s27\n\ts_add_u32 s28, s20, 0x4000\n\ts_addc_u32 s29, s21, 0\n\tglobal_load_lds_dwordx4 v107, s[28:29] sc1\n\t" \
    "7:\n\t" \
    "s_add_u32 s20, s20, 0x4c00\n\ts_addc_u32 s21, s21, 0\n\t"
#define AWPF \
    "s_mov_b32 m0, s25\n\ts_add_u32 s25, s25, 0x4000\n\tglobal_load_lds_dwordx4 v107, s[30:31]\n\tglobal_load_lds_dwordx4 v107, s[30:31] offset:1024\n\t" \
    "s_add_u32 s30, s30, 0x4000\n\ts_addc_u32 s31, s31, 0\n\t"
#define AKADV "v_add_u32 v104, s23, v104\n\tv_add_u32 v105, s23, v105\n\t"
#define AVADV "v_add_u32 v106, s23, v106\n\t"

template <bool FUSED>
__device__ __forceinline__ void attn_phase(char* ws, char* smem, int L, int w, int lane) {
    LAUNDER();
    const int r32 = lane & 31, hh = lane >> 5, kh = w >> 2;
    const int xcd = bid & 7, idx = bid >> 3;
    const int head = 4 * xcd + (idx >> 3), qb = idx & 7;
    const int qA = qb * 256 + (w & 3) * 64 + r32;
    char* lay = ws + OFF_LAY + L * LAY_BYTES;
    const unsigned lds0 = lds_off(smem) + w * 1024;
    const Polls pl = wait_cnt(ws, L * C_LAY + C_QB + (head >> 1), 16);
    bf16x8 q1a, q1b; unsigned q2wa, q2wb;
    {
        const char* qp = lay + LO_Q1 + ((size_t)head * SEQ + qA) * 32 + hh * 16;
        const char* q2p = lay + LO_Q2 + ((size_t)head * SEQ + qA) * 4;
        gld16c<0>(q1a, qp); gld16c<1024>(q1b, qp); gld4c<0>(q2wa, q2p); gld4c<128>(q2wb, q2p);
    }
    const char* srcw = lay + LO_IMG + (size_t)head * IMG_HEAD + w * 1024;
    {
        const char* src = srcw + lane * 16;
#pragma unroll
        for (int k = 0; k < 4; ++k) glds1_sc1(src + k * 8192, lds0 + k * 8192);
        glds1_sc1(src + (w < 6 ? 4 : 3) * 8192, lds0 + (w < 6 ? 4 : 3) * 8192);
    }
    lds_cptr kp1 = (lds_cptr)smem + lane * 16;
    lds_cptr kp2 = (lds_cptr)smem + 8192 + r32 * 4;
    const int dmap = r32 < 19 ? r32 : 19;
    lds_cptr vp = (lds_cptr)smem + 9216 + hh * 320 + dmap * 16;
    asm volatile("s_waitcnt vmcnt(2)\n\ts_barrier" : "+v"(q1a), "+v"(q1b), "+v"(q2wa), "+v"(q2wb) :: "memory");
    RETIRE(pl);
#define LDK1(t) (*(lds_f8)(kp1 + (t) * 1024))
#define LDK2(t) (__builtin_bit_cast(bf16x8, (u32x4){*(lds_vu32)(kp2 + (t) * 128), 0x3f80u, 0u, 0u}))
#define LDV(u) (*(lds_f8)(vp + (u) * 640))
    const f32x16 z16 = {};
    f32x16 s0, s1, oa = {}, ob = {};
    bf16x8 k1 = LDK1(0), k2 = LDK2(0), v0, v1, q2a, q2b;
    u32x4 p0, p1 = {0u, 0u, 0u, 0u};
    {
        q2a = __builtin_bit_cast(bf16x8, (u32x4){hh ? 0u : q2wa, 0u, 0u, 0u});
        q2b = __builtin_bit_cast(bf16x8, (u32x4){hh ? 0u : q2wb, 0u, 0u, 0u});
        f32x16 sa = MFMA32(k1, q1a, z16), sb = MFMA32(k1, q1b, z16); sa = MFMA32(k2, q2a, sa); sb = MFMA32(k2, q2b, sb);
        kp1 += kh * 4096; kp2 += kh * 512; vp += kh * 5120;
        k1 = LDK1(0); k2 = LDK2(0); v0 = LDV(0); v1 = v0;
        float ma = fmaxf(sa[0], sa[1]), mb = fmaxf(sb[0], sb[1]);
#pragma unroll
        for (int i = 2; i < 16; ++i) { ma = fmaxf(ma, sa[i]); mb = fmaxf(mb, sb[i]); }
        ma = half_max(ma); mb = half_max(mb);
        q2a = __builtin_bit_cast(bf16x8, (u32x4){hh ? 0u : q2wa, hh ? 0u : (pack2(-ma, 0.f) & 0xffffu), 0u, 0u});
        q2b = __builtin_bit_cast(bf16x8, (u32x4){hh ? 0u : q2wb, hh ? 0u : (pack2(-mb, 0.f) & 0xffffu), 0u, 0u});
    }
    s0 = MFMA32(k1, q1a, z16); s0 = MFMA32(k2, q2a, s0);
    {
        unsigned a1 = (unsigned)(size_t)kp1, a2 = (unsigned)(size_t)kp2, a3 = (unsigned)(size_t)vp;
        const unsigned loff = lane * 16;
        const char* dsrc = srcw + 2 * IMG_CHUNK; const unsigned dbase = lds0;
        const int jt = (bid >> 3) & 7;
        const char* wsrc = ws + OFF_WOB + (size_t)jt * WO_TILE + w * 2048; const unsigned wdst = lds_off(smem) + LDS_WO + w * 2048;
        asm volatile(
            "s_mov_b32 s26, m0\n\t"
            "s_mov_b64 s[20:21], %[dsrc]\n\t"
            "s_add_u32 s22, %[dbase], 0x9800\n\t"
            "s_mov_b64 s[30:31], %[wsrc]\n\t"
            "s_mov_b32 s25, %[wdst]\n\t"
            "s_mov_b32 s24, 0\n\t"
            "s_mov_b32 s33, 0\n\t"
            "s_mov_b32 s34, 0x4c00\n\t"
            "s_waitcnt lgkmcnt(0)\n\t"
            "s_nop 7\n\t"
            "s_nop 3\n\t"
            "2:\n\t"
            "s_setprio 2\n\t"
            ATILE(1024, 128, 640, 1280, "", "", "", "")
            "s_setprio 1\n\t"
            "s_cmp_eq_u32 s33, 2\n\t"
            "s_cselect_b32 s23, 0xffff6800, s34\n\t"
            "s_sub_u32 s35, s24, 1\n\t"
            "s_cmp_lt_u32 s35, %[npf]\n\t"
            "s_cbranch_scc0 3f\n\t"
            AWPF
            "3:\n\t"
            ATILE(2048, 256, 1920, 2560, "", "", "", "")
            "s_setprio 0\n\t"
            ATILE(3072, 384, 3200, 3840, "", "", "", "")
            "s_waitcnt vmcnt(2)\n\t"
            "s_cmp_lt_u32 s35, %[npf]\n\t"
            "s_cbranch_scc1 4f\n\t"
            "s_waitcnt vmcnt(0)\n\t"
            "4:\n\t"
            "s_barrier\n\t"
            "s_cmp_lt_u32 s24, 6\n\t"
            "s_cbranch_scc0 5f\n\t"
            ACHUNK
            "s_cmp_eq_u32 s33, 0\n\t"
            "s_cselect_b32 s27, 0xffff6800, s34\n\t"
            "s_add_u32 s22, s22, s27\n\t"
            "5:\n\t"
            "s_setprio 3\n\t"
            ATILE(0, 0, 4480, 0, AKADV, "", "", AVADV)
            "s_add_u32 s24, s24, 1\n\t"
            "s_add_u32 s33, s33, 1\n\t"
            "s_cmp_eq_u32 s33, 3\n\t"
            "s_cselect_b32 s33, 0, s33\n\t"
            "s_cmp_lt_u32 s24, 8\n\t"
            "s_cbranch_scc1 2b\n\t"
            "s_waitcnt lgkmcnt(0)\n\t"
            "v_mfma_f32_32x32x16_bf16 v[16:31], v[92:95], v[100:103], v[16:31]\n\t"
            "s_mov_b32 m0, s26\n\t"
            "s_setprio 0\n\t"
            "s_nop 7\n\t"
            "s_nop 3\n\t"
            : "+{v[0:15]}"(oa), "+{v[16:31]}"(ob), "+{v[32:47]}"(s0), "={v[48:63]}"(s1), "+{v[80:83]}"(k1), "+{v[84:87]}"(k2), "+{v[88:91]}"(v0), "+{v[92:95]}"(v1),
              "={v[96:99]}"(p0), "+{v[100:103]}"(p1), "+{v104}"(a1), "+{v105}"(a2), "+{v106}"(a3)
            : "{v[64:67]}"(q1a), "{v[68:71]}"(q2a), "{v[72:75]}"(q1b), "{v[76:79]}"(q2b), "{v107}"(loff), [dsrc] "s"(dsrc), [dbase] "s"(dbase),
              [wsrc] "s"(wsrc), [wdst] "s"(wdst), [w] "s"(w), [npf] "s"(FUSED ? (w < 5 ? 6 : 5) : 0)
            : "s20", "s21", "s22", "s23", "s24", "s25", "s26", "s27", "s28", "s29", "s30", "s31", "s33", "s34", "s35", "scc", "memory");
    }
    lds_ptr ex = (lds_ptr)smem + (w < 6 ? w * 3072 : 2 * (int)IMG_CHUNK + 12288 + (w - 6) * 3072) + lane * 16;
    const int wo_ = w ^ 4;
    lds_ptr ey = (lds_ptr)smem + (wo_ < 6 ? wo_ * 3072 : 2 * (int)IMG_CHUNK + 12288 + (wo_ - 6) * 3072) + lane * 16;
    f32x16 o;
    {
        const f32x16 snd = kh ? oa : ob; o = kh ? ob : oa;
        *(lds_f4)(ex) = (f32x4){snd[0], snd[1], snd[2], snd[3]}; *(lds_f4)(ex + 1024) = (f32x4){snd[4], snd[5], snd[6], snd[7]};
        *(lds_f4)(ex + 2048) = (f32x4){snd[8], snd[9], snd[10], snd[11]};
    }
    asm volatile("s_waitcnt lgkmcnt(0)\n\ts_barrier" ::: "memory");
    {
        const f32x4 a = *(lds_f4)(ey), b = *(lds_f4)(ey + 1024), c = *(lds_f4)(ey + 2048);
#pragma unroll
        for (int i = 0; i < 4; ++i) { o[i] += a[i]; o[4 + i] += b[i]; o[8 + i] += c[i]; }
    }
    const int q = qA + 32 * kh;
    bool bad = !(fabsf(o[10]) < 1.0e30f);
#pragma unroll
    for (int i = 0; i < 10; ++i) bad = bad || !(fabsf(o[i]) < 1.0e38f);
    if (__any(bad)) {
        o = (f32x16){};
        float mrun = -INFINITY;
        const bf16x8 q1 = kh ? q1b : q1a;
        const bf16x8 q2 = __builtin_bit_cast(bf16x8, (u32x4){hh ? 0u : (kh ? q2wb : q2wa), 0u, 0u, 0u});
        const char* img = lay + LO_IMG + (size_t)head * IMG_HEAD;
#pragma unroll 1
        for (int t = 0; t < 64; ++t) {
            const char* cb = img + (t >> 3) * IMG_CHUNK; const int tt = t & 7;
            const char* vb = cb + 9216 + tt * 1280 + hh * 320 + dmap * 16;
            bf16x8 fk, fv0, fv1; unsigned fk2;
            gld16c<0>(fk, cb + tt * 1024 + lane * 16); gld4c<0>(fk2, cb + 8192 + tt * 128 + r32 * 4); gld16c<0>(fv0, vb); gld16c<640>(fv1, vb);
            asm volatile("s_waitcnt vmcnt(0)" : "+v"(fk), "+v"(fk2), "+v"(fv0), "+v"(fv1) :: "memory");
            f32x16 s = MFMA32(fk, q1, z16);
            s = MFMA32(__builtin_bit_cast(bf16x8, (u32x4){fk2, 0u, 0u, 0u}), q2, s);
            float mx = fmaxf(s[0], s[1]);
#pragma unroll
            for (int i = 2; i < 16; ++i) mx = fmaxf(mx, s[i]);
            mx = fmaxf(half_max(mx), mrun);
            const float al = EXP2(mrun - mx);
            mrun = mx;
#pragma unroll
            for (int i = 0; i < 16; ++i) o[i] *= al;
#pragma unroll
            for (int i = 0; i < 4; ++i) {
                p0[i] = pack2(EXP2(s[2 * i] - mx), EXP2(s[2 * i + 1] - mx));
                p1[i] = pack2(EXP2(s[8 + 2 * i] - mx), EXP2(s[8 + 2 * i + 1] - mx));
            }
            o = MFMA32(fv0, __builtin_bit_cast(bf16x8, p0), o);
            o = MFMA32(fv1, __builtin_bit_cast(bf16x8, p1), o);
        }
    }
    {
        auto r = __builtin_amdgcn_permlane32_swap(__float_as_uint(o[10]), __float_as_uint(o[10]), false, false);
        const float rl = __builtin_amdgcn_rcpf(__uint_as_float(r[0]));
        const int rb = q >> 4, m = q & 15;
        gst16c(lay + LO_O + O_BYTES + ((size_t)(rb * 16 + (head >> 1)) * 64 + (2 * (head & 1) + hh) * 16 + m) * 16,
               (u32x4){pack2(o[0] * rl, o[1] * rl), pack2(o[2] * rl, o[3] * rl), pack2(o[4] * rl, o[5] * rl), pack2(o[6] * rl, o[7] * rl)});
        const unsigned t0 = pack2(o[8] * rl, o[9] * rl);
        const unsigned t1 = __builtin_amdgcn_mov_dpp(t0, 0x55, 0xf, 0xf, true), t2 = __builtin_amdgcn_mov_dpp(t0, 0xAA, 0xf, 0xf, true),
                       t3 = __builtin_amdgcn_mov_dpp(t0, 0xFF, 0xf, 0xf, true);
        if (hh == 0 && (r32 & 3) == 0) gst16c(lay + LO_O + O_BYTES + O_TAIL + ((size_t)(rb * 32 + head) * 16 + m) * 4, (u32x4){t0, t1, t2, t3});
    }
    signal_cnt<0>(ws, L * C_LAY + C_AT + qb);
#undef LDK1
#undef LDK2
#undef LDV
}

__global__ __launch_bounds__(512) void prep_kernel(const unsigned short* __restrict__ x0, const unsigned short* __restrict__ wA,
                                                   const unsigned short* __restrict__ woA, const unsigned short* __restrict__ wB,
                                                   const unsigned short* __restrict__ woB, char* ws) {
    __shared__ __attribute__((aligned(16))) char tbuf[7 * 4096];
    __shared__ __attribute__((aligned(16))) float red[72 * 16];
    const int tid = threadIdx.x, bid = blockIdx.x, w = __builtin_amdgcn_readfirstlane(tid >> 6), l = tid & 63;
    const int lin = (bid & 7) * 27 + (bid >> 3);
    const bool jobW = (bid >> 3) < 27;
    const int cmat = lin >= 108, cblk = lin - 108 * cmat;
    u32x2 sv[8]; u32x4 xv[9];
    if (w < 5) {
        if (jobW) {
            const int T = min(tid, 287);
            const char* p = (const char*)((cmat ? wB : wA) + (size_t)(8 * (T >> 2)) * 1728 + 16 * cblk + 4 * (T & 3));
            gld8<0>(sv[0], p); gld8<3456>(sv[1], p); p += 6912; gld8<0>(sv[2], p); gld8<3456>(sv[3], p); p += 6912;
            gld8<0>(sv[4], p); gld8<3456>(sv[5], p); p += 6912; gld8<0>(sv[6], p); gld8<3456>(sv[7], p);
        }
    } else if (w < 7) {
        const int T = bid * 90 + min(tid - 320, 89), jmat = T / 11520, r = T - jmat * 11520, jsg = r / 160, jq = r - jsg * 160;
        const int jt = jq / 20, lc = min(4 * (jq - jt * 20), 68), ss = jsg >> 2, gg = jsg & 3;
        int r0, rb_, rc;
        if (ss >= 16) { r0 = (16 * (ss - 16) + 4 * gg) * 18 + 16; rb_ = 18; rc = 36; }
        else if (jmat == 0) { r0 = 36 * ss + 4 * gg; rb_ = 2; rc = 18; }
        else { r0 = (2 * ss + (gg >> 1)) * 18 + 4 * (gg & 1); rb_ = 2; rc = 8; }
        const char* p = (const char*)((jmat ? woB : woA) + (size_t)r0 * 576 + 72 * jt + lc);
        const char* p2 = p + rb_ * 1152; const char* p4 = p + rc * 1152; const char* p6 = p4 + rb_ * 1152;
        gld8<0>(sv[0], p); gld8<1152>(sv[1], p); gld8<0>(sv[2], p2); gld8<1152>(sv[3], p2);
        gld8<0>(sv[4], p4); gld8<1152>(sv[5], p4); gld8<0>(sv[6], p6); gld8<1152>(sv[7], p6);
    } else {
        const int T = bid * 64 + l, m = T & 15, j = (T >> 4) & 7, rb = T >> 7;
        const char* p = (const char*)(x0 + (size_t)(rb * 16 + m) * 576 + 72 * j);
        gld16<0>(xv[0], p); gld16<16>(xv[1], p); gld16<32>(xv[2], p); gld16<48>(xv[3], p); gld16<64>(xv[4], p);
        gld16<80>(xv[5], p); gld16<96>(xv[6], p); gld16<112>(xv[7], p); gld16<128>(xv[8], p);
    }
    if (w == 4 && (bid >> 3) >= 27 && (bid >> 3) < 31) {
        unsigned long long pc;
        asm volatile("s_getpc_b64 s[40:41]\n\ts_add_u32 s40, s40, _Z11mega_kernelILi7EEvPcPKtPtii@rel32@lo+4\n\ts_addc_u32 s41, s41, _Z11mega_kernelILi7EEvPcPKtPtii@rel32@hi+12\n\t"
                     "s_mov_b64 %0, s[40:41]" : "=s"(pc) :: "s40", "s41", "scc");
        const char* cp = (const char*)pc + ((bid >> 3) - 27) * 5120 + min(l, 39) * 128;
        unsigned t; asm volatile("global_load_dword %0, %1, off\n\ts_waitcnt vmcnt(0)" : "=v"(t) : "v"(cp) : "memory");
    }
    int tct, tcb, tc; qkv_col_map(16 * cblk + (l & 15), tct, tcb, tc);
    asm volatile("s_waitcnt vmcnt(0)" : "+v"(sv[0]), "+v"(sv[1]), "+v"(sv[2]), "+v"(sv[3]), "+v"(sv[4]), "+v"(sv[5]), "+v"(sv[6]), "+v"(sv[7]) :: "memory");
    asm volatile("" : "+v"(xv[0]), "+v"(xv[1]), "+v"(xv[2]), "+v"(xv[3]), "+v"(xv[4]), "+v"(xv[5]), "+v"(xv[6]), "+v"(xv[7]), "+v"(xv[8]));
    u32x4 cc[4];
    if (w < 7) {
        transpose8x4(sv, cc);
        lds_ptr tb = (lds_ptr)tbuf + w * 4096;
#pragma unroll
        for (int i = 0; i < 4; ++i) *(lds_u4)(tb + (l * 4 + i) * 16) = cc[i];
#pragma unroll
        for (int i = 0; i < 4; ++i) cc[i] = *(lds_u4)(tb + (i * 64 + l) * 16);
        if (w < 5) {
#pragma unroll
            for (int i = 0; i < 4; ++i) {
                const int sg = 16 * w + 4 * i + (l >> 4);
                if (sg < 72) red[sg * 16 + (l & 15)] = dot_ones(cc[i][3], dot_ones(cc[i][2], dot_ones(cc[i][1], dot_ones(cc[i][0], 0.f))));
            }
        }
    }
    if (w < 5) {
        if (jobW) {
            char* dst = ws + (cmat ? OFF_WTB : OFF_WTA) + ((size_t)(tct * 18 * 7 + tcb) * 64 + (l >> 4) * 16 + tc) * 16;
#pragma unroll
            for (int i = 0; i < 4; ++i)
                if (4 * w + i < 18) gst16c(dst + (4 * w + i) * 7168, cc[i]);
        }
    } else if (w < 7) {
#pragma unroll
        for (int i = 0; i < 4; ++i) {
            const int loc = (w - 5) * 256 + i * 64 + l;
            const int X = bid * 360 + loc, mat = X / 46080, r = X - mat * 46080, sg = r / 640, n = r - sg * 640, jt = n / 80, lc = n - jt * 80;
            const int ss = sg >> 2, okh = ss < 16 ? (ss >> 3) : (ss - 16), oi = ss < 16 ? (ss & 7) : 8;
            if (loc < 360) gst16c(ws + (mat ? OFF_WOB : OFF_WOA) + (size_t)jt * WO_TILE + (size_t)((oi * 2 + okh) * 5 + (lc >> 4)) * 1024 + ((sg & 3) * 16 + (lc & 15)) * 16,
                                  lc < 72 ? cc[i] : (u32x4){0u, 0u, 0u, 0u});
        }
    } else {
        const int T = bid * 64 + l, m = T & 15, j = (T >> 4) & 7, rb = T >> 7;
        float sa = 0.f, sb = 0.f;
#pragma unroll
        for (int i = 0; i < 9; ++i) {
            const int q = 9 * j + i;
            gst16c(ws + OFF_XF + ((size_t)(rb * 18 + (q >> 2)) * 64 + (q & 3) * 16 + m) * 16, xv[i]);
            const float a = dot_ones(xv[i][3], dot_ones(xv[i][2], dot_ones(xv[i][1], dot_ones(xv[i][0], 0.f))));
            if (i < 4 || i == 8) sa += a; else sb += a;
        }
        const float ma = sa * (1.f / 40.f), mb = sb * (1.f / 32.f);
        float qa = 0.f, qb = 0.f;
#pragma unroll
        for (int i = 0; i < 9; ++i) {
            const bool pa = i < 4 || i == 8; const float mu = pa ? ma : mb; float a = 0.f;
#pragma unroll
            for (int k = 0; k < 4; ++k) { const float d0 = bf_lo(xv[i][k]) - mu, d1 = bf_hi(xv[i][k]) - mu; a += d0 * d0 + d1 * d1; }
            if (pa) qa += a; else qb += a;
        }
        gst16c(ws + OFF_ST + (size_t)(rb * 16 + m) * 128 + j * 16, __builtin_bit_cast(u32x4, (f32x4){sa, qa, sb, qb}));
    }
    if (w == 6) {
        if (l < 36) {
            const int Z = bid * 36 + l, zm = Z / 4608, z2 = Z - zm * 4608, zct = z2 / 288, r = z2 - zct * 288;
            gst16c(ws + (zm ? OFF_WTB : OFF_WTA) + ((size_t)((zct * 18 + (r >> 4)) * 7 + 6) * 64 + ((r >> 2) & 3) * 16 + 12 + (r & 3)) * 16, (u32x4){0u, 0u, 0u, 0u});
        } else if (l < 49) {
            const int Z = bid * 13 + (l - 36);
            if ((size_t)Z * 16 < ZERO_BYTES) gst16c(ws + (size_t)Z * 16, (u32x4){0u, 0u, 0u, 0u});
        } else if (l == 49 && bid < 32) gst16c(ws + ((bid & 16) ? OFF_CSB : OFF_CSA) + ((bid & 15) * 112 + 108) * 4, (u32x4){0u, 0u, 0u, 0u});
    }
    asm volatile("s_waitcnt lgkmcnt(0)\n\ts_barrier" ::: "memory");
    if (jobW && w == 0) {
        float a = 0.f;
#pragma unroll
        for (int i = 0; i < 18; ++i) a += red[((l >> 4) * 18 + i) * 16 + (l & 15)];
        a = rowsum4(a);
        if (l < 16) __hip_atomic_store((gu32*)(ws + (cmat ? OFF_CSB : OFF_CSA)) + tct * 112 + tcb * 16 + tc, __float_as_uint(a), RLX_AGENT);
    }
}

template <int MASK>
__global__ __launch_bounds__(512, 2) void mega_kernel(char* ws, const unsigned short* x0, unsigned short* out, int h_lo, int h_hi) {
    __shared__ __attribute__((aligned(16))) char smem[160 * 1024];
    const int lane = threadIdx.x & 63, w = __builtin_amdgcn_readfirstlane(threadIdx.x >> 6);
    constexpr bool FUSED = MASK == 7;
#pragma unroll 1
    for (int hl = h_lo; hl < h_hi; ++hl) {
        const int L = hl >> 1;
        if (hl & 1) {
            if (MASK & 1) qkv_phase<1, FUSED>(ws, smem, L, w, lane);
            if (MASK & 2) attn_phase<FUSED>(ws, smem, L, w, lane);
        } else {
            if (MASK & 1) qkv_phase<0, FUSED>(ws, smem, L, w, lane);
        }
        if (MASK & 4) wo_phase<FUSED>(ws, smem, x0, out, L, hl & 1, w, lane);
    }
}

extern "C" void kernel_launch(void* const* d_in, const int* in_sizes, int n_in, void* d_out, int out_size, void* d_ws, size_t ws_size,
                              hipStream_t stream) {
    const unsigned short* x0 = (const unsigned short*)d_in[0];
    const unsigned short* wA = (const unsigned short*)d_in[1];
    const unsigned short* woA = (const unsigned short*)d_in[2];
    const unsigned short* wB = (const unsigned short*)d_in[4];
    const unsigned short* woB = (const unsigned short*)d_in[5];
    char* ws = (char*)d_ws;
    unsigned short* out = (unsigned short*)d_out;
    prep_kernel<<<256, 512, 0, stream>>>(x0, wA, woA, wB, woB, ws);
    mega_kernel<7><<<256, 512, 0, stream>>>(ws, x0, out, 0, 4);
}
```

```cpp
#include <hip/hip_runtime.h>
#include <hip/hip_bf16.h>
#include <stdint.h>

typedef __attribute__((ext_vector_type(8))) short bf16x8;
typedef __attribute__((ext_vector_type(4))) short bf16x4;
typedef __attribute__((ext_vector_type(4))) float f32x4;
typedef __attribute__((ext_vector_type(2))) float f32x2;
typedef __attribute__((ext_vector_type(16))) float f32x16;
typedef __attribute__((ext_vector_type(4))) unsigned u32x4;
typedef __attribute__((ext_vector_type(2))) unsigned u32x2;
typedef __attribute__((ext_vector_type(2))) __bf16 bfx2;
#define LDSAS __attribute__((address_space(3)))
typedef LDSAS char* lds_ptr;
typedef const LDSAS char* lds_cptr;
typedef LDSAS f32x4* lds_f4;
typedef LDSAS f32x2* lds_f2;
typedef LDSAS float* lds_f1;
typedef LDSAS u32x4* lds_u4;
typedef const volatile LDSAS unsigned* lds_vu32;
typedef const LDSAS bf16x8* lds_f8;

constexpr int SEQ = 2048, DM = 576, NH = 32;

constexpr size_t WT_TILE = 126 * 1024;
constexpr size_t WO_TILE = 90 * 1024;
constexpr size_t IMG_CHUNK = 19 * 1024;
constexpr size_t IMG_HEAD = 8 * IMG_CHUNK;
constexpr int KV_HEAD = 19 * 20;
constexpr size_t XF_BYTES = (size_t)SEQ * DM * 2, ST_BYTES = (size_t)SEQ * 128;
constexpr size_t OFF_CNT = 0;
constexpr size_t CNT_BYTES = 2 * 96 * 256 + 256;
constexpr size_t ZERO_BYTES = 49664;
constexpr size_t OFF_KV  = 65536;
constexpr size_t OFF_WTA = OFF_KV + 2 * 16 * 16 * 3072;
constexpr size_t OFF_WTB = OFF_WTA + 16 * WT_TILE + 4096;
constexpr size_t OFF_CSA = OFF_WTB + 16 * WT_TILE + 4096;
constexpr size_t OFF_CSB = OFF_CSA + 8192;
constexpr size_t OFF_WOA = OFF_CSB + 8192;
constexpr size_t OFF_WOB = OFF_WOA + 8 * WO_TILE + 8192;
constexpr size_t OFF_XF  = OFF_WOB + 8 * WO_TILE + 8192;
constexpr size_t OFF_ST  = OFF_XF + 4 * XF_BYTES;
constexpr size_t OFF_LAY = OFF_ST + 4 * ST_BYTES;
constexpr size_t O_BYTES = (size_t)128 * 16 * 1024 + (size_t)128 * 32 * 64;
constexpr size_t O_TAIL = (size_t)128 * 16 * 1024;
constexpr size_t LO_O   = 0;
constexpr size_t LO_Q1  = LO_O + 2 * O_BYTES;
constexpr size_t LO_Q2  = LO_Q1 + (size_t)NH * SEQ * 32;
constexpr size_t LO_IMG = LO_Q2 + (size_t)NH * SEQ * 4;
constexpr size_t LAY_BYTES = LO_IMG + (size_t)NH * IMG_HEAD;
static_assert(ZERO_BYTES <= OFF_KV && CNT_BYTES <= ZERO_BYTES && ZERO_BYTES % 16 == 0, "layout");
constexpr int C_QA = 0, C_OA = 16, C_WA = 32, C_QB = 48, C_AT = 64, C_WB = 80, C_LAY = 96;

constexpr float QSCALE = 0.23570226039551584f * 1.4426950408889634f;

__device__ __forceinline__ unsigned pack2(float lo, float hi) {
    f32x2 v = {lo, hi};
    bfx2 r = __builtin_convertvector(v, bfx2);
    return __builtin_bit_cast(unsigned, r);
}
__device__ __forceinline__ float bf_lo(unsigned u) { return __uint_as_float(u << 16); }
__device__ __forceinline__ float bf_hi(unsigned u) { return __uint_as_float(u & 0xffff0000u); }
__device__ __forceinline__ float phi_f(float t) { return (t > 0.f ? t + 1.f : __expf(t)) + 1e-6f; }
__device__ __forceinline__ unsigned lds_off(const void* p) { return (unsigned)(size_t)(lds_cptr)p; }

#define MFMA16(a, b, c) __builtin_amdgcn_mfma_f32_16x16x32_bf16(a, b, c, 0, 0, 0)
#define MFMA32(a, b, c) __builtin_amdgcn_mfma_f32_32x32x16_bf16(a, b, c, 0, 0, 0)
#define MFMA16K16(a, b, c) __builtin_amdgcn_mfma_f32_16x16x16bf16_1k(a, b, c, 0, 0, 0)
__device__ __forceinline__ float rowsum4(float v) {
    auto a = __builtin_amdgcn_permlane16_swap(__float_as_uint(v), __float_as_uint(v), false, false);
    v = __uint_as_float(a[0]) + __uint_as_float(a[1]);
    auto b = __builtin_amdgcn_permlane32_swap(__float_as_uint(v), __float_as_uint(v), false, false);
    return __uint_as_float(b[0]) + __uint_as_float(b[1]);
}
#define PIN(x) asm volatile("" : "+v"(x))
#define LAUNDER() int bid = blockIdx.x; asm volatile("" : "+v"(lane)); asm volatile("" : "+s"(w), "+s"(ws), "+s"(bid), "+s"(L))
#define SBAR() __builtin_amdgcn_sched_barrier(0)
#define EXP2(x) __builtin_amdgcn_exp2f(x)

template <int N>
__device__ __forceinline__ void glds(const void* g, unsigned lds) {
    unsigned keep;
    if constexpr (N == 4)
        asm volatile("s_mov_b32 %0, m0\n\ts_mov_b32 m0, %2\n\ts_nop 0\n\t"
                     "global_load_lds_dwordx4 %1, off\n\tglobal_load_lds_dwordx4 %1, off offset:1024\n\t"
                     "global_load_lds_dwordx4 %1, off offset:2048\n\tglobal_load_lds_dwordx4 %1, off offset:3072\n\t"
                     "s_mov_b32 m0, %0" : "=&s"(keep) : "v"(g), "s"(lds) : "memory");
    else
        asm volatile("s_mov_b32 %0, m0\n\ts_mov_b32 m0, %2\n\ts_nop 0\n\t"
                     "global_load_lds_dwordx4 %1, off\n\tglobal_load_lds_dwordx4 %1, off offset:1024\n\t"
                     "s_mov_b32 m0, %0" : "=&s"(keep) : "v"(g), "s"(lds) : "memory");
}
__device__ __forceinline__ void glds1_sc1(const void* g, unsigned lds) {
    unsigned keep;
    asm volatile("s_mov_b32 %0, m0\n\ts_mov_b32 m0, %2\n\ts_nop 0\n\tglobal_load_lds_dwordx4 %1, off sc1\n\ts_mov_b32 m0, %0"
                 : "=&s"(keep) : "v"(g), "s"(lds) : "memory");
}
template <int OFF, class T> __device__ __forceinline__ void gld16(T& d, const void* p) {
    asm volatile("global_load_dwordx4 %0, %1, off offset:%2" : "=v"(d) : "v"(p), "i"(OFF) : "memory"); }
template <int OFF, class T> __device__ __forceinline__ void gld4(T& d, const void* p) {
    asm volatile("global_load_dword %0, %1, off offset:%2" : "=v"(d) : "v"(p), "i"(OFF) : "memory"); }
template <int OFF, class T> __device__ __forceinline__ void gld16c(T& d, const void* p) {
    asm volatile("global_load_dwordx4 %0, %1, off offset:%2 sc1" : "=v"(d) : "v"(p), "i"(OFF) : "memory"); }
template <int OFF, class T> __device__ __forceinline__ void gld8c(T& d, const void* p) {
    asm volatile("global_load_dwordx2 %0, %1, off offset:%2 sc1" : "=v"(d) : "v"(p), "i"(OFF) : "memory"); }
template <int OFF, class T> __device__ __forceinline__ void gld4c(T& d, const void* p) {
    asm volatile("global_load_dword %0, %1, off offset:%2 sc1" : "=v"(d) : "v"(p), "i"(OFF) : "memory"); }
__device__ __forceinline__ void gst16c(void* p, u32x4 v) { asm volatile("global_store_dwordx4 %0, %1, off sc1\n\ts_nop 1" :: "v"(p), "v"(v) : "memory"); }
__device__ __forceinline__ void gst8c(void* p, f32x2 v) { asm volatile("global_store_dwordx2 %0, %1, off sc1" :: "v"(p), "v"(v) : "memory"); }

typedef __attribute__((address_space(1))) unsigned gu32;
#define RLX_AGENT __ATOMIC_RELAXED, __HIP_MEMORY_SCOPE_AGENT
template <int N> __device__ __forceinline__ void signal_cnt(char* ws, int line) {
    asm volatile("s_waitcnt vmcnt(%0) lgkmcnt(0)\n\ts_barrier" :: "i"(N) : "memory");
    if (threadIdx.x == 0) __hip_atomic_fetch_add((gu32*)(ws + OFF_CNT + (size_t)line * 256), 1u, RLX_AGENT);
}
struct Polls { unsigned v0, v1, v2, v3; };
#define RETIRE(pl) asm volatile("" :: "v"((pl).v0), "v"((pl).v1), "v"((pl).v2), "v"((pl).v3))
__device__ __forceinline__ Polls wait_cnt(char* ws, int line, unsigned want) {
    unsigned v0, v1, v2, v3, r, n;
    const char* p = ws + OFF_CNT + (size_t)line * 256;
    const unsigned poller = __builtin_amdgcn_readfirstlane(threadIdx.x < 64 ? 1u : 0u);
    asm volatile(
        "s_cmp_eq_u32 %[poller], 0\n\t"
        "s_cbranch_scc1 3f\n\t"
        "global_load_dword %[v0], %[p], off sc1\n\ts_sleep 1\n\tglobal_load_dword %[v1], %[p], off sc1\n\ts_sleep 1\n\t"
        "global_load_dword %[v2], %[p], off sc1\n\ts_sleep 1\n\tglobal_load_dword %[v3], %[p], off sc1\n\t"
        "s_mov_b32 %[n], 0x20000\n\t"
        "1:\n\t"
        "s_waitcnt vmcnt(3)\n\tv_readfirstlane_b32 %[r], %[v0]\n\ts_nop 0\n\tglobal_load_dword %[v0], %[p], off sc1\n\ts_cmp_ge_u32 %[r], %[want]\n\ts_cbranch_scc1 4f\n\t"
        "s_waitcnt vmcnt(3)\n\tv_readfirstlane_b32 %[r], %[v1]\n\ts_nop 0\n\tglobal_load_dword %[v1], %[p], off sc1\n\ts_cmp_ge_u32 %[r], %[want]\n\ts_cbranch_scc1 4f\n\t"
        "s_waitcnt vmcnt(3)\n\tv_readfirstlane_b32 %[r], %[v2]\n\ts_nop 0\n\tglobal_load_dword %[v2], %[p], off sc1\n\ts_cmp_ge_u32 %[r], %[want]\n\ts_cbranch_scc1 4f\n\t"
        "s_waitcnt vmcnt(3)\n\tv_readfirstlane_b32 %[r], %[v3]\n\ts_nop 0\n\tglobal_load_dword %[v3], %[p], off sc1\n\ts_cmp_ge_u32 %[r], %[want]\n\ts_cbranch_scc1 4f\n\t"
        "s_sub_u32 %[n], %[n], 1\n\t"
        "s_cmp_lg_u32 %[n], 0\n\t"
        "s_cbranch_scc1 1b\n\t"
        "3:\n\t"
        "s_waitcnt vmcnt(0)\n\t"
        "4:\n\t"
        "s_waitcnt lgkmcnt(0)\n\t"
        "s_barrier"
        : [v0] "=&v"(v0), [v1] "=&v"(v1), [v2] "=&v"(v2), [v3] "=&v"(v3), [r] "=&s"(r), [n] "=&s"(n)
        : [p] "v"(p), [want] "s"(want), [poller] "s"(poller) : "scc", "memory");
    return Polls{v0, v1, v2, v3};
}

__device__ __forceinline__ void qkv_col_map(int n, int& ct, int& cb, int& c) {
    const int part = n / 576, rem = n - part * 576, head = rem / 18, d = rem - head * 18;
    ct = head >> 1; const int hb = head & 1;
    if (d < 16) { cb = part * 2 + hb; c = d; } else { cb = 6; c = part * 4 + hb * 2 + (d - 16); }
}
__device__ __forceinline__ float dot_ones(unsigned u, float acc) {
    return __builtin_amdgcn_fdot2_f32_bf16(__builtin_bit_cast(bfx2, u), __builtin_bit_cast(bfx2, 0x3f803f80u), acc, false); }
__device__ __forceinline__ float dot_self(unsigned u, float acc) {
    return __builtin_amdgcn_fdot2_f32_bf16(__builtin_bit_cast(bfx2, u), __builtin_bit_cast(bfx2, u), acc, false); }
__device__ __forceinline__ void transpose8x4(const u32x2 (&v)[8], u32x4 (&c)[4]) {
#pragma unroll
    for (int k = 0; k < 4; ++k) {
        c[0][k] = __builtin_amdgcn_perm(v[2 * k + 1][0], v[2 * k][0], 0x05040100u); c[1][k] = __builtin_amdgcn_perm(v[2 * k + 1][0], v[2 * k][0], 0x07060302u);
        c[2][k] = __builtin_amdgcn_perm(v[2 * k + 1][1], v[2 * k][1], 0x05040100u); c[3][k] = __builtin_amdgcn_perm(v[2 * k + 1][1], v[2 * k][1], 0x07060302u);
    }
}
template <int OFF, class T> __device__ __forceinline__ void gld8(T& d, const void* p) {
    asm volatile("global_load_dwordx2 %0, %1, off offset:%2" : "=v"(d) : "v"(p), "i"(OFF) : "memory"); }

constexpr int LDS_W = 34 * 1024, LDS_WO = 70 * 1024;
struct Pf { const char* src; unsigned dst; };
__device__ __forceinline__ Pf pf_qkv(char* ws, char* smem, int layer, int bid, int w, int lane) {
    const int ct = 8 * (bid & 1) + ((bid >> 3) & 7);
    return Pf{ws + (layer ? OFF_WTB : OFF_WTA) + (size_t)ct * WT_TILE + w * 2048 + lane * 16, lds_off(smem) + LDS_W + w * 2048};
}
__device__ __forceinline__ Pf pf_wo(char* ws, char* smem, int mode, int bid, int w, int lane) {
    const int jt = (bid >> 3) & 7;
    return Pf{ws + (mode ? OFF_WOB : OFF_WOA) + (size_t)jt * WO_TILE + w * 2048 + lane * 16, lds_off(smem) + LDS_WO + w * 2048};
}
#define PF(p, j) glds<2>((p).src + (j) * 16384, (p).dst + (j) * 16384)
#define PF_LAST(p, j, n) do { const int jj_ = w < (n) ? (j) : (j) - 1; PF(p, jj_); } while (0)
#define PFQ_LAST(p) PF_LAST(p, 7, 7)
#define PFO_LAST(p) PF_LAST(p, 5, 5)

__device__ __forceinline__ void lin_kv_part(char* ws, char* smem, int L, int panel, int ct, int w, int lane, const f32x4 (&acc)[7], const float (&rs4)[4], unsigned (&qt)[2]) {
    const int m = lane & 15, g = lane >> 4, tid = w * 64 + lane;
    {
        u32x2 fr[5];
#pragma unroll
        for (int c = 2; c < 7; ++c) {
            float v[4];
#pragma unroll
            for (int r = 0; r < 4; ++r) {
                float x = rs4[r] * acc[c][r];
                if (c < 4) x = phi_f(x);
                else if (c == 6) { const float p = phi_f(x); x = m < 8 ? p : x; x = m == 12 ? 1.f : x; }
                v[r] = x;
            }
            fr[c - 2] = (u32x2){pack2(v[0], v[1]), pack2(v[2], v[3])};
        }
        const bf16x4 k0 = __builtin_bit_cast(bf16x4, fr[0]), k1 = __builtin_bit_cast(bf16x4, fr[1]);
        const bf16x4 v0 = __builtin_bit_cast(bf16x4, fr[2]), v1 = __builtin_bit_cast(bf16x4, fr[3]), T = __builtin_bit_cast(bf16x4, fr[4]);
        const f32x4 z4 = {0.f, 0.f, 0.f, 0.f};
        const f32x4 d1 = MFMA16K16(k0, v0, z4), d2 = MFMA16K16(k1, v1, z4), d3 = MFMA16K16(T, v0, z4), d4 = MFMA16K16(T, v1, z4);
        const f32x4 d5 = MFMA16K16(k0, T, z4), d6 = MFMA16K16(k1, T, z4), d7 = MFMA16K16(T, T, z4);
        lds_ptr red = (lds_ptr)smem + (w ^ 4) * 5120;
        *(lds_f4)(red + (m * 20 + 4 * g) * 4) = d1;
        *(lds_f4)(red + (KV_HEAD + m * 20 + 4 * g) * 4) = d2;
        if (g == 1) {
            *(lds_f2)(red + (m * 20 + 16) * 4) = (f32x2){d3[0], d3[1]};
            *(lds_f2)(red + (KV_HEAD + m * 20 + 16) * 4) = (f32x2){d4[2], d4[3]};
        }
        if (m == 8 || m == 9 || m == 12) {
            const int e = m == 12 ? 18 : 8 + m;
            *(lds_f4)(red + (e * 20 + 4 * g) * 4) = d5;
            if (g == 1) *(lds_f2)(red + (e * 20 + 16) * 4) = (f32x2){d7[0], d7[1]};
        }
        if (m == 10 || m == 11 || m == 12) {
            const int e = m == 12 ? 18 : 6 + m;
            *(lds_f4)(red + (KV_HEAD + e * 20 + 4 * g) * 4) = d6;
            if (g == 1) *(lds_f2)(red + (KV_HEAD + e * 20 + 16) * 4) = (f32x2){d7[2], d7[3]};
        }
        {
            const unsigned o0 = fr[4][0], o1 = fr[4][1];
            const unsigned n0 = __builtin_amdgcn_mov_dpp(o0, 0xB1, 0xf, 0xf, true), n1 = __builtin_amdgcn_mov_dpp(o1, 0xB1, 0xf, 0xf, true);
            if (m == 0 || m == 2)
                *(lds_u4)(red + 4096 + (m >> 1) * 64 + g * 16) =
                    (u32x4){(o0 & 0xffffu) | (n0 << 16), (o0 >> 16) | (n0 & 0xffff0000u), (o1 & 0xffffu) | (n1 << 16), (o1 >> 16) | (n1 & 0xffff0000u)};
        }
        asm volatile("s_waitcnt lgkmcnt(0)\n\ts_barrier" ::: "memory");
        char* kvp = ws + OFF_KV + (size_t)(L * 16 + ct) * 16 * 3072;
        if (tid < 190) {
            f32x4 s4 = *(lds_f4)((lds_ptr)smem + tid * 16);
#pragma unroll
            for (int i = 1; i < 8; ++i) s4 += *(lds_f4)((lds_ptr)smem + i * 5120 + tid * 16);
            gst16c(kvp + panel * 3072 + tid * 16, __builtin_bit_cast(u32x4, s4));
        }
        qt[0] = *(lds_vu32)(red + 4096 + m * 4); qt[1] = *(lds_vu32)(red + 4096 + 64 + m * 4);
    }
}
__device__ __forceinline__ void lin_o_part(char* ws, char* smem, int L, int rb, int ct, int w, int lane, u32x4 (&bq)[2], const Polls& pl) {
    const int m = lane & 15, g = lane >> 4, tid = w * 64 + lane, h0 = 2 * ct;
    char* lay = ws + OFF_LAY + L * LAY_BYTES;
    char* kvp = ws + OFF_KV + (size_t)(L * 16 + ct) * 16 * 3072;
    const f32x4 z4 = {0.f, 0.f, 0.f, 0.f};
    {
        {
            f32x4 pa[3], pb[3];
            const char* pp = kvp + w * 6144 + lane * 16;
            gld16c<0>(pa[0], pp); gld16c<1024>(pa[1], pp); gld16c<2048>(pa[2], pp);
            gld16c<0>(pb[0], pp + 3072); gld16c<1024>(pb[1], pp + 3072); gld16c<2048>(pb[2], pp + 3072);
            asm volatile("s_waitcnt vmcnt(0)" : "+v"(pa[0]), "+v"(pa[1]), "+v"(pa[2]), "+v"(pb[0]), "+v"(pb[1]), "+v"(pb[2]), "+v"(bq[0]), "+v"(bq[1]) :: "memory");
            RETIRE(pl);
#pragma unroll
            for (int i = 0; i < 3; ++i) *(lds_f4)((lds_ptr)smem + w * 3072 + i * 1024 + lane * 16) = pa[i] + pb[i];
            asm volatile("s_waitcnt lgkmcnt(0)\n\ts_barrier" ::: "memory");
            if (tid < 190) {
                f32x4 s4 = *(lds_f4)((lds_ptr)smem + tid * 16);
#pragma unroll
                for (int i = 1; i < 8; ++i) s4 += *(lds_f4)((lds_ptr)smem + i * 3072 + tid * 16);
                *(lds_f4)((lds_ptr)smem + 24576 + tid * 16) = s4;
            }
            asm volatile("s_waitcnt lgkmcnt(0)\n\ts_barrier" ::: "memory");
        }
        f32x4 ka[2], kb[2]; f32x2 ka2[2], kb2[2];
        const int r3 = m & 3; const bool isz = r3 == 2;
        {
            lds_ptr p1 = (lds_ptr)smem + 24576 + m * 80, p2 = (lds_ptr)smem + 24576 + (isz ? 18 : 16 + (r3 & 1)) * 80;
#pragma unroll
            for (int h = 0; h < 2; ++h) {
                ka[h] = *(lds_f4)(p1 + h * KV_HEAD * 4 + 16 * g); ka2[h] = *(lds_f2)(p1 + h * KV_HEAD * 4 + 64);
                kb[h] = *(lds_f4)(p2 + h * KV_HEAD * 4 + 16 * g); kb2[h] = *(lds_f2)(p2 + h * KV_HEAD * 4 + 64);
            }
        }
        const bool use2 = isz || m < 2;
        unsigned om[2][2], ot[2];
#pragma unroll
        for (int h = 0; h < 2; ++h) {
            const u32x4 f1 = {pack2(ka[h][0], ka[h][1]), pack2(ka[h][2], ka[h][3]), g == 0 ? pack2(ka2[h][0], ka2[h][1]) : 0u, 0u};
            u32x4 f2 = {pack2(kb[h][0], kb[h][1]), pack2(kb[h][2], kb[h][3]), g == 0 ? pack2(kb2[h][0], kb2[h][1]) : 0u, 0u};
            if (!use2) f2 = (u32x4){0u, 0u, 0u, 0u};
            const bf16x8 b = __builtin_bit_cast(bf16x8, bq[h]);
            const f32x4 e1 = MFMA16(__builtin_bit_cast(bf16x8, f1), b, z4), e2 = MFMA16(__builtin_bit_cast(bf16x8, f2), b, z4);
            const float r = __builtin_amdgcn_rcpf(e2[2] + 1e-6f);
            om[h][0] = pack2(e1[0] * r, e1[1] * r); om[h][1] = pack2(e1[2] * r, e1[3] * r);
            ot[h] = pack2(e2[0] * r, e2[1] * r);
        }
        gst16c(lay + LO_O + ((size_t)(rb * 16 + ct) * 64 + lane) * 16, (u32x4){om[0][0], om[0][1], om[1][0], om[1][1]});
        {
            const unsigned a0 = __builtin_amdgcn_mov_dpp(ot[0], 0x00, 0xf, 0xf, true), a1 = __builtin_amdgcn_mov_dpp(ot[0], 0x55, 0xf, 0xf, true),
                           a2 = __builtin_amdgcn_mov_dpp(ot[0], 0xAA, 0xf, 0xf, true), a3 = __builtin_amdgcn_mov_dpp(ot[0], 0xFF, 0xf, 0xf, true);
            const unsigned b0 = __builtin_amdgcn_mov_dpp(ot[1], 0x00, 0xf, 0xf, true), b1 = __builtin_amdgcn_mov_dpp(ot[1], 0x55, 0xf, 0xf, true),
                           b2 = __builtin_amdgcn_mov_dpp(ot[1], 0xAA, 0xf, 0xf, true), b3 = __builtin_amdgcn_mov_dpp(ot[1], 0xFF, 0xf, 0xf, true);
            const bool h1 = r3 == 1;
            if (g == 0 && r3 < 2)
                gst16c(lay + LO_O + O_TAIL + ((size_t)(rb * 32 + h0 + r3) * 16 + (m & 12)) * 4, (u32x4){h1 ? b0 : a0, h1 ? b1 : a1, h1 ? b2 : a2, h1 ? b3 : a3});
        }
    }
}

template <int LAYER, bool FUSED>
__device__ __forceinline__ void qkv_phase(char* ws, char* smem, int L, int w, int lane) {
    LAUNDER();
    int m = lane & 15, g = lane >> 4; const int rp = w & 3, kh = w >> 2;
    const int xcd = bid & 7, idx = bid >> 3;
    const int panel = 4 * (xcd >> 1) + (idx >> 3), ct = 8 * (xcd & 1) + (idx & 7);
    const int rb = panel * 8 + 2 * rp + kh, rbo = panel * 8 + 2 * rp + 1 - kh;
    const int xv = 2 * L + LAYER;
    char* lay = ws + OFF_LAY + L * LAY_BYTES;
    f32x4 st[2], cs4[4]; float cs1[5];
    {
        const float* cs = (const float*)(ws + (LAYER ? OFF_CSB : OFF_CSA)) + ct * 112;
        const float* c4 = cs + 4 * g; const float* c1 = cs + m;
        if constexpr (LAYER == 0) {
            gld16<0>(cs4[0], c4); gld16<64>(cs4[1], c4);
            gld4<128>(cs1[0], c1); gld4<192>(cs1[1], c1); gld4<256>(cs1[2], c1); gld4<320>(cs1[3], c1); gld4<384>(cs1[4], c1);
        } else {
            gld16<0>(cs4[0], c4); gld16<64>(cs4[1], c4); gld16<128>(cs4[2], c4); gld16<192>(cs4[3], c4);
            gld4<256>(cs1[0], c1); gld4<320>(cs1[1], c1); gld4<384>(cs1[2], c1);
        }
    }
    if (!FUSED || xv == 0) {
        const Pf pf = pf_qkv(ws, smem, LAYER, bid, w, lane);
        PF(pf, 0); PF(pf, 1); PF(pf, 2); PF(pf, 3); PF(pf, 4); PF(pf, 5); PF(pf, 6); PFQ_LAST(pf);
    }
    const Polls pl = wait_cnt(ws, LAYER ? L * C_LAY + C_WA + panel : C_WB + panel, xv ? 16u : 0u);
    bf16x8 a[2][9];
    const char* xa0 = ws + OFF_XF + xv * XF_BYTES + (size_t)(rb * 18 + kh) * 1024 + lane * 16;
    const char* xa1 = ws + OFF_XF + xv * XF_BYTES + (size_t)(rbo * 18 + kh) * 1024 + lane * 16;
#define LDA(j) do { gld16c<((j) & 1) * 2048>(a[0][j], xa0 + ((j) >> 1) * 4096); gld16c<((j) & 1) * 2048>(a[1][j], xa1 + ((j) >> 1) * 4096); } while (0)
    {
        const char* sp = ws + OFF_ST + xv * ST_BYTES + (size_t)(rb * 16 + m) * 128 + g * 32;
        gld16c<0>(st[0], sp); gld16c<16>(st[1], sp);
    }
    LDA(0); LDA(1); LDA(2);
    const lds_cptr bl = (lds_cptr)smem + LDS_W + kh * 7 * 1024 + lane * 16;
    bf16x8 bb[2][7];
#pragma unroll
    for (int c = 0; c < 7; ++c) bb[0][c] = *(lds_f8)(bl + c * 1024);
    constexpr int NTR = LAYER == 0 ? 2 : 4;
    f32x4 acc[2][7];
#pragma unroll
    for (int c = 0; c < 7; ++c) { acc[0][c] = (f32x4){0.f, 0.f, 0.f, 0.f}; acc[1][c] = (f32x4){0.f, 0.f, 0.f, 0.f}; }
    float nmu = 0.f, rs = 0.f, nmu4[4] = {0.f, 0.f, 0.f, 0.f}, rs4[4] = {0.f, 0.f, 0.f, 0.f};
    auto stats = [&]() {
        if constexpr (LAYER == 0) asm volatile("" : "+v"(st[0]), "+v"(st[1]), "+v"(cs4[0]), "+v"(cs4[1]), "+v"(cs1[0]), "+v"(cs1[1]), "+v"(cs1[2]), "+v"(cs1[3]), "+v"(cs1[4]));
        else asm volatile("" : "+v"(st[0]), "+v"(st[1]), "+v"(cs4[0]), "+v"(cs4[1]), "+v"(cs4[2]), "+v"(cs4[3]), "+v"(cs1[0]), "+v"(cs1[1]), "+v"(cs1[2]));
        float s = rowsum4(st[0][0] + st[0][2] + st[1][0] + st[1][2]);
        const float mean = s * (1.f / 576.f);
        const float d0 = st[0][0] * (1.f / 40.f) - mean, d1 = st[0][2] * (1.f / 32.f) - mean;
        const float d2 = st[1][0] * (1.f / 40.f) - mean, d3 = st[1][2] * (1.f / 32.f) - mean;
        const float q = rowsum4(st[0][1] + st[0][3] + st[1][1] + st[1][3] + 40.f * (d0 * d0 + d2 * d2) + 32.f * (d1 * d1 + d3 * d3));
        nmu = -mean; rs = __builtin_amdgcn_rsqf(q * (1.f / 576.f) + 1e-5f);
#pragma unroll
        for (int r = 0; r < 4; ++r) { nmu4[r] = __shfl(nmu, 4 * g + r); rs4[r] = __shfl(rs, 4 * g + r); }
#pragma unroll
        for (int c = 0; c < NTR; ++c) cs4[c] *= nmu;
        SBAR();
    };
#define MM(j) do { _Pragma("unroll") for (int c = 0; c < 7; ++c) { \
            if ((j) < 8 && !(LAYER == 0 && c < 2 && (j) >= 3)) bb[((j) + 1) & 1][c] = *(lds_f8)(bl + (((j) + 1) * 14 + c) * 1024); \
            SBAR(); \
            if (LAYER == 0 && c < 2 && (j) >= 4) {} else \
            if (c < NTR) { acc[1][c] = MFMA16(bb[(j) & 1][c], a[1][j], acc[1][c]); acc[0][c] = MFMA16(bb[(j) & 1][c], a[0][j], acc[0][c]); } \
            else         { acc[1][c] = MFMA16(a[1][j], bb[(j) & 1][c], acc[1][c]); acc[0][c] = MFMA16(a[0][j], bb[(j) & 1][c], acc[0][c]); } \
            SBAR(); \
        } } while (0)
#define WAITA(j, N) asm volatile("s_waitcnt vmcnt(" #N ")" : "+v"(a[0][j]), "+v"(a[1][j]) :: "memory")
    SBAR();
    WAITA(0, 4); RETIRE(pl); LDA(3); LDA(4); MM(0);
    WAITA(1, 6); LDA(5); LDA(6); MM(1);
    if (kh == 0) stats();
    WAITA(2, 8); LDA(7); LDA(8); MM(2);
    WAITA(3, 10); MM(3);
    WAITA(4, 8); MM(4);
    asm volatile("s_barrier" ::: "memory");
    WAITA(5, 6); MM(5);
    if (kh == 1) stats();
    WAITA(6, 4); MM(6);
    WAITA(7, 2); MM(7);
    WAITA(8, 0); MM(8);
#undef MM
#undef WAITA
#undef LDA
    asm volatile("" : "+v"(lane)); m = lane & 15; g = lane >> 4;
    constexpr int C0 = LAYER == 0 ? 2 : 0, SLOT = (7 - C0) * 1024;
    {
        lds_ptr ex = (lds_ptr)smem + w * SLOT + lane * 16;
#pragma unroll
        for (int c = C0; c < 7; ++c) *(lds_f4)(ex + (c - C0) * 1024) = acc[1][c];
        asm volatile("s_waitcnt lgkmcnt(0)\n\ts_barrier" ::: "memory");
        lds_ptr ey = (lds_ptr)smem + (w ^ 4) * SLOT + lane * 16;
#pragma unroll
        for (int c = C0; c < 7; ++c) {
            acc[0][c] += *(lds_f4)(ey + (c - C0) * 1024);
            if (c < NTR) acc[0][c] += cs4[c];
            else { const float cv = cs1[c - NTR]; acc[0][c] += (f32x4){nmu4[0] * cv, nmu4[1] * cv, nmu4[2] * cv, nmu4[3] * cv}; }
        }
    }
    const int h0 = 2 * ct;
    if constexpr (LAYER == 0) {
        unsigned qt[2];
        lin_kv_part(ws, smem, L, panel, ct, w, lane, acc[0], rs4, qt);
        const lds_cptr bl2 = (lds_cptr)smem + LDS_W + kh * 7 * 1024 + lane * 16;
#pragma unroll
        for (int j = 4; j < 9; ++j) {
            const bf16x8 b0 = *(lds_f8)(bl2 + (j * 14) * 1024), b1 = *(lds_f8)(bl2 + (j * 14 + 1) * 1024);
            acc[1][0] = MFMA16(b0, a[1][j], acc[1][0]); acc[0][0] = MFMA16(b0, a[0][j], acc[0][0]);
            acc[1][1] = MFMA16(b1, a[1][j], acc[1][1]); acc[0][1] = MFMA16(b1, a[0][j], acc[0][1]);
            if (j == 5) signal_cnt<0>(ws, L * C_LAY + C_QA + ct);
        }
        lds_ptr e2 = (lds_ptr)smem + 40960 + w * 2048 + lane * 16;
        *(lds_f4)(e2) = acc[1][0]; *(lds_f4)(e2 + 1024) = acc[1][1];
        asm volatile("s_waitcnt lgkmcnt(0)\n\ts_barrier" ::: "memory");
        const Pf pf = pf_wo(ws, smem, 0, bid, w, lane);
        if (FUSED) { PF(pf, 0); PF(pf, 1); PF(pf, 2); }
        u32x4 bq[2];
        lds_ptr e3 = (lds_ptr)smem + 40960 + (w ^ 4) * 2048 + lane * 16;
#pragma unroll
        for (int c = 0; c < 2; ++c) {
            const f32x4 t = acc[0][c] + *(lds_f4)(e3 + c * 1024) + cs4[c];
            bq[c] = (u32x4){pack2(phi_f(rs * t[0]), phi_f(rs * t[1])), pack2(phi_f(rs * t[2]), phi_f(rs * t[3])), qt[c], 0u};
        }
        const Polls pl2 = wait_cnt(ws, L * C_LAY + C_QA + ct, 16);
        lin_o_part(ws, smem, L, rb, ct, w, lane, bq, pl2);
        signal_cnt<0>(ws, L * C_LAY + C_OA + panel);
        if (FUSED) { PF(pf, 3); PF(pf, 4); PFO_LAST(pf); }
    } else {
        const int tok = rb * 16 + m;
        char* img = lay + LO_IMG + (size_t)h0 * IMG_HEAD + (size_t)(rb >> 4) * IMG_CHUNK;
        const float rq = rs * QSCALE;
#pragma unroll
        for (int p = 0; p < 2; ++p) {
            const float sc = p == 0 ? rq : rs;
            const f32x4 A = acc[0][2 * p], B = acc[0][2 * p + 1];
            auto s0 = __builtin_amdgcn_permlane16_swap(pack2(sc * A[0], sc * A[1]), pack2(sc * B[0], sc * B[1]), false, false);
            auto s1 = __builtin_amdgcn_permlane16_swap(pack2(sc * A[2], sc * A[3]), pack2(sc * B[2], sc * B[3]), false, false);
            const u32x4 d = {s0[0], s1[0], s0[1], s1[1]};
            if (p == 0) gst16c(lay + LO_Q1 + ((size_t)(h0 + (g & 1)) * SEQ + tok) * 32 + (g >> 1) * 16, d);
            else gst16c(img + (g & 1) * IMG_HEAD + ((tok & 255) >> 5) * 1024 + (g >> 1) * 512 + (tok & 31) * 16, d);
        }
        {
            const f32x4 A = acc[0][4], B = acc[0][5];
            auto s0 = __builtin_amdgcn_permlane32_swap(pack2(rs4[0] * A[0], rs4[1] * A[1]), pack2(rs4[0] * B[0], rs4[1] * B[1]), false, false);
            auto s1 = __builtin_amdgcn_permlane32_swap(pack2(rs4[2] * A[2], rs4[3] * A[3]), pack2(rs4[2] * B[2], rs4[3] * B[3]), false, false);
            gst16c(img + (g >> 1) * IMG_HEAD + 9216 + ((rb & 15) * 2 + (g & 1)) * 320 + m * 16, (u32x4){s0[0], s1[0], s0[1], s1[1]});
        }
        {
            const float qs = m < 4 ? QSCALE : 1.f;
            const f32x4 A = acc[0][6];
            const unsigned o0 = pack2(qs * rs4[0] * A[0], qs * rs4[1] * A[1]), o1 = pack2(qs * rs4[2] * A[2], qs * rs4[3] * A[3]);
            const unsigned n0 = __builtin_amdgcn_mov_dpp(o0, 0xB1, 0xf, 0xf, true), n1 = __builtin_amdgcn_mov_dpp(o1, 0xB1, 0xf, 0xf, true);
            auto u0 = __builtin_amdgcn_permlane32_swap(o0, o0, false, false); auto u1 = __builtin_amdgcn_permlane32_swap(o1, o1, false, false);
            const int hsel = (m >> 1) & 1, tok0 = rb * 16 + 4 * g;
            u32x4 d; char* p;
            if (m < 8) {
                d = (u32x4){(o0 & 0xffffu) | (n0 << 16), (o0 >> 16) | (n0 & 0xffff0000u), (o1 & 0xffffu) | (n1 << 16), (o1 >> 16) | (n1 & 0xffff0000u)};
                p = m < 4 ? lay + LO_Q2 + ((size_t)(h0 + hsel) * SEQ + tok0) * 4 : img + hsel * IMG_HEAD + 8192 + (tok0 & 255) * 4;
            } else {
                const unsigned cst = (m & 1) ? 0u : 0x3f803f80u;
                d = m < 12 ? (u32x4){o0, o1, u0[1], u1[1]} : (u32x4){cst, cst, cst, cst};
                p = img + hsel * IMG_HEAD + 9216 + ((rb & 15) * 2 + (g & 1)) * 320 + (16 + (m & 1) + ((m >> 1) & 2)) * 16;
            }
            if (m < 8 ? !(m & 1) : g < 2) gst16c(p, d);
        }
        signal_cnt<0>(ws, L * C_LAY + C_QB + ct);
    }
}

template <bool FUSED>
__device__ __forceinline__ void wo_phase(char* ws, char* smem, const unsigned short* x0, unsigned short* out, int L, int mode, int w, int lane) {
    LAUNDER();
    const int m = lane & 15, g = lane >> 4, rb4 = w & 3, kh = w >> 2;
    const int xcd = bid & 7, idx = bid >> 3;
    const int panel = 4 * xcd + (idx >> 3), jt = idx & 7;
    const int rb = panel * 4 + rb4;
    const int xv = 2 * L + mode;
    const char* ob = ws + OFF_LAY + L * LAY_BYTES + LO_O + mode * O_BYTES;
    const bool last = xv == 3;
    const bool dopf = FUSED && !last;
    const Pf pf = pf_qkv(ws, smem, mode ? 0 : 1, bid, w, lane);
    if (!FUSED) { const Pf po = pf_wo(ws, smem, mode, bid, w, lane); PF(po, 0); PF(po, 1); PF(po, 2); PF(po, 3); PF(po, 4); PFO_LAST(po); }
    u32x2 xr[3];
#pragma unroll
    for (int c = 0; c < 3; ++c) {
        const int cb = c == 2 ? 4 : 2 * kh + c, gg = c == 2 ? (g & 1) : g;
        const int n0 = 72 * jt + 16 * cb + 4 * gg;
        const char* xp = xv == 0 ? (const char*)(x0 + (size_t)(rb * 16 + m) * 576 + n0)
                                 : ws + OFF_XF + xv * XF_BYTES + ((size_t)(rb * 18 + (n0 >> 5)) * 64 + ((n0 >> 3) & 3) * 16 + m) * 16 + ((n0 >> 2) & 1) * 8;
        gld8c<0>(xr[c], xp);
    }
    if (dopf) { PF(pf, 0); PF(pf, 1); }
    const Polls pl = wait_cnt(ws, L * C_LAY + (mode ? C_AT + (panel >> 2) : C_OA + (panel >> 1)), mode ? 32u : 16u);
    asm volatile("" : "+v"(xr[0]), "+v"(xr[1]), "+v"(xr[2]));
    bf16x8 af[9]; unsigned pt[4];
    {
        const char* op = ob + ((size_t)(rb * 16 + 8 * kh) * 64 + lane) * 16;
        gld16c<0>(af[0], op); gld16c<1024>(af[1], op); gld16c<2048>(af[2], op); gld16c<3072>(af[3], op);
        gld16c<0>(af[4], op + 4096); gld16c<1024>(af[5], op + 4096); gld16c<2048>(af[6], op + 4096); gld16c<3072>(af[7], op + 4096);
        const char* tp = ob + O_TAIL + ((size_t)(rb * 32 + 16 * kh + 4 * g) * 16 + m) * 4;
        gld4c<0>(pt[0], tp); gld4c<64>(pt[1], tp); gld4c<128>(pt[2], tp); gld4c<192>(pt[3], tp);
    }
    f32x4 acc[5];
#pragma unroll
    for (int c = 0; c < 5; ++c) acc[c] = (f32x4){0.f, 0.f, 0.f, 0.f};
    const lds_cptr bl = (lds_cptr)smem + LDS_WO + kh * 5 * 1024 + lane * 16;
    bf16x8 bb[3][5];
#pragma unroll
    for (int c = 0; c < 5; ++c) { bb[0][c] = *(lds_f8)(bl + c * 1024); bb[1][c] = *(lds_f8)(bl + (10 + c) * 1024); }
#define KSTEP(i_) do { _Pragma("unroll") for (int c = 0; c < 5; ++c) { \
        if ((i_) < 7) bb[((i_) + 2) % 3][c] = *(lds_f8)(bl + (((i_) + 2) * 10 + c) * 1024); \
        SBAR(); acc[c] = MFMA16(bb[(i_) % 3][c], af[i_], acc[c]); SBAR(); } } while (0)
#define WAITF(i_, N) asm volatile("s_waitcnt vmcnt(" #N ")" : "+v"(af[i_]) :: "memory")
    SBAR();
    WAITF(0, 11); RETIRE(pl); KSTEP(0); WAITF(1, 10); KSTEP(1); WAITF(2, 9); KSTEP(2); WAITF(3, 8); KSTEP(3);
    WAITF(4, 7); KSTEP(4); WAITF(5, 6); KSTEP(5); WAITF(6, 5); KSTEP(6); WAITF(7, 4); KSTEP(7);
    asm volatile("s_waitcnt vmcnt(0)" : "+v"(pt[0]), "+v"(pt[1]), "+v"(pt[2]), "+v"(pt[3]) :: "memory");
    af[8] = __builtin_bit_cast(bf16x8, (u32x4){pt[0], pt[1], pt[2], pt[3]});
    KSTEP(8);
#undef KSTEP
#undef WAITF
    lds_ptr ex = (lds_ptr)smem + rb4 * 5 * 1024 + lane * 16;
    if (kh == 0) { *(lds_f4)(ex + 2 * 1024) = acc[2]; *(lds_f4)(ex + 3 * 1024) = acc[3]; }
    else { *(lds_f4)(ex) = acc[0]; *(lds_f4)(ex + 1024) = acc[1]; *(lds_f4)(ex + 4 * 1024) = acc[4]; }
    asm volatile("s_waitcnt lgkmcnt(0)\n\ts_barrier" ::: "memory");
    unsigned pk[3][2];
    {
        const f32x4 o0 = (kh ? acc[2] : acc[0]) + *(lds_f4)(ex + (2 * kh) * 1024), o1 = (kh ? acc[3] : acc[1]) + *(lds_f4)(ex + (2 * kh + 1) * 1024);
        const f32x4 o2 = acc[4] + *(lds_f4)(ex + 4 * 1024);
        pk[0][0] = pack2(o0[0] + bf_lo(xr[0][0]), o0[1] + bf_hi(xr[0][0])); pk[0][1] = pack2(o0[2] + bf_lo(xr[0][1]), o0[3] + bf_hi(xr[0][1]));
        pk[1][0] = pack2(o1[0] + bf_lo(xr[1][0]), o1[1] + bf_hi(xr[1][0])); pk[1][1] = pack2(o1[2] + bf_lo(xr[1][1]), o1[3] + bf_hi(xr[1][1]));
        pk[2][0] = pack2(o2[0] + bf_lo(xr[2][0]), o2[1] + bf_hi(xr[2][0])); pk[2][1] = pack2(o2[2] + bf_lo(xr[2][1]), o2[3] + bf_hi(xr[2][1]));
    }
    {
        auto s0 = __builtin_amdgcn_permlane16_swap(pk[0][0], pk[1][0], false, false), s1 = __builtin_amdgcn_permlane16_swap(pk[0][1], pk[1][1], false, false);
        auto t0 = __builtin_amdgcn_permlane16_swap(pk[2][0], pk[2][0], false, false), t1 = __builtin_amdgcn_permlane16_swap(pk[2][1], pk[2][1], false, false);
        const u32x4 d = {s0[0], s1[0], s0[1], s1[1]}, e = {t0[0], t1[0], t0[1], t1[1]};
        const int n0 = 72 * jt + 32 * kh + 16 * (g & 1) + 8 * (g >> 1), n1 = 72 * jt + 64;
        if (last) {
            typedef __attribute__((address_space(1))) u32x4* g_u4;
            unsigned short* orow = out + (size_t)(rb * 16 + m) * 576;
            *(g_u4)(orow + n0) = d;
            if (kh == 0 && g == 0) *(g_u4)(orow + n1) = e;
        } else {
            char* xo = ws + OFF_XF + (xv + 1) * XF_BYTES + (size_t)rb * 18 * 1024 + m * 16;
            gst16c(xo + (n0 >> 5) * 1024 + ((n0 >> 3) & 3) * 256, d);
            if (kh == 0 && g == 0) gst16c(xo + (n1 >> 5) * 1024 + ((n1 >> 3) & 3) * 256, e);
        }
    }
    if (!last) {
        const bool v2 = kh == 0 && g < 2;
        float v[12];
#pragma unroll
        for (int c = 0; c < 3; ++c) { v[4 * c] = bf_lo(pk[c][0]); v[4 * c + 1] = bf_hi(pk[c][0]); v[4 * c + 2] = bf_lo(pk[c][1]); v[4 * c + 3] = bf_hi(pk[c][1]); }
        float s = ((v[0] + v[1]) + (v[2] + v[3])) + ((v[4] + v[5]) + (v[6] + v[7])) + (v2 ? (v[8] + v[9]) + (v[10] + v[11]) : 0.f);
        s = rowsum4(s);
        const float mean = s * (kh ? (1.f / 32.f) : (1.f / 40.f));
        float q = 0.f, q2 = 0.f;
#pragma unroll
        for (int r = 0; r < 8; ++r) { const float dd = v[r] - mean; q += dd * dd; }
#pragma unroll
        for (int r = 8; r < 12; ++r) { const float dd = v[r] - mean; q2 += dd * dd; }
        q += v2 ? q2 : 0.f;
        q = rowsum4(q);
        if (g == 0) gst8c(ws + OFF_ST + (xv + 1) * ST_BYTES + (size_t)(rb * 16 + m) * 128 + jt * 16 + kh * 8, (f32x2){s, q});
        const int line = L * C_LAY + (mode ? C_WB : C_WA) + (panel >> 1);
        signal_cnt<0>(ws, line);
        if (FUSED) { PF(pf, 2); PF(pf, 3); PF(pf, 4); PF(pf, 5); PF(pf, 6); PFQ_LAST(pf); }
    }
}

__device__ __forceinline__ float half_max(float v) {
    auto r = __builtin_amdgcn_permlane32_swap(__float_as_uint(v), __float_as_uint(v), false, false);
    return fmaxf(__uint_as_float(r[0]), __uint_as_float(r[1]));
}
#define STR_(x) #x
#define STR(x) STR_(x)
#define AEXP(r) "v_exp_f32 v" STR(r) ", v" STR(r) "\n\t"
#define ACVT(d, r0, r1) "v_cvt_pk_bf16_f32 v" STR(d) ", v" STR(r0) ", v" STR(r1) "\n\t"
#define AE4(r0, r1, r2, r3, d0, d1) AEXP(r0) AEXP(r1) AEXP(r2) AEXP(r3) ACVT(d0, r0, r1) ACVT(d1, r2, r3)
#define ATILE(KO, K2O, V1O, V0O, XK, X0, X1, XV) \
    "v_mfma_f32_32x32x16_bf16 v[48:63], v[80:83], v[72:75], 0\n\t" XK \
    "ds_read_b128 v[80:83], v104 offset:" STR(KO) "\n\t" \
    AE4(32, 33, 34, 35, 96, 97) \
    "v_mfma_f32_32x32x16_bf16 v[16:31], v[92:95], v[100:103], v[16:31]\n\t" \
    "ds_read_b128 v[92:95], v106 offset:" STR(V1O) "\n\t" X0 \
    AE4(36, 37, 38, 39, 98, 99) \
    "v_mfma_f32_32x32x16_bf16 v[48:63], v[84:87], v[76:79], v[48:63]\n\t" \
    "ds_read_b32 v84, v105 offset:" STR(K2O) "\n\t" \
    AE4(40, 41, 42, 43, 100, 101) \
    "s_waitcnt lgkmcnt(3)\n\t" \
    "v_mfma_f32_32x32x16_bf16 v[0:15], v[88:91], v[96:99], v[0:15]\n\t" \
    AE4(44, 45, 46, 47, 102, 103) \
    "s_waitcnt lgkmcnt(1)\n\t" \
    "v_mfma_f32_32x32x16_bf16 v[32:47], v[80:83], v[64:67], 0\n\t" \
    AE4(48, 49, 50, 51, 96, 97) \
    "v_mfma_f32_32x32x16_bf16 v[0:15], v[92:95], v[100:103], v[0:15]\n\t" X1 \
    AE4(52, 53, 54, 55, 98, 99) \
    "s_waitcnt lgkmcnt(0)\n\t" \
    "v_mfma_f32_32x32x16_bf16 v[32:47], v[84:87], v[68:71], v[32:47]\n\t" \
    AE4(56, 57, 58, 59, 100, 101) \
    "v_mfma_f32_32x32x16_bf16 v[16:31], v[88:91], v[96:99], v[16:31]\n\t" XV \
    "ds_read_b128 v[88:91], v106 offset:" STR(V0O) "\n\t" \
    AE4(60, 61, 62, 63, 102, 103)
#define ACHUNK \
    "s_mov_b32 m0, s22\n\ts_add_u32 s28, s20, 0x2000\n\ts_addc_u32 s29, s21, 0\n\tglobal_load_lds_dwordx4 v107, s[20:21] sc1\n\t" \
    "s_add_u32 s27, s22, 0x2000\n\ts_mov_b32 m0, s27\n\ts_cmp_lt_u32 %[w], 3\n\tglobal_load_lds_dwordx4 v107, s[28:29] sc1\n\t" \
    "s_cbranch_scc0 7f\n\t" \
    "s_add_u32 s27, s22, 0x4000\n\ts_mov_b32 m0, s27\n\ts_add_u32 s28, s20, 0x4000\n\ts_addc_u32 s29, s21, 0\n\tglobal_load_lds_dwordx4 v107, s[28:29] sc1\n\t" \
    "7:\n\t" \
    "s_add_u32 s20, s20, 0x4c00\n\ts_addc_u32 s21, s21, 0\n\t"
#define AWPF \
    "s_mov_b32 m0, s25\n\ts_add_u32 s25, s25, 0x4000\n\tglobal_load_lds_dwordx4 v107, s[30:31]\n\tglobal_load_lds_dwordx4 v107, s[30:31] offset:1024\n\t" \
    "s_add_u32 s30, s30, 0x4000\n\ts_addc_u32 s31, s31, 0\n\t"
#define AKADV "v_add_u32 v104, s23, v104\n\tv_add_u32 v105, s23, v105\n\t"
#define AVADV "v_add_u32 v106, s23, v106\n\t"

template <bool FUSED>
__device__ __forceinline__ void attn_phase(char* ws, char* smem, int L, int w, int lane) {
    LAUNDER();
    const int r32 = lane & 31, hh = lane >> 5, kh = w >> 2;
    const int xcd = bid & 7, idx = bid >> 3;
    const int head = 4 * xcd + (idx >> 3), qb = idx & 7;
    const int qA = qb * 256 + (w & 3) * 64 + r32;
    char* lay = ws + OFF_LAY + L * LAY_BYTES;
    const unsigned lds0 = lds_off(smem) + w * 1024;
    const Polls pl = wait_cnt(ws, L * C_LAY + C_QB + (head >> 1), 16);
    bf16x8 q1a, q1b; unsigned q2wa, q2wb;
    {
        const char* qp = lay + LO_Q1 + ((size_t)head * SEQ + qA) * 32 + hh * 16;
        const char* q2p = lay + LO_Q2 + ((size_t)head * SEQ + qA) * 4;
        gld16c<0>(q1a, qp); gld16c<1024>(q1b, qp); gld4c<0>(q2wa, q2p); gld4c<128>(q2wb, q2p);
    }
    const char* srcw = lay + LO_IMG + (size_t)head * IMG_HEAD + w * 1024;
    {
        const char* src = srcw + lane * 16;
#pragma unroll
        for (int k = 0; k < 4; ++k) glds1_sc1(src + k * 8192, lds0 + k * 8192);
        glds1_sc1(src + (w < 6 ? 4 : 3) * 8192, lds0 + (w < 6 ? 4 : 3) * 8192);
    }
    lds_cptr kp1 = (lds_cptr)smem + lane * 16;
    lds_cptr kp2 = (lds_cptr)smem + 8192 + r32 * 4;
    const int dmap = r32 < 19 ? r32 : 19;
    lds_cptr vp = (lds_cptr)smem + 9216 + hh * 320 + dmap * 16;
    asm volatile("s_waitcnt vmcnt(2)\n\ts_barrier" : "+v"(q1a), "+v"(q1b), "+v"(q2wa), "+v"(q2wb) :: "memory");
    RETIRE(pl);
#define LDK1(t) (*(lds_f8)(kp1 + (t) * 1024))
#define LDK2(t) (__builtin_bit_cast(bf16x8, (u32x4){*(lds_vu32)(kp2 + (t) * 128), 0x3f80u, 0u, 0u}))
#define LDV(u) (*(lds_f8)(vp + (u) * 640))
    const f32x16 z16 = {};
    f32x16 s0, s1, oa = {}, ob = {};
    bf16x8 k1 = LDK1(0), k2 = LDK2(0), v0, v1, q2a, q2b;
    u32x4 p0, p1 = {0u, 0u, 0u, 0u};
    {
        q2a = __builtin_bit_cast(bf16x8, (u32x4){hh ? 0u : q2wa, 0u, 0u, 0u});
        q2b = __builtin_bit_cast(bf16x8, (u32x4){hh ? 0u : q2wb, 0u, 0u, 0u});
        f32x16 sa = MFMA32(k1, q1a, z16), sb = MFMA32(k1, q1b, z16); sa = MFMA32(k2, q2a, sa); sb = MFMA32(k2, q2b, sb);
        kp1 += kh * 4096; kp2 += kh * 512; vp += kh * 5120;
        k1 = LDK1(0); k2 = LDK2(0); v0 = LDV(0); v1 = v0;
        float ma = fmaxf(sa[0], sa[1]), mb = fmaxf(sb[0], sb[1]);
#pragma unroll
        for (int i = 2; i < 16; ++i) { ma = fmaxf(ma, sa[i]); mb = fmaxf(mb, sb[i]); }
        ma = half_max(ma); mb = half_max(mb);
        q2a = __builtin_bit_cast(bf16x8, (u32x4){hh ? 0u : q2wa, hh ? 0u : (pack2(-ma, 0.f) & 0xffffu), 0u, 0u});
        q2b = __builtin_bit_cast(bf16x8, (u32x4){hh ? 0u : q2wb, hh ? 0u : (pack2(-mb, 0.f) & 0xffffu), 0u, 0u});
    }
    s0 = MFMA32(k1, q1a, z16); s0 = MFMA32(k2, q2a, s0);
    {
        unsigned a1 = (unsigned)(size_t)kp1, a2 = (unsigned)(size_t)kp2, a3 = (unsigned)(size_t)vp;
        const unsigned loff = lane * 16;
        const char* dsrc = srcw + 2 * IMG_CHUNK; const unsigned dbase = lds0;
        const int jt = (bid >> 3) & 7;
        const char* wsrc = ws + OFF_WOB + (size_t)jt * WO_TILE + w * 2048; const unsigned wdst = lds_off(smem) + LDS_WO + w * 2048;
        asm volatile(
            "s_mov_b32 s26, m0\n\t"
            "s_mov_b64 s[20:21], %[dsrc]\n\t"
            "s_add_u32 s22, %[dbase], 0x9800\n\t"
            "s_mov_b64 s[30:31], %[wsrc]\n\t"
            "s_mov_b32 s25, %[wdst]\n\t"
            "s_mov_b32 s24, 0\n\t"
            "s_mov_b32 s33, 0\n\t"
            "s_mov_b32 s34, 0x4c00\n\t"
            "s_waitcnt lgkmcnt(0)\n\t"
            "s_nop 7\n\t"
            "s_nop 3\n\t"
            "2:\n\t"
            "s_setprio 2\n\t"
            ATILE(1024, 128, 640, 1280, "", "", "", "")
            "s_setprio 1\n\t"
            "s_cmp_eq_u32 s33, 2\n\t"
            "s_cselect_b32 s23, 0xffff6800, s34\n\t"
            "s_sub_u32 s35, s24, 1\n\t"
            "s_cmp_lt_u32 s35, %[npf]\n\t"
            "s_cbranch_scc0 3f\n\t"
            AWPF
            "3:\n\t"
            ATILE(2048, 256, 1920, 2560, "", "", "", "")
            "s_setprio 0\n\t"
            ATILE(3072, 384, 3200, 3840, "", "", "", "")
            "s_waitcnt vmcnt(2)\n\t"
            "s_cmp_lt_u32 s35, %[npf]\n\t"
            "s_cbranch_scc1 4f\n\t"
            "s_waitcnt vmcnt(0)\n\t"
            "4:\n\t"
            "s_barrier\n\t"
            "s_cmp_lt_u32 s24, 6\n\t"
            "s_cbranch_scc0 5f\n\t"
            ACHUNK
            "s_cmp_eq_u32 s33, 0\n\t"
            "s_cselect_b32 s27, 0xffff6800, s34\n\t"
            "s_add_u32 s22, s22, s27\n\t"
            "5:\n\t"
            "s_setprio 3\n\t"
            ATILE(0, 0, 4480, 0, AKADV, "", "", AVADV)
            "s_add_u32 s24, s24, 1\n\t"
            "s_add_u32 s33, s33, 1\n\t"
            "s_cmp_eq_u32 s33, 3\n\t"
            "s_cselect_b32 s33, 0, s33\n\t"
            "s_cmp_lt_u32 s24, 8\n\t"
            "s_cbranch_scc1 2b\n\t"
            "s_waitcnt lgkmcnt(0)\n\t"
            "v_mfma_f32_32x32x16_bf16 v[16:31], v[92:95], v[100:103], v[16:31]\n\t"
            "s_mov_b32 m0, s26\n\t"
            "s_setprio 0\n\t"
            "s_nop 7\n\t"
            "s_nop 3\n\t"
            : "+{v[0:15]}"(oa), "+{v[16:31]}"(ob), "+{v[32:47]}"(s0), "={v[48:63]}"(s1), "+{v[80:83]}"(k1), "+{v[84:87]}"(k2), "+{v[88:91]}"(v0), "+{v[92:95]}"(v1),
              "={v[96:99]}"(p0), "+{v[100:103]}"(p1), "+{v104}"(a1), "+{v105}"(a2), "+{v106}"(a3)
            : "{v[64:67]}"(q1a), "{v[68:71]}"(q2a), "{v[72:75]}"(q1b), "{v[76:79]}"(q2b), "{v107}"(loff), [dsrc] "s"(dsrc), [dbase] "s"(dbase),
              [wsrc] "s"(wsrc), [wdst] "s"(wdst), [w] "s"(w), [npf] "s"(FUSED ? (w < 5 ? 6 : 5) : 0)
            : "s20", "s21", "s22", "s23", "s24", "s25", "s26", "s27", "s28", "s29", "s30", "s31", "s33", "s34", "s35", "scc", "memory");
    }
    lds_ptr ex = (lds_ptr)smem + (w < 6 ? w * 3072 : 2 * (int)IMG_CHUNK + 12288 + (w - 6) * 3072) + lane * 16;
    const int wo_ = w ^ 4;
    lds_ptr ey = (lds_ptr)smem + (wo_ < 6 ? wo_ * 3072 : 2 * (int)IMG_CHUNK + 12288 + (wo_ - 6) * 3072) + lane * 16;
    f32x16 o;
    {
        const f32x16 snd = kh ? oa : ob; o = kh ? ob : oa;
        *(lds_f4)(ex) = (f32x4){snd[0], snd[1], snd[2], snd[3]}; *(lds_f4)(ex + 1024) = (f32x4){snd[4], snd[5], snd[6], snd[7]};
        *(lds_f4)(ex + 2048) = (f32x4){snd[8], snd[9], snd[10], snd[11]};
    }
    asm volatile("s_waitcnt lgkmcnt(0)\n\ts_barrier" ::: "memory");
    {
        const f32x4 a = *(lds_f4)(ey), b = *(lds_f4)(ey + 1024), c = *(lds_f4)(ey + 2048);
#pragma unroll
        for (int i = 0; i < 4; ++i) { o[i] += a[i]; o[4 + i] += b[i]; o[8 + i] += c[i]; }
    }
    const int q = qA + 32 * kh;
    bool bad = !(fabsf(o[10]) < 1.0e30f);
#pragma unroll
    for (int i = 0; i < 10; ++i) bad = bad || !(fabsf(o[i]) < 1.0e38f);
    if (__any(bad)) {
        o = (f32x16){};
        float mrun = -INFINITY;
        const bf16x8 q1 = kh ? q1b : q1a;
        const bf16x8 q2 = __builtin_bit_cast(bf16x8, (u32x4){hh ? 0u : (kh ? q2wb : q2wa), 0u, 0u, 0u});
        const char* img = lay + LO_IMG + (size_t)head * IMG_HEAD;
#pragma unroll 1
        for (int t = 0; t < 64; ++t) {
            const char* cb = img + (t >> 3) * IMG_CHUNK; const int tt = t & 7;
            const char* vb = cb + 9216 + tt * 1280 + hh * 320 + dmap * 16;
            bf16x8 fk, fv0, fv1; unsigned fk2;
            gld16c<0>(fk, cb + tt * 1024 + lane * 16); gld4c<0>(fk2, cb + 8192 + tt * 128 + r32 * 4); gld16c<0>(fv0, vb); gld16c<640>(fv1, vb);
            asm volatile("s_waitcnt vmcnt(0)" : "+v"(fk), "+v"(fk2), "+v"(fv0), "+v"(fv1) :: "memory");
            f32x16 s = MFMA32(fk, q1, z16);
            s = MFMA32(__builtin_bit_cast(bf16x8, (u32x4){fk2, 0u, 0u, 0u}), q2, s);
            float mx = fmaxf(s[0], s[1]);
#pragma unroll
            for (int i = 2; i < 16; ++i) mx = fmaxf(mx, s[i]);
            mx = fmaxf(half_max(mx), mrun);
            const float al = EXP2(mrun - mx);
            mrun = mx;
#pragma unroll
            for (int i = 0; i < 16; ++i) o[i] *= al;
#pragma unroll
            for (int i = 0; i < 4; ++i) {
                p0[i] = pack2(EXP2(s[2 * i] - mx), EXP2(s[2 * i + 1] - mx));
                p1[i] = pack2(EXP2(s[8 + 2 * i] - mx), EXP2(s[8 + 2 * i + 1] - mx));
            }
            o = MFMA32(fv0, __builtin_bit_cast(bf16x8, p0), o);
            o = MFMA32(fv1, __builtin_bit_cast(bf16x8, p1), o);
        }
    }
    {
        auto r = __builtin_amdgcn_permlane32_swap(__float_as_uint(o[10]), __float_as_uint(o[10]), false, false);
        const float rl = __builtin_amdgcn_rcpf(__uint_as_float(r[0]));
        const int rb = q >> 4, m = q & 15;
        gst16c(lay + LO_O + O_BYTES + ((size_t)(rb * 16 + (head >> 1)) * 64 + (2 * (head & 1) + hh) * 16 + m) * 16,
               (u32x4){pack2(o[0] * rl, o[1] * rl), pack2(o[2] * rl, o[3] * rl), pack2(o[4] * rl, o[5] * rl), pack2(o[6] * rl, o[7] * rl)});
        const unsigned t0 = pack2(o[8] * rl, o[9] * rl);
        const unsigned t1 = __builtin_amdgcn_mov_dpp(t0, 0x55, 0xf, 0xf, true), t2 = __builtin_amdgcn_mov_dpp(t0, 0xAA, 0xf, 0xf, true),
                       t3 = __builtin_amdgcn_mov_dpp(t0, 0xFF, 0xf, 0xf, true);
        if (hh == 0 && (r32 & 3) == 0) gst16c(lay + LO_O + O_BYTES + O_TAIL + ((size_t)(rb * 32 + head) * 16 + m) * 4, (u32x4){t0, t1, t2, t3});
    }
    signal_cnt<0>(ws, L * C_LAY + C_AT + qb);
#undef LDK1
#undef LDK2
#undef LDV
}

__global__ __launch_bounds__(512) void prep_kernel(const unsigned short* __restrict__ x0, const unsigned short* __restrict__ wA,
                                                   const unsigned short* __restrict__ woA, const unsigned short* __restrict__ wB,
                                                   const unsigned short* __restrict__ woB, char* ws) {
    __shared__ __attribute__((aligned(16))) char tbuf[7 * 4096];
    __shared__ __attribute__((aligned(16))) float red[72 * 16];
    const int tid = threadIdx.x, bid = blockIdx.x, w = __builtin_amdgcn_readfirstlane(tid >> 6), l = tid & 63;
    const int lin = (bid & 7) * 27 + (bid >> 3);
    const bool jobW = (bid >> 3) < 27;
    const int cmat = lin >= 108, cblk = lin - 108 * cmat;
    u32x2 sv[8]; u32x4 xv[9];
    if (w < 5) {
        if (jobW) {
            const int T = min(tid, 287);
            const char* p = (const char*)((cmat ? wB : wA) + (size_t)(8 * (T >> 2)) * 1728 + 16 * cblk + 4 * (T & 3));
            gld8<0>(sv[0], p); gld8<3456>(sv[1], p); p += 6912; gld8<0>(sv[2], p); gld8<3456>(sv[3], p); p += 6912;
            gld8<0>(sv[4], p); gld8<3456>(sv[5], p); p += 6912; gld8<0>(sv[6], p); gld8<3456>(sv[7], p);
        }
    } else if (w < 7) {
        const int T = bid * 90 + min(tid - 320, 89), jmat = T / 11520, r = T - jmat * 11520, jsg = r / 160, jq = r - jsg * 160;
        const int jt = jq / 20, lc = min(4 * (jq - jt * 20), 68), ss = jsg >> 2, gg = jsg & 3;
        int r0, rb_, rc;
        if (ss >= 16) { r0 = (16 * (ss - 16) + 4 * gg) * 18 + 16; rb_ = 18; rc = 36; }
        else if (jmat == 0) { r0 = 36 * ss + 4 * gg; rb_ = 2; rc = 18; }
        else { r0 = (2 * ss + (gg >> 1)) * 18 + 4 * (gg & 1); rb_ = 2; rc = 8; }
        const char* p = (const char*)((jmat ? woB : woA) + (size_t)r0 * 576 + 72 * jt + lc);
        const char* p2 = p + rb_ * 1152; const char* p4 = p + rc * 1152; const char* p6 = p4 + rb_ * 1152;
        gld8<0>(sv[0], p); gld8<1152>(sv[1], p); gld8<0>(sv[2], p2); gld8<1152>(sv[3], p2);
        gld8<0>(sv[4], p4); gld8<1152>(sv[5], p4); gld8<0>(sv[6], p6); gld8<1152>(sv[7], p6);
    } else {
        const int T = bid * 64 + l, m = T & 15, j = (T >> 4) & 7, rb = T >> 7;
        const char* p = (const char*)(x0 + (size_t)(rb * 16 + m) * 576 + 72 * j);
        gld16<0>(xv[0], p); gld16<16>(xv[1], p); gld16<32>(xv[2], p); gld16<48>(xv[3], p); gld16<64>(xv[4], p);
        gld16<80>(xv[5], p); gld16<96>(xv[6], p); gld16<112>(xv[7], p); gld16<128>(xv[8], p);
    }
    int tct, tcb, tc; qkv_col_map(16 * cblk + (l & 15), tct, tcb, tc);
    asm volatile("s_waitcnt vmcnt(0)" : "+v"(sv[0]), "+v"(sv[1]), "+v"(sv[2]), "+v"(sv[3]), "+v"(sv[4]), "+v"(sv[5]), "+v"(sv[6]), "+v"(sv[7]) :: "memory");
    asm volatile("" : "+v"(xv[0]), "+v"(xv[1]), "+v"(xv[2]), "+v"(xv[3]), "+v"(xv[4]), "+v"(xv[5]), "+v"(xv[6]), "+v"(xv[7]), "+v"(xv[8]));
    u32x4 cc[4];
    if (w < 7) {
        transpose8x4(sv, cc);
        lds_ptr tb = (lds_ptr)tbuf + w * 4096;
#pragma unroll
        for (int i = 0; i < 4; ++i) *(lds_u4)(tb + (l * 4 + i) * 16) = cc[i];
#pragma unroll
        for (int i = 0; i < 4; ++i) cc[i] = *(lds_u4)(tb + (i * 64 + l) * 16);
        if (w < 5) {
#pragma unroll
            for (int i = 0; i < 4; ++i) {
                const int sg = 16 * w + 4 * i + (l >> 4);
                if (sg < 72) red[sg * 16 + (l & 15)] = dot_ones(cc[i][3], dot_ones(cc[i][2], dot_ones(cc[i][1], dot_ones(cc[i][0], 0.f))));
            }
        }
    }
    if (w < 5) {
        if (jobW) {
            char* dst = ws + (cmat ? OFF_WTB : OFF_WTA) + ((size_t)(tct * 18 * 7 + tcb) * 64 + (l >> 4) * 16 + tc) * 16;
#pragma unroll
            for (int i = 0; i < 4; ++i)
                if (4 * w + i < 18) gst16c(dst + (4 * w + i) * 7168, cc[i]);
        }
    } else if (w < 7) {
#pragma unroll
        for (int i = 0; i < 4; ++i) {
            const int loc = (w - 5) * 256 + i * 64 + l;
            const int X = bid * 360 + loc, mat = X / 46080, r = X - mat * 46080, sg = r / 640, n = r - sg * 640, jt = n / 80, lc = n - jt * 80;
            const int ss = sg >> 2, okh = ss < 16 ? (ss >> 3) : (ss - 16), oi = ss < 16 ? (ss & 7) : 8;
            if (loc < 360) gst16c(ws + (mat ? OFF_WOB : OFF_WOA) + (size_t)jt * WO_TILE + (size_t)((oi * 2 + okh) * 5 + (lc >> 4)) * 1024 + ((sg & 3) * 16 + (lc & 15)) * 16,
                                  lc < 72 ? cc[i] : (u32x4){0u, 0u, 0u, 0u});
        }
    } else {
        const int T = bid * 64 + l, m = T & 15, j = (T >> 4) & 7, rb = T >> 7;
        float sa = 0.f, sb = 0.f;
#pragma unroll
        for (int i = 0; i < 9; ++i) {
            const int q = 9 * j + i;
            gst16c(ws + OFF_XF + ((size_t)(rb * 18 + (q >> 2)) * 64 + (q & 3) * 16 + m) * 16, xv[i]);
            const float a = dot_ones(xv[i][3], dot_ones(xv[i][2], dot_ones(xv[i][1], dot_ones(xv[i][0], 0.f))));
            if (i < 4 || i == 8) sa += a; else sb += a;
        }
        const float ma = sa * (1.f / 40.f), mb = sb * (1.f / 32.f);
        float qa = 0.f, qb = 0.f;
#pragma unroll
        for (int i = 0; i < 9; ++i) {
            const bool pa = i < 4 || i == 8; const float mu = pa ? ma : mb; float a = 0.f;
#pragma unroll
            for (int k = 0; k < 4; ++k) { const float d0 = bf_lo(xv[i][k]) - mu, d1 = bf_hi(xv[i][k]) - mu; a += d0 * d0 + d1 * d1; }
            if (pa) qa += a; else qb += a;
        }
        gst16c(ws + OFF_ST + (size_t)(rb * 16 + m) * 128 + j * 16, __builtin_bit_cast(u32x4, (f32x4){sa, qa, sb, qb}));
    }
    if (w == 6) {
        if (l < 36) {
            const int Z = bid * 36 + l, zm = Z / 4608, z2 = Z - zm * 4608, zct = z2 / 288, r = z2 - zct * 288;
            gst16c(ws + (zm ? OFF_WTB : OFF_WTA) + ((size_t)((zct * 18 + (r >> 4)) * 7 + 6) * 64 + ((r >> 2) & 3) * 16 + 12 + (r & 3)) * 16, (u32x4){0u, 0u, 0u, 0u});
        } else if (l < 49) {
            const int Z = bid * 13 + (l - 36);
            if ((size_t)Z * 16 < ZERO_BYTES) gst16c(ws + (size_t)Z * 16, (u32x4){0u, 0u, 0u, 0u});
        } else if (l == 49 && bid < 32) gst16c(ws + ((bid & 16) ? OFF_CSB : OFF_CSA) + ((bid & 15) * 112 + 108) * 4, (u32x4){0u, 0u, 0u, 0u});
    }
    asm volatile("s_waitcnt lgkmcnt(0)\n\ts_barrier" ::: "memory");
    if (jobW && w == 0) {
        float a = 0.f;
#pragma unroll
        for (int i = 0; i < 18; ++i) a += red[((l >> 4) * 18 + i) * 16 + (l & 15)];
        a = rowsum4(a);
        if (l < 16) __hip_atomic_store((gu32*)(ws + (cmat ? OFF_CSB : OFF_CSA)) + tct * 112 + tcb * 16 + tc, __float_as_uint(a), RLX_AGENT);
    }
}

template <int MASK>
__global__ __launch_bounds__(512, 2) void mega_kernel(char* ws, const unsigned short* x0, unsigned short* out, int h_lo, int h_hi) {
    __shared__ __attribute__((aligned(16))) char smem[160 * 1024];
    const int lane = threadIdx.x & 63, w = __builtin_amdgcn_readfirstlane(threadIdx.x >> 6);
    constexpr bool FUSED = MASK == 7;
    if (FUSED && w == 7 && (blockIdx.x >> 3) < 4) {
        unsigned long long pc; asm volatile("s_getpc_b64 %0" : "=s"(pc));
        const char* cp = (const char*)(pc & ~127ull) + (blockIdx.x >> 3) * 5120 + min(lane, 39) * 128;
        unsigned t; asm volatile("global_load_dword %0, %1, off\n\ts_waitcnt vmcnt(0)" : "=v"(t) : "v"(cp) : "memory");
    }
#pragma unroll 1
    for (int hl = h_lo; hl < h_hi; ++hl) {
        const int L = hl >> 1;
        if (hl & 1) {
            if (MASK & 1) qkv_phase<1, FUSED>(ws, smem, L, w, lane);
            if (MASK & 2) attn_phase<FUSED>(ws, smem, L, w, lane);
        } else {
            if (MASK & 1) qkv_phase<0, FUSED>(ws, smem, L, w, lane);
        }
        if (MASK & 4) wo_phase<FUSED>(ws, smem, x0, out, L, hl & 1, w, lane);
    }
}

extern "C" void kernel_launch(void* const* d_in, const int* in_sizes, int n_in, void* d_out, int out_size, void* d_ws, size_t ws_size,
                              hipStream_t stream) {
    const unsigned short* x0 = (const unsigned short*)d_in[0];
    const unsigned short* wA = (const unsigned short*)d_in[1];
    const unsigned short* woA = (const unsigned short*)d_in[2];
    const unsigned short* wB = (const unsigned short*)d_in[4];
    const unsigned short* woB = (const unsigned short*)d_in[5];
    char* ws = (char*)d_ws;
    unsigned short* out = (unsigned short*)d_out;
    prep_kernel<<<256, 512, 0, stream>>>(x0, wA, woA, wB, woB, ws);
    mega_kernel<7><<<256, 512, 0, stream>>>(ws, x0, out, 0, 4);
}
```
